```python
import torch

def _softmax(x, axis=-1):
    return torch.softmax(x, dim=axis)

def _gelu(x):
    return torch.nn.functional.gelu(x, approximate='tanh')

def _silu(x):
    return torch.nn.functional.silu(x)

def _sigmoid(x):
    return torch.sigmoid(x)

def _layernorm(x, axis=-1, eps=1e-05):
    m = x.mean(dim=axis, keepdim=True)
    v = x.var(dim=axis, keepdim=True, unbiased=False)
    return (x - m) / torch.sqrt(v + eps)

def _scatter_add(tgt, idx, src, axis=0):
    return tgt.clone().index_add_(axis, idx, src)

def _segment_sum(src, seg, n):
    out = torch.zeros((n,) + src.shape[1:], dtype=src.dtype)
    return out.index_add_(0, seg, src)

def _pad(x, pw):
    flat = tuple((v for pair in reversed(pw) for v in pair))
    return torch.nn.functional.pad(x, flat)
kernel_name = 'adaLN_cond__linear_kernel_hedgehog__dense_activation_squared_relu__prompt'

def setup_inputs():

    _w = lambda a: (lambda t: t.to(torch.bfloat16) if t.is_floating_point() else t)(torch.as_tensor(a))

    def _A_setup():
        B, S, D, H = (1, 2048, 1024, 32)
        Dh = D // H
        return {'x': _w((lambda *s: torch.randn(*s, dtype=torch.bfloat16) * 0.05)(B, S, D)), 'wqkv': _w((lambda *s: torch.randn(*s, dtype=torch.bfloat16) * 0.4)(D, 3 * D)), 'w_phi': _w((lambda *s: torch.randn(*s, dtype=torch.bfloat16) * 0.4)(H, Dh, Dh) * Dh ** (-0.5)), 'wo': _w((lambda *s: torch.randn(*s, dtype=torch.bfloat16) * 0.05)(D, D))}
    _A_in = _A_setup()

    def _B_setup():
        B, S, D, F = (1, 2048, 1024, 3584)
        return {'x': _w((lambda *s: torch.randn(*s, dtype=torch.bfloat16) * 0.05)(B, S, D)), 'w1': _w((lambda *s: torch.randn(*s, dtype=torch.bfloat16) * 0.05)(D, F)), 'b1': _w((lambda *s: torch.randn(*s, dtype=torch.bfloat16) * 0.4)(F)), 'w2': _w((lambda *s: torch.randn(*s, dtype=torch.bfloat16) * 0.05)(F, D)), 'b2': _w((lambda *s: torch.randn(*s, dtype=torch.bfloat16) * 6.4)(D))}
    _B_in = _B_setup()
    return {'A_x': _A_in['x'], 'A_wqkv': _A_in['wqkv'], 'A_w_phi': _A_in['w_phi'], 'A_wo': _A_in['wo'], 'B_x': _B_in['x'], 'B_w1': _B_in['w1'], 'B_b1': _B_in['b1'], 'B_w2': _B_in['w2'], 'B_b2': _B_in['b2'], 'c': _w((lambda *s: torch.randn(*s, dtype=torch.bfloat16) * 0.1)(1, 1024) * 8.0), 'w_mod': _w((lambda *s: torch.randn(*s, dtype=torch.bfloat16) * 0.1)(1024, 6 * 1024) * 1024 ** (-0.5) * 8.0)}

def reference(A_x, A_wqkv, A_w_phi, A_wo, B_x, B_w1, B_b1, B_w2, B_b2, c, w_mod):

    def _A_ref(x, wqkv, w_phi, wo):
        B, S, D = x.shape
        H = 32
        Dh = D // H
        qkv = (lambda x, *d: x.permute(*d))((x @ wqkv).reshape(B, S, 3, H, Dh), 2, 0, 3, 1, 4)
        q, k, v = (qkv[0], qkv[1], qkv[2])
        pq = torch.einsum('bhsd,hdf->bhsf', q, w_phi)
        pk = torch.einsum('bhsd,hdf->bhsf', k, w_phi)
        phi_q = torch.concatenate([_softmax(pq, axis=-1), _softmax(-pq, axis=-1)], axis=-1)
        phi_k = torch.concatenate([_softmax(pk, axis=-1), _softmax(-pk, axis=-1)], axis=-1)
        kv = torch.einsum('bhsf,bhsd->bhfd', phi_k, v)
        z = torch.einsum('bhsf->bhf', phi_k)
        num = torch.einsum('bhqf,bhfd->bhqd', phi_q, kv)
        den = torch.einsum('bhqf,bhf->bhq', phi_q, z)[..., None] + 1e-06
        o = num / den
        return (lambda x, *d: x.permute(*d))(o, 0, 2, 1, 3).reshape(B, S, D) @ wo

    def _B_ref(x, w1, b1, w2, b2):
        h = torch.einsum('bsd,df->bsf', x, w1) + b1
        zero = torch.zeros_like(h)
        r = torch.where(h > zero, h, zero)
        h = r * r
        out = torch.einsum('bsf,fd->bsd', h, w2) + b2
        return out
    x = A_x
    mod = (_silu(c) @ w_mod).reshape(c.shape[0], 6, 1, -1)
    sa, ca, ga, sb, cb, gb = (mod[:, 0].clone(), mod[:, 1].clone(), mod[:, 2].clone(), mod[:, 3].clone(), mod[:, 4].clone(), mod[:, 5].clone())
    h = _layernorm(x, axis=-1) * (1 + ca) + sa
    x = x + ga * _A_ref(h, A_wqkv, A_w_phi, A_wo)
    h = _layernorm(x, axis=-1) * (1 + cb) + sb
    x = x + gb * _B_ref(h, B_w1, B_b1, B_w2, B_b2)
    return x
```

```cpp
#include <hip/hip_runtime.h>
#include <hip/hip_bf16.h>
typedef unsigned short u16;
__device__ __forceinline__ float bf2f(u16 v){ return __uint_as_float(((unsigned)v)<<16); }
__device__ __forceinline__ u16 f2bf(float f){ __bf16 b=(__bf16)f; return __builtin_bit_cast(u16,b); }
constexpr int S=2048,D=1024,F=3584,H=32,DH=32;

__global__ void k_mod(const u16* c,const u16* wm,float* mod){
  int col=blockIdx.x*blockDim.x+threadIdx.x; float a=0;
  for(int k=0;k<D;k++){ float cv=bf2f(c[k]); float s=bf2f(f2bf(cv/(1.f+__expf(-cv)))); a+=s*bf2f(wm[(size_t)k*6*D+col]); }
  mod[col]=bf2f(f2bf(a));
}
__global__ void k_ln(const u16* x,const float* shift,const float* scale,u16* h){
  __shared__ float red[256]; int r=blockIdx.x,t=threadIdx.x; float v[4],s=0;
  for(int i=0;i<4;i++){ v[i]=bf2f(x[(size_t)r*D+t*4+i]); s+=v[i]; }
  red[t]=s; __syncthreads(); for(int o=128;o>0;o>>=1){ if(t<o) red[t]+=red[t+o]; __syncthreads(); }
  float m=red[0]/D; __syncthreads(); s=0; for(int i=0;i<4;i++){ float d=v[i]-m; s+=d*d; }
  red[t]=s; __syncthreads(); for(int o=128;o>0;o>>=1){ if(t<o) red[t]+=red[t+o]; __syncthreads(); }
  float rs=rsqrtf(red[0]/D+1e-5f);
  for(int i=0;i<4;i++){ int c=t*4+i; h[(size_t)r*D+c]=f2bf((v[i]-m)*rs*(1.f+scale[c])+shift[c]); }
}
template<int MODE> __global__ void k_gemm(const u16* A,const u16* B,u16* C,int N,int K,const u16* bias,const u16* res,const float* gate){
  __shared__ float As[64][17],Bs[16][65]; int tx=threadIdx.x&15,ty=threadIdx.x>>4; int m0=blockIdx.y*64,n0=blockIdx.x*64; float acc[4][4]={};
  for(int k0=0;k0<K;k0+=16){
    for(int i=threadIdx.x;i<1024;i+=256){ As[i>>4][i&15]=bf2f(A[(size_t)(m0+(i>>4))*K+k0+(i&15)]); Bs[i>>6][i&63]=bf2f(B[(size_t)(k0+(i>>6))*N+n0+(i&63)]); }
    __syncthreads();
    for(int k=0;k<16;k++) for(int i=0;i<4;i++) for(int j=0;j<4;j++) acc[i][j]+=As[ty*4+i][k]*Bs[k][tx*4+j];
    __syncthreads();
  }
  for(int i=0;i<4;i++) for(int j=0;j<4;j++){ int r=m0+ty*4+i,c=n0+tx*4+j; float v=acc[i][j];
    if(MODE==1){ v=bf2f(res[(size_t)r*N+c])+gate[c]*v; }
    if(MODE==2){ v+=bf2f(bias[c]); v=v>0?v*v:0; }
    if(MODE==3){ v+=bf2f(bias[c]); v=bf2f(res[(size_t)r*N+c])+gate[c]*v; }
    C[(size_t)r*N+c]=f2bf(v); }
}
__global__ void k_phi(const u16* qkv,const u16* wphi,float* phi){
  int s=blockIdx.x, t=threadIdx.x;
  int which=t>>5,h=t&31; float p[32];
  for(int f=0;f<32;f++){ float a=0; for(int d=0;d<32;d++) a+=bf2f(qkv[(size_t)s*3*D+which*D+h*32+d])*bf2f(wphi[(h*32+d)*32+f]); p[f]=a; }
  float mx=-1e30f,mn=1e30f; for(int f=0;f<32;f++){ mx=fmaxf(mx,p[f]); mn=fminf(mn,p[f]); }
  float s1=0,s2=0; for(int f=0;f<32;f++){ s1+=__expf(p[f]-mx); s2+=__expf(mn-p[f]); }
  float* o=phi+(((size_t)which*S+s)*H+h)*64;
  for(int f=0;f<32;f++){ o[f]=__expf(p[f]-mx)/s1; o[32+f]=__expf(mn-p[f])/s2; }
}
__global__ void k_kv(const u16* qkv,const float* phi,float* kv){
  int h=blockIdx.x,f=blockIdx.y,d=threadIdx.x; if(d>32) return; float a=0;
  for(int s=0;s<S;s++){ float p=phi[(((size_t)S+s)*H+h)*64+f]; a+= d<32? p*bf2f(qkv[(size_t)s*3*D+2*D+h*32+d]) : p; }
  kv[(h*64+f)*33+d]=a;
}
__global__ void k_o(const float* phi,const float* kv,u16* o){ int s=blockIdx.x,t=threadIdx.x,h=t>>5,d=t&31; float n=0,den=0;
  for(int f=0;f<64;f++){ float p=phi[((size_t)s*H+h)*64+f]; n+=p*kv[(h*64+f)*33+d]; den+=p*kv[(h*64+f)*33+32]; }
  o[(size_t)s*D+t]=f2bf(n/(den+1e-6f)); }

extern "C" void kernel_launch(void* const* d_in,const int* in_sizes,int n_in,void* d_out,int out_size,void* d_ws,size_t ws_size,hipStream_t stream){
  const u16 *x=(const u16*)d_in[0],*wqkv=(const u16*)d_in[1],*wphi=(const u16*)d_in[2],*wo=(const u16*)d_in[3],*w1=(const u16*)d_in[5],*b1=(const u16*)d_in[6],*w2=(const u16*)d_in[7],*b2=(const u16*)d_in[8],*c=(const u16*)d_in[9],*wm=(const u16*)d_in[10];
  char* ws=(char*)d_ws; float* mod=(float*)ws; u16* h=(u16*)(ws+(1<<20)); u16* qkv=(u16*)(ws+(8<<20)); float* phi=(float*)(ws+(32<<20)); float* kv=(float*)(ws+(80<<20));
  u16* o=(u16*)(ws+(84<<20)); u16* x1=(u16*)(ws+(92<<20)); u16* h1=(u16*)(ws+(100<<20));
  k_mod<<<6*D/64,64,0,stream>>>(c,wm,mod);
  k_ln<<<S,256,0,stream>>>(x,mod,mod+D,h);
  k_gemm<0><<<dim3(3*D/64,S/64),256,0,stream>>>(h,wqkv,qkv,3*D,D,nullptr,nullptr,nullptr);
  k_phi<<<S,64,0,stream>>>(qkv,wphi,phi);
  k_kv<<<dim3(H,64),64,0,stream>>>(qkv,phi,kv);
  k_o<<<S,1024,0,stream>>>(phi,kv,o);
  k_gemm<1><<<dim3(D/64,S/64),256,0,stream>>>(o,wo,x1,D,D,nullptr,x,mod+2*D);
  k_ln<<<S,256,0,stream>>>(x1,mod+3*D,mod+4*D,h);
  k_gemm<2><<<dim3(F/64,S/64),256,0,stream>>>(h,w1,h1,F,D,b1,nullptr,nullptr);
  k_gemm<3><<<dim3(D/64,S/64),256,0,stream>>>(h1,w2,(u16*)d_out,D,F,b2,x1,mod+5*D);
}
```

```cpp
#include <hip/hip_runtime.h>
#include <hip/hip_bf16.h>

typedef unsigned short u16;
typedef unsigned u32x4 __attribute__((ext_vector_type(4)));
typedef unsigned u32x2 __attribute__((ext_vector_type(2)));
typedef short s16x4 __attribute__((ext_vector_type(4)));
typedef __bf16 bf16x8 __attribute__((ext_vector_type(8)));
typedef __bf16 bf16x2 __attribute__((ext_vector_type(2)));
typedef float f32x2 __attribute__((ext_vector_type(2)));
typedef float f32x4 __attribute__((ext_vector_type(4)));
#define LAS __attribute__((address_space(3)))
#define DI __device__ __forceinline__
#define SB __builtin_amdgcn_sched_barrier(0)

constexpr int S_ = 2048, D_ = 1024, F_ = 3584, MODN = 6 * D_;
constexpr size_t WS_FLAGS = 0;
constexpr size_t WS_MODP  = 1 << 16;
constexpr size_t WS_STAT1 = WS_MODP + (1 << 17);
constexpr size_t WS_STAT2 = WS_STAT1 + (1 << 14);
constexpr size_t WS_ZP    = WS_STAT2 + (1 << 18);
constexpr size_t WS_KVP   = 1 << 20;
constexpr size_t WS_O     = 4 << 20;
constexpr size_t WS_X1    = 8 << 20;
constexpr size_t WS_H1    = 12 << 20;
constexpr size_t WS_STAMP = 32 << 20;

#define STAMP(k, i) do { } while (0)

DI void glds16(const void* sbase, unsigned voff, unsigned ldsaddr) {
  asm volatile("s_mov_b32 m0, %2\n\ts_nop 0\n\tglobal_load_lds_dwordx4 %0, %1" :: "v"(voff), "s"(sbase), "s"(ldsaddr) : "memory");
}
DI void glds16x4(const void* sbase, unsigned voff, unsigned ldsaddr) {
  asm volatile("s_mov_b32 m0, %2\n\ts_nop 0\n\tglobal_load_lds_dwordx4 %0, %1\n\tglobal_load_lds_dwordx4 %0, %1 offset:1024\n\t"
               "global_load_lds_dwordx4 %0, %1 offset:2048\n\tglobal_load_lds_dwordx4 %0, %1 offset:3072" :: "v"(voff), "s"(sbase), "s"(ldsaddr) : "memory");
}
DI void gload16(u32x4& r, const void* sbase, unsigned voff) {
  asm volatile("global_load_dwordx4 %0, %1, %2" : "+v"(r) : "v"(voff), "s"(sbase) : "memory");
}
template <int N> DI void waitvm() { asm volatile("s_waitcnt vmcnt(%0)" :: "i"(N) : "memory"); }
template <int N> DI void waitvm_r(u32x4& r) { asm volatile("s_waitcnt vmcnt(%1)" : "+v"(r) : "i"(N) : "memory"); }
DI void waitlgkm0() { __builtin_amdgcn_s_waitcnt(0xC07F); }
DI void bar() { asm volatile("s_barrier" ::: "memory"); }
DI void lbar() { asm volatile("s_waitcnt lgkmcnt(0)\n\ts_barrier" ::: "memory"); }
DI unsigned cvtpk(float a, float b) { f32x2 v = {a, b}; return __builtin_bit_cast(unsigned, __builtin_convertvector(v, bf16x2)); }
DI float rbf(float a) { return __uint_as_float(cvtpk(a, a) << 16); }
DI float blo(unsigned u) { return __uint_as_float(u << 16); }
DI float bhi(unsigned u) { return __uint_as_float(u & 0xffff0000u); }
DI bf16x8 pack8(const f32x4& a, const f32x4& b) { u32x4 o = {cvtpk(a[0], a[1]), cvtpk(a[2], a[3]), cvtpk(b[0], b[1]), cvtpk(b[2], b[3])}; return __builtin_bit_cast(bf16x8, o); }
DI f32x4 mfma(bf16x8 a, bf16x8 b, f32x4 c) { return __builtin_amdgcn_mfma_f32_16x16x32_bf16(a, b, c, 0, 0, 0); }
DI s16x4 trd(unsigned a) { return __builtin_amdgcn_ds_read_tr16_b64_v4i16((LAS s16x4*)(size_t)a); }
DI bf16x8 cat(s16x4 lo, s16x4 hi) { return __builtin_bit_cast(bf16x8, __builtin_shufflevector(lo, hi, 0, 1, 2, 3, 4, 5, 6, 7)); }
template <class T> DI T ldsr(unsigned a) { return *(const LAS T*)(size_t)a; }
template <class T> DI void ldsw(unsigned a, T v) { *(LAS T*)(size_t)a = v; }
template <int C> DI float dpp(float x) { return __builtin_bit_cast(float, __builtin_amdgcn_update_dpp(0, __builtin_bit_cast(int, x), C, 0xf, 0xf, true)); }
DI float vmax(float a, float b) { float r; asm("v_max_f32_e32 %0, %1, %2" : "=v"(r) : "v"(a), "v"(b)); return r; }
DI float sw32_add(float a, float b) { auto r = __builtin_amdgcn_permlane32_swap(__float_as_uint(a), __float_as_uint(b), false, false); return __uint_as_float(r[0]) + __uint_as_float(r[1]); }
DI float sw16_add(float a, float b) { auto r = __builtin_amdgcn_permlane16_swap(__float_as_uint(a), __float_as_uint(b), false, false); return __uint_as_float(r[0]) + __uint_as_float(r[1]); }
DI float xg_sum(float a) { a = sw32_add(a, a); return sw16_add(a, a); }
DI float xg_max(float a) {
  auto r = __builtin_amdgcn_permlane32_swap(__float_as_uint(a), __float_as_uint(a), false, false); a = vmax(__uint_as_float(r[0]), __uint_as_float(r[1]));
  auto q = __builtin_amdgcn_permlane16_swap(__float_as_uint(a), __float_as_uint(a), false, false); return vmax(__uint_as_float(q[0]), __uint_as_float(q[1]));
}
DI float row16_sum(float a) { a += dpp<0x128>(a); a += dpp<0x124>(a); a += dpp<0x122>(a); a += dpp<0x121>(a); return a; }
DI float row8_sum(float a) { a += dpp<0xB1>(a); a += dpp<0x4E>(a); a += dpp<0x141>(a); return a; }
DI float wave_sum(float a) { return xg_sum(row16_sum(a)); }
DI f32x4 modsum(const float* modp, int col) {
  f32x4 a = *(const f32x4*)(modp + col), b = *(const f32x4*)(modp + MODN + col), c = *(const f32x4*)(modp + 2 * MODN + col), d = *(const f32x4*)(modp + 3 * MODN + col);
  f32x4 s = (a + b) + (c + d); return f32x4{rbf(s[0]), rbf(s[1]), rbf(s[2]), rbf(s[3])};
}

DI void gemv_load(u32x4 (&w)[4], float (&cs)[4], const u16* wm, const u16* c, int cg, int kq, int wv, int lane) {
#pragma unroll
  for (int i = 0; i < 4; i++) { const int k = kq * 256 + wv * 32 + i * 8 + (lane >> 3);
    w[i] = *(const u32x4*)(wm + (size_t)k * MODN + cg * 64 + (lane & 7) * 8); cs[i] = blo(c[k]); }
}
DI float gemv_red(const u32x4 (&w)[4], const float (&cs)[4], int lane) {
  float a[8] = {0, 0, 0, 0, 0, 0, 0, 0};
#pragma unroll
  for (int i = 0; i < 4; i++) { const float x = cs[i], s = rbf(x * __builtin_amdgcn_rcpf(1.f + __expf(-x)));
#pragma unroll
    for (int j = 0; j < 4; j++) { a[2 * j] += s * blo(w[i][j]); a[2 * j + 1] += s * bhi(w[i][j]); } }
  const float b0 = sw32_add(a[0], a[4]), b1 = sw32_add(a[1], a[5]), b2 = sw32_add(a[2], a[6]), b3 = sw32_add(a[3], a[7]);
  const float c0 = sw16_add(b0, b2), c1 = sw16_add(b1, b3);
  const bool up = lane & 8; return (up ? c1 : c0) + dpp<0x128>(up ? c0 : c1);
}
__global__ __launch_bounds__(512) void k0_mod(const u16* __restrict__ x, const u16* __restrict__ c, const u16* __restrict__ wm, char* __restrict__ ws) {
  __shared__ float red[2][8][64];
  const int tid = threadIdx.x, lane = tid & 63, wv = tid >> 6, b = blockIdx.x;
  STAMP(0, wv);
  const int role = (b >> 3) & 1, u = (b >> 4) * 8 + (b & 7);
  float* modp = (float*)(ws + WS_MODP);
  u32x4 w0[4], w1[4]; float c0[4], c1[4];
  if (role == 0) gemv_load(w0, c0, wm, c, u >> 2, u & 3, wv, lane);
  else {
#pragma unroll
    for (int i = 0; i < 4; i++) w0[i] = *(const u32x4*)(x + (size_t)(u * 16 + wv * 2 + (i >> 1)) * D_ + (i & 1) * 512 + lane * 8);
  }
  gemv_load(w1, c1, wm, c, 32 + (b >> 2), b & 3, wv, lane);
  if (b == 0 && tid < 64) *(unsigned*)(ws + WS_FLAGS + tid * 256) = 0u;
  const int col = (lane & 7) * 8 + 4 * (lane >> 5) + 2 * ((lane >> 4) & 1) + ((lane >> 3) & 1);
  if (role == 0) red[0][wv][col] = gemv_red(w0, c0, lane);
  else {
#pragma unroll
    for (int r = 0; r < 2; r++) { float v[16], s = 0;
#pragma unroll
      for (int j = 0; j < 8; j++) { const unsigned t = w0[2 * r + (j >> 2)][j & 3]; v[2 * j] = blo(t); v[2 * j + 1] = bhi(t); s += v[2 * j] + v[2 * j + 1]; }
      const float mean = wave_sum(s) * (1.f / D_); float q = 0;
#pragma unroll
      for (int j = 0; j < 16; j++) { const float d = v[j] - mean; q += d * d; }
      const float rstd = __builtin_amdgcn_rsqf(wave_sum(q) * (1.f / D_) + 1e-5f);
      if (lane == 0) ((f32x2*)(ws + WS_STAT1))[u * 16 + wv * 2 + r] = f32x2{mean, rstd};
    }
  }
  red[1][wv][col] = gemv_red(w1, c1, lane);
  __syncthreads();
  if (wv < 2 && (wv == 1 || role == 0)) { float s = 0;
#pragma unroll
    for (int i = 0; i < 8; i++) s += red[wv][i][lane];
    if (wv == 0) modp[(u & 3) * MODN + (u >> 2) * 64 + lane] = s; else modp[(b & 3) * MODN + (32 + (b >> 2)) * 64 + lane] = s;
  }
  STAMP(0, 8 + wv);
}

template <int CFG> struct Cfg;
template <> struct Cfg<0> { static constexpr int NT = 6, NP = 3, HALF = 0, NB = 2, T = 1, NS = 4, NA = 2; };
template <> struct Cfg<1> { static constexpr int NT = 7, NP = 3, HALF = 1, NB = 7, T = 1, NS = 4, NA = 2; };
template <> struct Cfg<2> { static constexpr int NT = 2, NP = 1, HALF = 0, NB = 2, T = 0, NS = 4, NA = 4; };
template <int CFG> struct Lay { using C = Cfg<CFG>; static constexpr int SBB = C::NP * 8192 + C::HALF * 4096, B0 = C::NA * 16384, END = B0 + C::NS * SBB; };

template <int CFG, int KT> DI void compute_loop(f32x4 (&acc)[4][Cfg<CFG>::NT], const f32x4 (&init)[Cfg<CFG>::NT], unsigned lds0, int wm, int wn, int lane) {
  using C = Cfg<CFG>; using L = Lay<CFG>; constexpr int MT = 4, NT = C::NT, NB = C::NB, NAB = C::T ? 2 : 1;
  const int g = lane >> 4, q = (lane & 15) >> 2, p = lane & 3, r = lane & 15;
  unsigned aoff[NAB], boff[NB];
  if (C::T) { aoff[0] = lds0 + wm * 8192 + g * 256 + ((r ^ g) * 16); aoff[NAB - 1] = (aoff[0] ^ 64) + 1024; } else aoff[0] = lds0 + wm * 8192 + lane * 16;
  const int row = C::T ? 8 * g + q : 4 * g + q; const unsigned sw = C::T ? ((q >> 1) | ((g & 1) << 1)) : ((row >> 1) & 3);
  constexpr int TS = C::T ? 512 : 2048;
#pragma unroll
  for (int i = 0; i < NB; i++) {
    if (CFG != 1) boff[i] = lds0 + L::B0 + row * 128 + (((2 * wn + i) ^ sw) * 32) + p * 8;
    else if (i < 6) { const int tl = wn * 6 + i; boff[i] = lds0 + L::B0 + (tl >> 2) * 8192 + row * 128 + (((tl & 3) ^ sw) * 32) + p * 8; }
    else boff[i] = lds0 + L::B0 + C::NP * 8192 + row * 64 + ((wn ^ (g & 1)) * 32) + p * 8;
  }
  bf16x8 fa[2][MT]; s16x4 fb[2][NT][2]; unsigned va[NAB], vb[NB];
  constexpr int NR = MT + 2 * NT, NM = MT * NT, NU = NAB + NB;
  auto upd = [&](int i, unsigned sa, unsigned sb) { if (i < NAB) { va[i] = aoff[i] + sa; asm volatile("" : "+v"(va[i])); } else { vb[i - NAB] = boff[i - NAB] + sb; asm volatile("" : "+v"(vb[i - NAB])); } };
  auto rd1 = [&](int buf, int ks, int i) {
    if (i == 0 || i > 2 * NT) { const int m = i ? i - 2 * NT : 0; fa[buf][m] = ldsr<bf16x8>(C::T ? va[ks & (NAB - 1)] + m * 2048 : va[0] + m * 2048 + ks * 1024); }
    else { const int n = (i - 1) >> 1, t = (i - 1) & 1;
      fb[buf][n][t] = trd(CFG == 0 ? vb[n & 1] + (n >> 1) * 8192 + ks * 4096 + t * TS : (CFG == 1 && n == 6) ? vb[NB - 1] + ks * 2048 + t * 256 : vb[n] + ks * 4096 + t * TS); }
  };
  auto step = [&](int buf, int ks, bool doread, bool first, bool doupd, unsigned sa, unsigned sb) {
#pragma unroll
    for (int i = 0; i < NM; i++) {
      const int m = i / NT, n = i % NT; const bf16x8 b = cat(fb[buf][n][0], fb[buf][n][1]);
      const f32x4 cin = first ? init[n] : acc[m][n];
      acc[m][n] = (CFG == 0 && n >= 4) ? mfma(fa[buf][m], b, cin) : mfma(b, fa[buf][m], cin);
      SB;
      if (doread && i < NR) { rd1(buf ^ 1, ks, i); SB; }
      if (doupd && NM - NR >= NU && i >= NR && i < NR + NU) { upd(i - NR, sa, sb); SB; }
    }
    if (doupd && NM - NR < NU) {
#pragma unroll
      for (int i = 0; i < NU; i++) upd(i, sa, sb);
      SB;
    }
  };
#pragma unroll
  for (int i = 0; i < NU; i++) upd(i, 0, 0);
  SB; bar();
#pragma unroll
  for (int i = 0; i < NR; i++) rd1(0, 0, i);
  SB;
  unsigned sa = 16384, sb = L::SBB;
  step(0, 1, true, true, true, sa, sb);
  for (int t = 0; t < KT - 1; t++) {
    waitlgkm0(); bar(); SB;
    sa += 16384; if (sa == C::NA * 16384) sa = 0;
    sb += L::SBB; if (sb == C::NS * L::SBB) sb = 0;
    step(1, 0, true, false, false, 0, 0);
    if (NM - NR >= 4) { waitlgkm0(); SB; }
    step(0, 1, true, false, true, sa, sb);
  }
  step(1, 0, false, false, false, 0, 0);
}

template <int CFG, int N> struct BLoad {
  using C = Cfg<CFG>; using L = Lay<CFG>; static constexpr int NPC = 2 * C::NP + C::HALF;
  const char* bb; const char* hb; unsigned bv0, bv1, hv, l0;
  DI void init(const u16* W, int tn, int lw, int lane, unsigned lds0) {
    const int col0 = CFG == 1 ? tn * 224 + ((tn & 1) ? 32 : 0) : tn * 64, r = lane >> 3;
    bb = (const char*)W + ((size_t)(lw * 16) * N + col0) * 2;
    if (C::T) { bv0 = r * N * 2 + (((lane & 7) ^ (((r >> 1) & 1) << 1)) * 16); bv1 = bv0 ^ 64; } else { bv0 = bv1 = r * N * 2 + (((lane & 7) ^ (((r >> 1) & 3) << 1)) * 16); }
    const int hr = lane >> 2; hv = hr * N * 2 + (((lane & 3) ^ (((hr >> 3) & 1) << 1)) * 16);
    hb = (const char*)W + ((size_t)(lw * 16) * N + ((tn & 1) ? tn * 224 : tn * 224 + 192)) * 2;
    l0 = lds0 + L::B0 + lw * 2048 ; lwk = lw * 1024;
  }
  unsigned lwk;
  DI void piece(int i, int t, unsigned slotoff) const {
    if (i < 2 * C::NP) glds16(bb + (size_t)t * 64 * N * 2 + (i >> 1) * (CFG == 0 ? 2048 : 128) + (i & 1) * (8 * N * 2), (i & 1) ? bv1 : bv0, l0 + slotoff + (i >> 1) * 8192 + (i & 1) * 1024);
    else glds16(hb + (size_t)t * 64 * N * 2, hv, l0 - lwk + slotoff + C::NP * 8192);
  }
};

template <int CFG, int N, int KT> DI void load_loop_g(const u16* A, const u16* W, int tm, int tn, int lw, int lane, unsigned lds0) {
  using C = Cfg<CFG>; using L = Lay<CFG>; constexpr int NS = C::NS; BLoad<CFG, N> B; B.init(W, tn, lw, lane, lds0);
  constexpr int PER = 4 + BLoad<CFG, N>::NPC;
  const char* ab = (const char*)A + (size_t)tm * KT * 16384 + lw * 4096; const unsigned av = lane * 16;
  auto issue = [&](int t, int slot) { glds16x4(ab + (size_t)t * 16384, av, lds0 + slot * 16384 + lw * 4096);
#pragma unroll
    for (int i = 0; i < PER - 4; i++) B.piece(i, t, slot * L::SBB); };
  static_assert(NS == 4, "");
  issue(0, 0); issue(1, 1); waitvm<PER>(); bar();
  issue(2, 2); waitvm<PER>(); bar();
  issue(3, 3); int slot = 0;
  for (int t = 1; t < KT - 3; t++) { issue(t + 3, slot); slot = (slot + 1) & 3; waitvm<2 * PER>(); bar(); }
  waitvm<PER>(); bar(); waitvm<0>(); bar();
}

template <int CFG, int N> struct TLoad {
  using C = Cfg<CFG>; using L = Lay<CFG>; static constexpr int NPC = BLoad<CFG, N>::NPC, PER = 4 + NPC, KT = 16;
  BLoad<CFG, N> B; const char* xb; unsigned xv, aw0, tb; u32x4 ar[2][4];
  DI void init(const u16* X, const u16* W, int tm, int tn, int lw, int lane, unsigned lds0, unsigned tab) {
    B.init(W, tn, lw, lane, lds0); const int r = lane >> 3, c = lane & 7;
    xb = (const char*)X + (size_t)(tm * 128 + lw * 32) * 2048; xv = r * 2048 + c * 16;
    aw0 = lds0 + lw * 4096 + (c >> 2) * 1024 + (c & 3) * 256 + ((r ^ c) * 16); tb = tab + c * 32;
#pragma unroll
    for (int s = 0; s < 2; s++)
#pragma unroll
      for (int i = 0; i < 4; i++) ar[s][i] = u32x4{0, 0, 0, 0};
  }
  DI void sub_issue(int ring, int i, int t, unsigned slotoff) { gload16(ar[ring][i], xb + t * 128 + i * 16384, xv);
#pragma unroll
    for (int k = 2 * i; k < 2 * i + 2; k++) if (k < NPC) B.piece(k, t, slotoff); }
  DI void first_issue() {
#pragma unroll
    for (int s = 0; s < 2; s++)
#pragma unroll
      for (int i = 0; i < 4; i++) sub_issue(s, i, s, s * L::SBB);
  }
  template <int MODE> DI void iter(int ring, int u, const float (&rs)[4], const float (&nm)[4]) {
    const f32x4 s0 = ldsr<f32x4>(tb + u * 256), s1 = ldsr<f32x4>(tb + u * 256 + 16), h0 = ldsr<f32x4>(tb + 4096 + u * 256), h1 = ldsr<f32x4>(tb + 4096 + u * 256 + 16);
#pragma unroll
    for (int i = 0; i < 4; i++) {
      if (MODE == 0) waitvm_r<2 * PER - 1>(ar[ring][i]); else if (MODE == 1) waitvm_r<PER>(ar[ring][i]); else waitvm_r<0>(ar[ring][i]);
      const f32x2 r2 = {rs[i], rs[i]}, n2 = {nm[i], nm[i]}; u32x4 o;
#pragma unroll
      for (int j = 0; j < 4; j++) { const unsigned w = ar[ring][i][j]; f32x2 v = {blo(w), bhi(w)};
        const f32x2 sc = j < 2 ? f32x2{s0[2 * j], s0[2 * j + 1]} : f32x2{s1[2 * j - 4], s1[2 * j - 3]}, sh = j < 2 ? f32x2{h0[2 * j], h0[2 * j + 1]} : f32x2{h1[2 * j - 4], h1[2 * j - 3]};
        v = __builtin_elementwise_fma(v, r2, n2); v = __builtin_elementwise_fma(v, sc, sh); o[j] = cvtpk(v[0], v[1]); }
      ldsw<u32x4>(aw0 + ring * 16384 + (i >> 1) * 2048 + (i & 1) * 128, o);
      if (MODE == 0) sub_issue(ring, i, u + 2, ((u + 2) & 3) * L::SBB);
    }
    if (MODE == 0) waitvm<2 * PER>();
    lbar();
  }
  DI void run(const float (&rs)[4], const float (&nm)[4]) {
    for (int u = 0; u < KT - 2; u += 2) { iter<0>(0, u, rs, nm); iter<0>(1, u + 1, rs, nm); }
    iter<1>(0, KT - 2, rs, nm); iter<2>(1, KT - 1, rs, nm);
  }
};
DI void build_tables(const float* modp, int shift_col, int scale_col, unsigned tab, int tid) {
  const f32x4 sh = modsum(modp, shift_col + tid * 4), sc = modsum(modp, scale_col + tid * 4);
  ldsw<f32x4>(tab + tid * 16, f32x4{rbf(1.f + sc[0]), rbf(1.f + sc[1]), rbf(1.f + sc[2]), rbf(1.f + sc[3])});
  ldsw<f32x4>(tab + 4096 + tid * 16, sh);
}

DI void feat(f32x4 p0, f32x4 p1, bf16x8& fp, bf16x8& fn) {
  constexpr float L2E = 1.4426950408889634f; p0 *= L2E; p1 *= L2E;
  const float mx = xg_max(fmaxf(fmaxf(fmaxf(p0[0], p0[1]), fmaxf(p0[2], p0[3])), fmaxf(fmaxf(p1[0], p1[1]), fmaxf(p1[2], p1[3]))));
  const float nx = xg_max(-fminf(fminf(fminf(p0[0], p0[1]), fminf(p0[2], p0[3])), fminf(fminf(p1[0], p1[1]), fminf(p1[2], p1[3]))));
  f32x4 a0, a1, b0, b1;
#pragma unroll
  for (int j = 0; j < 4; j++) { a0[j] = __builtin_amdgcn_exp2f(p0[j] - mx); a1[j] = __builtin_amdgcn_exp2f(p1[j] - mx); b0[j] = __builtin_amdgcn_exp2f(-p0[j] - nx); b1[j] = __builtin_amdgcn_exp2f(-p1[j] - nx); }
  const f32x4 sa = a0 + a1, sb = b0 + b1;
  const float ra = __builtin_amdgcn_rcpf(xg_sum((sa[0] + sa[1]) + (sa[2] + sa[3]))), rb = __builtin_amdgcn_rcpf(xg_sum((sb[0] + sb[1]) + (sb[2] + sb[3])));
  fp = pack8(a0 * ra, a1 * ra); fn = pack8(b0 * rb, b1 * rb);
}

__global__ __launch_bounds__(512, 2) void k1_qkv(const u16* __restrict__ x, const u16* __restrict__ wqkv, const u16* __restrict__ wphi, char* __restrict__ ws) {
  using L = Lay<0>; constexpr int TAB = L::END, WPHI = TAB + 8192, TOT = WPHI + 4096;
  constexpr int TR = 0, PART = 32768, FIN = PART + 49152;
  __shared__ __attribute__((aligned(1024))) char smem[TOT];
  const int tid = threadIdx.x, lane = tid & 63, w = __builtin_amdgcn_readfirstlane(tid >> 6);
  const unsigned lds0 = (unsigned)(size_t)(LAS char*)smem;
  const int xcd = blockIdx.x & 7, j = blockIdx.x >> 3, tm = (xcd >> 2) * 8 + (j >> 2), tn = (xcd & 3) * 4 + (j & 3);
  const int g = lane >> 4;
  STAMP(1, w);
  if (w >= 4) {
    const int lw = w - 4; TLoad<0, 3 * D_> T; T.init(x, wqkv, tm, tn, lw, lane, lds0, lds0 + TAB);
    glds16((const char*)wphi + tn * 4096 + lw * 1024, lane * 16, lds0 + WPHI + lw * 1024);
    float rs[4], nm[4];
#pragma unroll
    for (int i = 0; i < 4; i++) { const f32x2 st = ((const f32x2*)(ws + WS_STAT1))[tm * 128 + lw * 32 + i * 8 + (lane >> 3)]; rs[i] = st[1]; nm[i] = -st[0] * st[1]; }
    asm volatile("" : "+v"(rs[0]), "+v"(rs[1]), "+v"(rs[2]), "+v"(rs[3]), "+v"(nm[0]), "+v"(nm[1]), "+v"(nm[2]), "+v"(nm[3]));
    T.first_issue();
    bar();
    T.run(rs, nm);
    bar();
    bar();
    const int hd = lw >> 1, sg = lw & 1; f32x4 t[6];
#pragma unroll
    for (int i = 0; i < 6; i++) t[i] = ldsr<f32x4>(lds0 + PART + hd * 12288 + (sg * 6 + i) * 1024 + lane * 16) + ldsr<f32x4>(lds0 + PART + (2 + hd) * 12288 + (sg * 6 + i) * 1024 + lane * 16);
    const auto rk = __builtin_amdgcn_make_buffer_rsrc(ws + WS_KVP + (size_t)(tn * 4 + lw) * 32768, 0, 32768, 0x00020000);
    const auto rz = __builtin_amdgcn_make_buffer_rsrc(ws + WS_ZP + (size_t)(tn * 4 + lw) * 2048, 0, 2048, 0x00020000);
    __builtin_amdgcn_raw_buffer_store_b128(__builtin_bit_cast(u32x4, pack8(t[0], t[2])), rk, tm * 2048 + lane * 16, 0, 16);
    __builtin_amdgcn_raw_buffer_store_b128(__builtin_bit_cast(u32x4, pack8(t[1], t[3])), rk, tm * 2048 + 1024 + lane * 16, 0, 16);
    if ((lane & 14) == 0) __builtin_amdgcn_raw_buffer_store_b128(__builtin_bit_cast(u32x4, (lane & 1) ? t[5] : t[4]), rz, tm * 128 + (lane & 1) * 64 + g * 16, 0, 16);
    waitvm<0>();
    unsigned* cnt = (unsigned*)(ws + WS_FLAGS + (tn * 4 + lw) * 256);
    if (lane == 0) __hip_atomic_fetch_add(cnt, 1u, __ATOMIC_RELAXED, __HIP_MEMORY_SCOPE_AGENT);
    STAMP(1, 16 + lw);
    for (int spin = 0; spin < (1 << 16); spin++) {
      if ((unsigned)__builtin_amdgcn_readfirstlane(__hip_atomic_load(cnt, __ATOMIC_RELAXED, __HIP_MEMORY_SCOPE_AGENT)) >= 16u) break;
      __builtin_amdgcn_s_sleep(1);
    }
    __builtin_amdgcn_fence(__ATOMIC_ACQUIRE, "wavefront");
    STAMP(1, 20 + lw);
    f32x4 z0 = __builtin_bit_cast(f32x4, __builtin_amdgcn_raw_buffer_load_b128(rz, (lane & 15) * 128 + g * 16, 0, 16)), z1 = __builtin_bit_cast(f32x4, __builtin_amdgcn_raw_buffer_load_b128(rz, (lane & 15) * 128 + 64 + g * 16, 0, 16));
    u32x4 pk[32];
#pragma unroll
    for (int i = 0; i < 32; i++) pk[i] = __builtin_amdgcn_raw_buffer_load_b128(rk, lane * 16, i * 1024, 16);
#pragma unroll
    for (int k = 0; k < 4; k++) { z0[k] = row16_sum(z0[k]); z1[k] = row16_sum(z1[k]); }
    ldsw<bf16x8>(lds0 + FIN + lw * 3072 + 2048 + lane * 16, pack8(z0, z1));
    f32x2 sm[2][4];
#pragma unroll
    for (int i = 0; i < 32; i++)
#pragma unroll
      for (int k = 0; k < 4; k++) { const f32x2 v = {blo(pk[i][k]), bhi(pk[i][k])}; sm[i & 1][k] = i < 2 ? v : sm[i & 1][k] + v; }
#pragma unroll
    for (int c = 0; c < 2; c++) ldsw<u32x4>(lds0 + FIN + lw * 3072 + c * 1024 + lane * 16, u32x4{cvtpk(sm[c][0][0], sm[c][0][1]), cvtpk(sm[c][1][0], sm[c][1][1]), cvtpk(sm[c][2][0], sm[c][2][1]), cvtpk(sm[c][3][0], sm[c][3][1])});
    lbar();
  } else {
    const int wm = w >> 1, wn = w & 1;
    build_tables((const float*)(ws + WS_MODP), 0, D_, lds0 + TAB, tid);
    lbar();
    f32x4 acc[4][6]; const f32x4 zero = {0, 0, 0, 0}; const f32x4 init[6] = {zero, zero, zero, zero, zero, zero};
    compute_loop<0, 16>(acc, init, lds0, wm, wn, lane);
    STAMP(1, 8 + w);
    bar();
    const int q = (lane & 15) >> 2, p = lane & 3, r = lane & 15;
    bf16x8 wf[2];
#pragma unroll
    for (int c = 0; c < 2; c++) { const unsigned a = lds0 + WPHI + wn * 2048 + (4 * g + q) * 64 + c * 32 + p * 8; wf[c] = cat(trd(a), trd(a + 1024)); }
    const unsigned trw = lds0 + TR + w * 8192;
#pragma unroll
    for (int m = 0; m < 4; m++) { const bf16x8 kf = pack8(acc[m][2], acc[m][3]); bf16x8 fp, fn;
      feat(mfma(wf[0], kf, zero), mfma(wf[1], kf, zero), fp, fn);
      const unsigned a = trw + m * 1024 + r * 64 + ((g ^ ((r >> 1) & 3)) * 16); ldsw<bf16x8>(a, fp); ldsw<bf16x8>(a + 4096, fn); }
    asm volatile("s_waitcnt lgkmcnt(0)" ::: "memory");
    const u32x4 one4 = {0x3f803f80u, 0x3f803f80u, 0x3f803f80u, 0x3f803f80u}; const bf16x8 ones = __builtin_bit_cast(bf16x8, one4);
    const unsigned tra = trw + (4 * g + q) * 64 + ((p ^ (((4 * g + q) >> 1) & 3)) * 16);
    bf16x8 vf[2][2];
#pragma unroll
    for (int u = 0; u < 2; u++)
#pragma unroll
      for (int c = 0; c < 2; c++) vf[u][c] = pack8(acc[2 * u][4 + c], acc[2 * u + 1][4 + c]);
#pragma unroll
    for (int s = 0; s < 2; s++)
#pragma unroll
      for (int ft = 0; ft < 2; ft++) { const unsigned a = tra + s * 4096 + ft * 8;
        const bf16x8 a0 = cat(trd(a), trd(a + 1024)), a1 = cat(trd(a + 2048), trd(a + 3072));
        const unsigned o = lds0 + PART + w * 12288 + s * 6144 + lane * 16;
        ldsw<f32x4>(o + (ft * 2) * 1024, mfma(a1, vf[1][0], mfma(a0, vf[0][0], zero)));
        ldsw<f32x4>(o + (ft * 2 + 1) * 1024, mfma(a1, vf[1][1], mfma(a0, vf[0][1], zero)));
        ldsw<f32x4>(o + (4 + ft) * 1024, mfma(a1, ones, mfma(a0, ones, zero))); }
    lbar();
    STAMP(1, 12 + w);
    asm volatile("" : "+v"(acc[0][0]), "+v"(acc[0][1]), "+v"(acc[1][0]), "+v"(acc[1][1]), "+v"(acc[2][0]), "+v"(acc[2][1]), "+v"(acc[3][0]), "+v"(acc[3][1]));
    bf16x8 qp[4], qn[4];
#pragma unroll
    for (int m = 0; m < 4; m++) { const bf16x8 qf = pack8(acc[m][0], acc[m][1]); feat(mfma(wf[0], qf, zero), mfma(wf[1], qf, zero), qp[m], qn[m]); }
    asm volatile("" : "+v"(qp[0]), "+v"(qp[1]), "+v"(qp[2]), "+v"(qp[3]), "+v"(qn[0]), "+v"(qn[1]), "+v"(qn[2]), "+v"(qn[3]));
    STAMP(1, 24 + w);
    bar();
    bf16x8 kv[2][3];
#pragma unroll
    for (int s = 0; s < 2; s++)
#pragma unroll
      for (int c = 0; c < 3; c++) kv[s][c] = ldsr<bf16x8>(lds0 + FIN + (wn * 2 + s) * 3072 + c * 1024 + lane * 16);
    char* ob = ws + WS_O + (size_t)(tm * 16 + tn) * 16384 + (wm * 8 + wn) * 1024 + lane * 16;
#pragma unroll
    for (int m = 0; m < 4; m++) {
      const f32x4 n0 = mfma(kv[1][0], qn[m], mfma(kv[0][0], qp[m], zero)), n1 = mfma(kv[1][1], qn[m], mfma(kv[0][1], qp[m], zero)), dn = mfma(kv[1][2], qn[m], mfma(kv[0][2], qp[m], zero));
      const float rd = __builtin_amdgcn_rcpf(dn[0] + 1e-6f);
      *(bf16x8*)(ob + m * 2048) = pack8(n0 * rd, n1 * rd);
    }
    STAMP(1, 28 + w);
  }
}

template <int KT, bool FINAL> __global__ __launch_bounds__(512, 2) void k_res(const u16* __restrict__ A, const u16* __restrict__ W, const u16* __restrict__ res, const u16* __restrict__ bias, u16* __restrict__ out, char* __restrict__ ws) {
  using L = Lay<2>; constexpr int RES = L::END, STT = RES + 16384, TOT = STT + 2048, KID = FINAL ? 4 : 2;
  __shared__ __attribute__((aligned(1024))) char smem[TOT];
  const int tid = threadIdx.x, lane = tid & 63, w = __builtin_amdgcn_readfirstlane(tid >> 6);
  const unsigned lds0 = (unsigned)(size_t)(LAS char*)smem;
  const int xcd = blockIdx.x & 7, j = blockIdx.x >> 3, tm = (xcd >> 1) * 4 + (j >> 3), tn = (xcd & 1) * 8 + (j & 7);
  STAMP(KID, w);
  const int r8 = lane >> 3; const unsigned rv0 = r8 * 2048 + (((lane & 7) ^ (r8 >> 1)) * 16);
  const size_t tb = ((size_t)tm * 128 * D_ + tn * 64) * 2;
  if (w >= 4) {
    const int lw = w - 4;
    load_loop_g<2, D_, KT>(A, W, tm, tn, lw, lane, lds0);
#pragma unroll
    for (int i = 0; i < 4; i++) glds16((const char*)res + tb + (size_t)(lw * 4 + i) * 16384, (i & 1) ? rv0 ^ 64 : rv0, lds0 + RES + (lw * 4 + i) * 1024);
    waitvm<0>(); bar();
    bar();
  } else {
    const int wm = w >> 1, wn = w & 1, g = lane >> 4, r = lane & 15, col = tn * 64 + wn * 32 + 4 * g;
    f32x4 gate[2], init[2];
#pragma unroll
    for (int n = 0; n < 2; n++) { gate[n] = modsum((const float*)(ws + WS_MODP), (FINAL ? 5 : 2) * D_ + col + n * 16);
      if (FINAL) { const u32x2 bb = *(const u32x2*)(bias + col + n * 16); init[n] = f32x4{blo(bb[0]), bhi(bb[0]), blo(bb[1]), bhi(bb[1])}; } else init[n] = f32x4{0, 0, 0, 0}; }
    f32x4 acc[4][2];
    compute_loop<2, KT>(acc, init, lds0, wm, wn, lane);
    STAMP(KID, 8 + w);
    bar();
#pragma unroll
    for (int m = 0; m < 4; m++) { float v[8];
#pragma unroll
      for (int n = 0; n < 2; n++) { const unsigned a = lds0 + RES + (wm * 64 + m * 16 + r) * 128 + (((wn * 4 + n * 2 + (g >> 1)) ^ (r >> 1)) * 16) + (g & 1) * 8;
        const u32x2 x = ldsr<u32x2>(a); const f32x4 y = f32x4{blo(x[0]), bhi(x[0]), blo(x[1]), bhi(x[1])} + gate[n] * acc[m][n];
        const u32x2 o = {cvtpk(y[0], y[1]), cvtpk(y[2], y[3])}; ldsw<u32x2>(a, o);
        v[4 * n] = blo(o[0]); v[4 * n + 1] = bhi(o[0]); v[4 * n + 2] = blo(o[1]); v[4 * n + 3] = bhi(o[1]); }
      if (!FINAL) {
        const float mean = xg_sum(((v[0] + v[1]) + (v[2] + v[3])) + ((v[4] + v[5]) + (v[6] + v[7]))) * (1.f / 32); float qq = 0;
#pragma unroll
        for (int k = 0; k < 8; k++) { const float d = v[k] - mean; qq += d * d; }
        qq = xg_sum(qq);
        if (g == 0) ldsw<f32x2>(lds0 + STT + (wm * 64 + m * 16 + r) * 16 + wn * 8, f32x2{mean, qq});
      }
    }
    lbar();
  }
#pragma unroll
  for (int i = 0; i < 2; i++) *(u32x4*)((char*)out + tb + (size_t)(w * 2 + i) * 16384 + (i ? rv0 ^ 64 : rv0)) = ldsr<u32x4>(lds0 + RES + (w * 2 + i) * 1024 + lane * 16);
  if (!FINAL && tid < 128) { const f32x4 s = ldsr<f32x4>(lds0 + STT + tid * 16); const float d = s[0] - s[2];
    ((f32x2*)(ws + WS_STAT2))[(tm * 16 + tn) * 128 + tid] = f32x2{0.5f * (s[0] + s[2]), s[1] + s[3] + 16.f * d * d}; }
  STAMP(KID, 16 + w);
}

__global__ __launch_bounds__(512, 2) void k3_w1(const u16* __restrict__ w1, const u16* __restrict__ b1, char* __restrict__ ws) {
  using L = Lay<1>; constexpr int TAB = L::END, TOT = TAB + 8192;
  __shared__ __attribute__((aligned(1024))) char smem[TOT];
  const int tid = threadIdx.x, lane = tid & 63, w = __builtin_amdgcn_readfirstlane(tid >> 6);
  const unsigned lds0 = (unsigned)(size_t)(LAS char*)smem;
  const int xcd = blockIdx.x & 7, j = blockIdx.x >> 3, tm = (xcd >> 2) * 8 + (j >> 2), tn = (xcd & 3) * 4 + (j & 3);
  STAMP(3, w);
  if (w >= 4) {
    const int lw = w - 4; TLoad<1, F_> T; T.init((const u16*)(ws + WS_X1), w1, tm, tn, lw, lane, lds0, lds0 + TAB);
    float rs[4], nm[4]; const f32x2* sp = (const f32x2*)(ws + WS_STAT2) + (tm * 16 + (lane & 7) * 2) * 128 + lw * 32 + (lane >> 3);
#pragma unroll
    for (int i = 0; i < 4; i++) { const f32x2 a = sp[i * 8], b = sp[128 + i * 8];
      const float mean = row8_sum(a[0] + b[0]) * (1.f / 16), da = a[0] - mean, db = b[0] - mean;
      const float m2 = row8_sum(a[1] + b[1] + 64.f * (da * da + db * db));
      rs[i] = __builtin_amdgcn_rsqf(m2 * (1.f / D_) + 1e-5f); nm[i] = -mean * rs[i]; }
    asm volatile("" : "+v"(rs[0]), "+v"(rs[1]), "+v"(rs[2]), "+v"(rs[3]), "+v"(nm[0]), "+v"(nm[1]), "+v"(nm[2]), "+v"(nm[3]));
    T.first_issue();
    bar();
    T.run(rs, nm);
  } else {
    const int wm = w >> 1, wn = w & 1, g = lane >> 4;
    build_tables((const float*)(ws + WS_MODP), 3 * D_, 4 * D_, lds0 + TAB, tid);
    const int fp0 = tn * 7 + (tn & 1) + wn * 3, fph = (tn & 1) ? tn * 7 : tn * 7 + 6;
    f32x4 init[7];
#pragma unroll
    for (int n = 0; n < 7; n++) { const u32x2 bb = *(const u32x2*)(b1 + (n < 6 ? fp0 * 32 + n * 16 : fph * 32 + wn * 16) + 4 * g); init[n] = f32x4{blo(bb[0]), bhi(bb[0]), blo(bb[1]), bhi(bb[1])}; }
    lbar();
    f32x4 acc[4][7];
    compute_loop<1, 16>(acc, init, lds0, wm, wn, lane);
    STAMP(3, 8 + w);
    char* hb = ws + WS_H1 + (size_t)tm * 56 * 16384 + wm * 8192 + lane * 16;
#pragma unroll
    for (int m = 0; m < 4; m++) {
#pragma unroll
      for (int n = 0; n < 7; n++)
#pragma unroll
        for (int k = 0; k < 4; k++) { const float v = acc[m][n][k], rr = v > 0.f ? v : 0.f; acc[m][n][k] = rr * rr; }
#pragma unroll
      for (int n = 0; n < 6; n += 2) { const int fp = fp0 + (n >> 1); *(bf16x8*)(hb + (fp >> 1) * 16384 + (fp & 1) * 1024 + m * 2048) = pack8(acc[m][n], acc[m][n + 1]); }
      *(u32x2*)(hb + (fph >> 1) * 16384 + (fph & 1) * 1024 + m * 2048 + wn * 8) = u32x2{cvtpk(acc[m][6][0], acc[m][6][1]), cvtpk(acc[m][6][2], acc[m][6][3])};
    }
    STAMP(3, 16 + w);
  }
}

__global__ void k_touch(const u32x4* a,int na,const u32x4* b,int nb,const u32x4* c,int nc,const u32x4* d,int nd,char* ws){ unsigned v=0; int i0=blockIdx.x*512+threadIdx.x;
  for(int i=i0;i<na;i+=131072*4) v^=a[i][0]; for(int i=i0;i<nb;i+=131072*4) v^=b[i][0]; for(int i=i0;i<nc;i+=131072*4) v^=c[i][0]; for(int i=i0;i<nd;i+=131072*4) v^=d[i][0];
  if(v==0x12345679) ws[40<<20]=1; }
extern "C" void kernel_launch(void* const* d_in, const int* in_sizes, int n_in, void* d_out, int out_size, void* d_ws, size_t ws_size, hipStream_t stream) {
  const u16 *x = (const u16*)d_in[0], *wqkv = (const u16*)d_in[1], *wphi = (const u16*)d_in[2], *wo = (const u16*)d_in[3], *w1 = (const u16*)d_in[5], *b1 = (const u16*)d_in[6],
            *w2 = (const u16*)d_in[7], *b2 = (const u16*)d_in[8], *c = (const u16*)d_in[9], *wm = (const u16*)d_in[10];
  char* ws = (char*)d_ws;
  k_touch<<<1024,512,0,stream>>>((const u32x4*)wqkv,3*D_*D_/8,(const u32x4*)wo,D_*D_/8,(const u32x4*)w1,D_*F_/8,(const u32x4*)w2,D_*F_/8,ws);
  k0_mod<<<256, 512, 0, stream>>>(x, c, wm, ws);
  k1_qkv<<<256, 512, 0, stream>>>(x, wqkv, wphi, ws);
  k_res<16, false><<<256, 512, 0, stream>>>((const u16*)(ws + WS_O), wo, x, b2, (u16*)(ws + WS_X1), ws);
  k3_w1<<<256, 512, 0, stream>>>(w1, b1, ws);
  k_res<56, true><<<256, 512, 0, stream>>>((const u16*)(ws + WS_H1), w2, (const u16*)(ws + WS_X1), b2, (u16*)d_out, ws);
}
```

```cpp
#include <hip/hip_runtime.h>
#include <hip/hip_bf16.h>

typedef unsigned short u16;
typedef unsigned u32x4 __attribute__((ext_vector_type(4)));
typedef unsigned u32x2 __attribute__((ext_vector_type(2)));
typedef short s16x4 __attribute__((ext_vector_type(4)));
typedef __bf16 bf16x8 __attribute__((ext_vector_type(8)));
typedef __bf16 bf16x2 __attribute__((ext_vector_type(2)));
typedef float f32x2 __attribute__((ext_vector_type(2)));
typedef float f32x4 __attribute__((ext_vector_type(4)));
#define LAS __attribute__((address_space(3)))
#define DI __device__ __forceinline__
#define SB __builtin_amdgcn_sched_barrier(0)
#define UNR _Pragma("unroll")

constexpr int D_ = 1024, F_ = 3584, MODN = 6 * D_;
constexpr size_t WS_SAP   = 0;
constexpr size_t WS_SIL   = 1 << 16;
constexpr size_t WS_S1    = 327680;
constexpr size_t WS_FLAG  = 3 << 17;
constexpr size_t WS_ST1   = 1 << 19;
constexpr size_t WS_ST2   = WS_ST1 + (1 << 18);
constexpr size_t WS_MARK  = 44 << 20;
constexpr size_t WS_KV1   = WS_MARK;
constexpr size_t WS_KV2   = WS_KV1 + (2 << 20);
constexpr size_t WS_ZP    = WS_KV2 + (2 << 20);
constexpr size_t WS_UV2   = WS_ZP + (1 << 17);
constexpr size_t WS_UV1   = WS_UV2 + (1 << 19);
constexpr size_t WS_MODP  = WS_UV1 + 16 * 12 * 2048;
constexpr int MARK_PER = 8192 + 8192 + 512 + 2048 + 1536 + 1024;
constexpr size_t WS_XCH   = 5 << 20;
constexpr size_t WS_Y1    = 10 << 20;
constexpr size_t WS_O     = 14 << 20;
constexpr size_t WS_X1    = 18 << 20;
constexpr size_t WS_H1    = 22 << 20;
constexpr size_t WS_Y2    = WS_Y1;
constexpr size_t WS_STAMP = 40 << 20;
constexpr int SPIN_CAP = 1 << 14;

#define STAMP(k, i) do { } while (0)
#define RSTAMP(v) do { } while (0)
#define WSTAMP(k, i, v) do { } while (0)

DI void glds16(const void* sbase, unsigned voff, unsigned ldsaddr) {
  asm volatile("s_mov_b32 m0, %2\n\ts_nop 0\n\tglobal_load_lds_dwordx4 %0, %1" :: "v"(voff), "s"(sbase), "s"(ldsaddr) : "memory");
}
DI void glds16nt(const void* sbase, unsigned voff, unsigned ldsaddr) {
  asm volatile("s_mov_b32 m0, %2\n\ts_nop 0\n\tglobal_load_lds_dwordx4 %0, %1 nt" :: "v"(voff), "s"(sbase), "s"(ldsaddr) : "memory");
}
DI void glds16x4(const void* sbase, unsigned voff, unsigned ldsaddr) {
  asm volatile("s_mov_b32 m0, %2\n\ts_nop 0\n\tglobal_load_lds_dwordx4 %0, %1 sc1\n\tglobal_load_lds_dwordx4 %0, %1 offset:1024 sc1\n\t"
               "global_load_lds_dwordx4 %0, %1 offset:2048 sc1\n\tglobal_load_lds_dwordx4 %0, %1 offset:3072 sc1" :: "v"(voff), "s"(sbase), "s"(ldsaddr) : "memory");
}
DI void touch(const void* sbase, unsigned voff, unsigned ldsdummy) {
  asm volatile("s_mov_b32 m0, %2\n\ts_nop 0\n\tglobal_load_lds_dword %0, %1" :: "v"(voff), "s"(sbase), "s"(ldsdummy) : "memory");
}
DI void touch_nt(const void* sbase, unsigned voff, unsigned ldsdummy) {
  asm volatile("s_mov_b32 m0, %2\n\ts_nop 0\n\tglobal_load_lds_dword %0, %1 nt" :: "v"(voff), "s"(sbase), "s"(ldsdummy) : "memory");
}
DI void gld(u32x4& r, const void* sb, unsigned vo) { asm volatile("global_load_dwordx4 %0, %1, %2" : "=v"(r) : "v"(vo), "s"(sb) : "memory"); }
DI void gld_nt(u32x4& r, const void* sb, unsigned vo) { asm volatile("global_load_dwordx4 %0, %1, %2 nt" : "=v"(r) : "v"(vo), "s"(sb) : "memory"); }
DI void gld_dev(u32x4& r, const void* sb, unsigned vo) { asm volatile("global_load_dwordx4 %0, %1, %2 sc1" : "=v"(r) : "v"(vo), "s"(sb) : "memory"); }
DI void gld8(f32x2& r, const void* sb, unsigned vo) { asm volatile("global_load_dwordx2 %0, %1, %2 sc1" : "=v"(r) : "v"(vo), "s"(sb) : "memory"); }
DI void gld4(unsigned& r, const void* sb, unsigned vo) { asm volatile("global_load_dword %0, %1, %2 nt" : "=v"(r) : "v"(vo), "s"(sb) : "memory"); }
template <class T> DI void pin(T& r) { asm volatile("" : "+v"(r)); }
template <class T, int N> DI void pin(T (&r)[N]) { UNR for (int i = 0; i < N; i++) pin(r[i]); }
template <class T> DI T opq(T v) { asm volatile("" : "+s"(v)); return v; }
DI f32x4 asf(u32x4 v) { return __builtin_bit_cast(f32x4, v); }
template <int N> DI void waitvm() { asm volatile("s_waitcnt vmcnt(%0)" :: "i"(N) : "memory"); }
template <int B> DI void waitx(int e) {
  if (e == 0) waitvm<B>(); else if (e == 1) waitvm<B + 1>(); else if (e == 2) waitvm<B + 2>(); else if (e == 3) waitvm<B + 3>(); else if (e < 8) waitvm<B + 4>(); else waitvm<B + 8>();
}
DI void waitlgkm0() { __builtin_amdgcn_s_waitcnt(0xC07F); }
DI void lwait() { asm volatile("s_waitcnt lgkmcnt(0)" ::: "memory"); }
DI void bar() { asm volatile("s_barrier" ::: "memory"); }
DI void lbar() { asm volatile("s_waitcnt lgkmcnt(0)\n\ts_barrier" ::: "memory"); }
DI unsigned cvtpk(float a, float b) { f32x2 v = {a, b}; return __builtin_bit_cast(unsigned, __builtin_convertvector(v, bf16x2)); }
DI float rbf(float a) { return __uint_as_float(cvtpk(a, a) << 16); }
DI f32x4 rbf4(f32x4 s) { return f32x4{rbf(s[0]), rbf(s[1]), rbf(s[2]), rbf(s[3])}; }
DI float blo(unsigned u) { return __uint_as_float(u << 16); }
DI float bhi(unsigned u) { return __uint_as_float(u & 0xffff0000u); }
DI f32x4 unp(unsigned a, unsigned b) { return f32x4{blo(a), bhi(a), blo(b), bhi(b)}; }
DI u32x4 pack8u(const f32x4& a, const f32x4& b) { return u32x4{cvtpk(a[0], a[1]), cvtpk(a[2], a[3]), cvtpk(b[0], b[1]), cvtpk(b[2], b[3])}; }
DI bf16x8 pack8(const f32x4& a, const f32x4& b) { return __builtin_bit_cast(bf16x8, pack8u(a, b)); }
DI f32x4 mfma(bf16x8 a, bf16x8 b, f32x4 c) { return __builtin_amdgcn_mfma_f32_16x16x32_bf16(a, b, c, 0, 0, 0); }
DI s16x4 trd(unsigned a) { return __builtin_amdgcn_ds_read_tr16_b64_v4i16((LAS s16x4*)(size_t)a); }
DI bf16x8 cat(s16x4 lo, s16x4 hi) { return __builtin_bit_cast(bf16x8, __builtin_shufflevector(lo, hi, 0, 1, 2, 3, 4, 5, 6, 7)); }
template <class T> DI T ldsr(unsigned a) { return *(const LAS T*)(size_t)a; }
template <class T> DI void ldsw(unsigned a, T v) { *(LAS T*)(size_t)a = v; }
template <int C> DI float dpp(float x) { return __builtin_bit_cast(float, __builtin_amdgcn_update_dpp(0, __builtin_bit_cast(int, x), C, 0xf, 0xf, true)); }
DI float vmax(float a, float b) { float r; asm("v_max_f32_e32 %0, %1, %2" : "=v"(r) : "v"(a), "v"(b)); return r; }
DI float sw32_add(float a, float b) { auto r = __builtin_amdgcn_permlane32_swap(__float_as_uint(a), __float_as_uint(b), false, false); return __uint_as_float(r[0]) + __uint_as_float(r[1]); }
DI float sw16_add(float a, float b) { auto r = __builtin_amdgcn_permlane16_swap(__float_as_uint(a), __float_as_uint(b), false, false); return __uint_as_float(r[0]) + __uint_as_float(r[1]); }
DI float xg_sum(float a) { a = sw32_add(a, a); return sw16_add(a, a); }
DI float xg_max(float a) {
  auto r = __builtin_amdgcn_permlane32_swap(__float_as_uint(a), __float_as_uint(a), false, false); a = vmax(__uint_as_float(r[0]), __uint_as_float(r[1]));
  auto q = __builtin_amdgcn_permlane16_swap(__float_as_uint(a), __float_as_uint(a), false, false); return vmax(__uint_as_float(q[0]), __uint_as_float(q[1]));
}
DI float row16_sum(float a) { a += dpp<0x128>(a); a += dpp<0x124>(a); a += dpp<0x122>(a); a += dpp<0x121>(a); return a; }
DI f32x4 row16_sum4(f32x4 a) { return f32x4{row16_sum(a[0]), row16_sum(a[1]), row16_sum(a[2]), row16_sum(a[3])}; }
DI f32x4 spl(float a) { return f32x4{a, a, a, a}; }
DI f32x4 fma4(f32x4 a, f32x4 b, f32x4 c) { return __builtin_elementwise_fma(a, b, c); }
struct RowSt { float rs, nm; DI void set(unsigned a, float k) { rs = ldsr<float>(a) * k; nm = ldsr<float>(a + 512) * k; asm("" : "+v"(rs)); asm("" : "+v"(nm)); } };
DI f32x4 lnf(f32x4 acc, const RowSt& st, f32x4 u, f32x4 v) { return fma4(acc, spl(st.rs), fma4(u, spl(st.nm), v)); }
DI float fadd(float a, float b) { float r; asm("v_add_f32_e32 %0, %1, %2" : "=v"(r) : "v"(a), "v"(b)); return r; }
DI float hsum4(f32x4 a) { const f32x2 t = f32x2{a[0], a[1]} + f32x2{a[2], a[3]}; float r; asm("v_add_f32_e32 %0, %1, %2" : "=v"(r) : "v"(t[0]), "v"(t[1])); return r; }
typedef __amdgpu_buffer_rsrc_t rsrc_t;
DI rsrc_t mkrsrc(const void* p, unsigned bytes) { return __builtin_amdgcn_make_buffer_rsrc((void*)p, 0, bytes, 0x00020000); }
DI u32x4 ld_dev(rsrc_t r, unsigned voff, unsigned soff) { return __builtin_amdgcn_raw_buffer_load_b128(r, voff, soff, 16); }
DI void st_dev(u32x4 v, rsrc_t r, unsigned voff, unsigned soff) { __builtin_amdgcn_raw_buffer_store_b128(v, r, voff, soff, 16); asm volatile("s_nop 1" :: "v"(v) : "memory"); }
DI unsigned umax4(u32x4 v) { return max(max(v[0], v[1]), max(v[2], v[3])); }
template <int N, class F> DI void poll(u32x4 (&v)[N], F ld) {
  _Pragma("nounroll") for (int spin = 0; spin < SPIN_CAP; spin++) { unsigned m = 0;
    UNR for (int i = 0; i < N; i++) v[i] = ld(i);
    UNR for (int i = 0; i < N; i++) m = max(m, umax4(v[i]));
    if (!__any(m == 0xffffffffu)) break;
    asm volatile("s_sleep 2" ::: "memory");
  }
}
#define EV 0
#define K0V 0
DI void st_dev8(u32x2 v, rsrc_t r, unsigned voff, unsigned soff) { __builtin_amdgcn_raw_buffer_store_b64(v, r, voff, soff, 16); }
DI float fmax3(float a, float b, float c) { return __builtin_elementwise_maximum(__builtin_elementwise_maximum(a, b), c); }
struct Blk { int tm, tn, rot, j, xq; };
DI Blk blk1() { const int xcd = blockIdx.x & 7, j = blockIdx.x >> 3; Blk b; b.tm = j >> 1; b.tn = xcd * 2 + (j & 1); b.rot = b.tm; b.j = j; b.xq = xcd; return b; }
DI Blk blk() { const int xcd = blockIdx.x & 7, j = blockIdx.x >> 3; Blk b; b.tm = (xcd >> 2) * 8 + (j >> 2); b.tn = (xcd & 3) * 4 + (j & 3); b.rot = (b.tm & 7) * 2 + (b.tm >> 3); b.j = j; b.xq = xcd & 3; return b; }
DI unsigned ptile(int c, int lane) { const int row = 4 * (lane >> 4) + ((lane & 15) >> 2); return row * 128 + ((c ^ ((row >> 1) & 3)) * 32) + (lane & 3) * 8; }
DI unsigned htile(int c, int lane) { const int row = 4 * (lane >> 4) + ((lane & 15) >> 2); return row * 64 + ((c ^ ((row >> 2) & 1)) * 32) + (lane & 3) * 8; }
DI bf16x8 wfrag(unsigned a, unsigned ts) { return cat(trd(a), trd(a + ts)); }

DI unsigned silu2(unsigned cw) { const float c0 = blo(cw), c1 = bhi(cw); return cvtpk(c0 * __builtin_amdgcn_rcpf(1.f + __expf(-c0)), c1 * __builtin_amdgcn_rcpf(1.f + __expf(-c1))); }
DI unsigned fragpos(int k) { return (k >> 5) * 64 + ((k >> 2) & 3) * 16 + ((k & 3) + 4 * ((k >> 4) & 1)) * 2; }
DI f32x4 gemv64(unsigned a, bf16x8 s0, bf16x8 s1, int lane) { f32x4 d[4];
  UNR for (int cc = 0; cc < 4; cc++) d[cc] = mfma(wfrag(a + 4096 + ptile(cc, lane), 2048), s1, mfma(wfrag(a + ptile(cc, lane), 2048), s0, f32x4{0, 0, 0, 0}));
  const bool b0 = lane & 1, b1 = lane & 2; f32x4 o;
  UNR for (int k = 0; k < 4; k++) { const float lo = b0 ? d[1][k] : d[0][k], hi = b0 ? d[3][k] : d[2][k]; o[k] = b1 ? hi : lo; }
  return o;
}
__global__ __launch_bounds__(512, 2) void k0_mod(const u16* __restrict__ x, const u16* __restrict__ c, const u16* __restrict__ wm, const u16* __restrict__ wqkv, char* __restrict__ ws) {
  constexpr int XT = 131072, SAU = XT + 16384, SIL = SAU + 8192, RED = SIL + 2048, SROW = RED + 2048, SIL2 = SROW + 2048, DUM = SIL2 + 256, TOT = DUM + 256;
  __shared__ __attribute__((aligned(1024))) char smem[TOT];
  const int tid = threadIdx.x, lane = tid & 63, w = __builtin_amdgcn_readfirstlane(tid >> 6), r8 = lane >> 3, g = lane >> 4, r = lane & 15;
  const unsigned lds0 = (unsigned)(size_t)(LAS char*)smem; const Blk B = blk1(); const f32x4 zero = {0, 0, 0, 0};
  STAMP(0, w);
  unsigned cw, cw2; gld4(cw, c, w * 256 + ((B.tm * 16 + lane * 4) & 255)); gld4(cw2, c, B.tm * 128 + (lane & 31) * 4);
  const char* wb = (const char*)wm + B.tn * 128; const unsigned wv = r8 * MODN * 2 + (((lane & 7) ^ (((r8 >> 1) & 3) << 1)) * 16);
  glds16nt(wb + (size_t)(w * 16 + B.tm) * 16 * MODN + 2 * D_, wv, lds0 + w * 16384);
  const unsigned rv0 = r8 * 2048 + (((lane & 7) ^ (r8 >> 1)) * 16);
  UNR for (int i = 0; i < 2; i++) glds16nt((const char*)x + ((size_t)(B.tm * 128 + w * 16 + i * 8) * D_ + B.tn * 64) * 2, i ? rv0 ^ 64 : rv0, lds0 + XT + (w * 2 + i) * 1024);
  bar();
  UNR for (int i = 1; i < 16; i++) glds16(wb + (size_t)(w * 16 + ((B.tm + i) & 15)) * 16 * MODN + 2 * D_, wv, lds0 + (w * 16 + i) * 1024);
  RSTAMP(ta);
  bar();
  glds16nt(wb + (size_t)(B.tm * 8 + w) * 16 * MODN, wv, lds0 + SAU + w * 1024);
  { const int e = min(w * 24 + lane, 191), row = e / 6, l = e - row * 6;
    if (lane < 24) touch_nt((const char*)wqkv + (size_t)B.j * 32 * 6144 + B.xq * 256, (K0V & 2) ? 0 : row * 6144 + (l >> 1) * 2048 + (l & 1) * 128, lds0 + DUM); }
  RSTAMP(tb);
  waitvm<10>(); pin(cw); pin(cw2);
  const unsigned sw = silu2(cw);
  ldsw<unsigned>(lds0 + SIL + w * 256 + fragpos(2 * lane), sw);
  if (w == 7) ldsw<unsigned>(lds0 + SIL2 + fragpos(2 * (lane & 31)), silu2(cw2));
  asm volatile("" ::: "memory");
  f32x4 a[4] = {zero, zero, zero, zero};
  auto kstep = [&](int ks) { const bf16x8 sf = ldsr<bf16x8>(lds0 + SIL + (w * 4 + ks) * 64 + g * 16);
    UNR for (int cc = 0; cc < 4; cc++) a[cc] = mfma(wfrag(lds0 + w * 16384 + ks * 4096 + ptile(cc, lane), 2048), sf, a[cc]); };
  kstep(0); kstep(1); waitvm<6>(); kstep(2); waitvm<2>(); kstep(3);
  RSTAMP(tc);
  if (r == 0) UNR for (int cc = 0; cc < 4; cc++) ldsw<f32x4>(lds0 + RED + w * 256 + cc * 64 + g * 16, a[cc]);
  f32x4 xv[4];
  UNR for (int cc = 0; cc < 4; cc++) { const u32x2 t = ldsr<u32x2>(lds0 + XT + (w * 16 + r) * 128 + (((cc * 2 + (g >> 1)) ^ (r >> 1)) * 16) + (g & 1) * 8); xv[cc] = unp(t[0], t[1]); }
  lbar(); RSTAMP(td);
  { float s = 0;
    UNR for (int i = 0; i < 8; i++) s += ldsr<float>(lds0 + RED + i * 256 + lane * 4);
    s = rbf(1.f + rbf(s)); ldsw<float>(lds0 + SROW + w * 256 + lane * 4, s);
    if (w == 6 && B.tm == 0) ((float*)(ws + WS_S1))[B.tn * 64 + lane] = s; }
  asm volatile("" ::: "memory");
  char* yb = ws + WS_Y1 + (size_t)(B.tm * 16 + B.tn) * 16384 + w * 2048 + lane * 16;
  UNR for (int ks = 0; ks < 2; ks++)
    *(u32x4*)(yb + ks * 1024) = pack8u(xv[2 * ks] * ldsr<f32x4>(lds0 + SROW + w * 256 + ks * 128 + g * 16), xv[2 * ks + 1] * ldsr<f32x4>(lds0 + SROW + w * 256 + ks * 128 + 64 + g * 16));
  STAMP(0, 8 + (w & 3));
  { const rsrc_t rc = mkrsrc(ws + WS_MARK + (size_t)blockIdx.x * MARK_PER, MARK_PER);
    UNR for (int i = 0; i < 3; i++) if (i * 8192 + tid * 16 < MARK_PER) st_dev(u32x4{~0u, ~0u, ~0u, ~0u}, rc, tid * 16, i * 8192);
    if (tid < 24) *(u32x4*)(ws + WS_FLAG + blockIdx.x * 384 + tid * 16) = u32x4{0, 0, 0, 0};
    if (blockIdx.x == 0) *(unsigned*)(ws + WS_SIL + w * 256 + fragpos(2 * lane)) = sw; }
  { const float mean = xg_sum(hsum4((xv[0] + xv[1]) + (xv[2] + xv[3]))) * (1.f / 64); f32x4 q = zero;
    UNR for (int cc = 0; cc < 4; cc++) { const f32x4 d = xv[cc] - mean; q = fma4(d, d, q); }
    const float m2 = xg_sum(hsum4(q));
    if (g == 0) ((f32x2*)(ws + WS_ST1))[(B.tm * 16 + B.tn) * 128 + w * 16 + r] = f32x2{mean, m2}; }
  waitvm<6>(); bar();
  if (w == 7) { const f32x4 d = gemv64(lds0 + SAU, ldsr<bf16x8>(lds0 + SIL2 + g * 16), ldsr<bf16x8>(lds0 + SIL2 + 64 + g * 16), lane);
    if (r < 4) *(f32x4*)(ws + WS_SAP + (size_t)(B.tn * 16 + B.tm) * 256 + r * 64 + g * 16) = d; }
  waitvm<0>();
  STAMP(0, 28 + (w & 3)); WSTAMP(0, 12 + (w & 3), ta); WSTAMP(0, 16 + (w & 3), tb); WSTAMP(0, 20 + (w & 3), tc); WSTAMP(0, 24 + (w & 3), td);
}

template <int CFG> struct Cfg;
template <> struct Cfg<0> { static constexpr int NT = 6, NP = 3, HALF = 0, NB = 2, NS = 3, N = 3 * D_, KT = 16, B0 = 49152; };
template <> struct Cfg<1> { static constexpr int NT = 7, NP = 3, HALF = 1, NB = 7, NS = 3, N = F_, KT = 16, B0 = 65536; };
template <> struct Cfg<2> { static constexpr int NT = 2, NP = 1, HALF = 0, NB = 2, NS = 4, N = D_, KT = 16, B0 = 106496; };
template <> struct Cfg<3> { static constexpr int NT = 4, NP = 2, HALF = 0, NB = 4, NS = 4, N = D_, KT = 28, B0 = 94208; };
template <int CFG> struct Lay { using C = Cfg<CFG>; static constexpr int SBB = C::NP * 8192 + C::HALF * 4096, PB = 2 * C::NP + C::HALF, PER = 4 + PB; };
template <int CFG> DI unsigned btile(int tl, int lane) { using C = Cfg<CFG>; return C::B0 + (tl < 4 * C::NP ? (tl >> 2) * 8192 + ptile(tl & 3, lane) : C::NP * 8192 + htile(tl & 1, lane)); }
constexpr int L_RES = 139264, L_WPHI = 155648, L_UVT = L_WPHI + 4096, L_STT = L_UVT + 2048, L_ET = L_STT + 1024, L_GT = L_ET + 256, L_DUM = L_GT + 512, L_TOT = 163840, L_REST = 122880, L_OT = 65536;
constexpr int L_TR = 0, L_VF = 32768, L_KX = 49152;

template <int CFG> DI void compute_loop(f32x4 (&acc)[4][Cfg<CFG>::NT], unsigned lds0, int wm, int wn, int lane, char* ws) {
  using C = Cfg<CFG>; using L = Lay<CFG>; constexpr int MT = 4, NT = C::NT, NB = C::NB, KT = C::KT;
  const unsigned aoff = lds0 + wm * 8192 + lane * 16; unsigned boff[NB];
  UNR for (int i = 0; i < NB; i++) boff[i] = lds0 + btile<CFG>(CFG == 1 ? (i < 6 ? wn * 6 + i : 12 + wn) : wn * NB + i, lane);
  bf16x8 fa[2][MT]; s16x4 fb[2][NT][2]; unsigned va, vb[NB];
  constexpr int NR = MT + 2 * NT, NM = MT * NT, NU = 1 + NB, FREE = NM - NR, UPG = FREE > 0 ? (NU + FREE - 1) / FREE : NU;
  auto upd = [&](int i, unsigned sa, unsigned sb) { if (i == 0) { va = aoff + sa; pin(va); } else { vb[i - 1] = boff[i - 1] + sb; pin(vb[i - 1]); } };
  auto rd1 = [&](int buf, int ks, int i) {
    if (i == 0 || i > 2 * NT) { const int m = i ? i - 2 * NT : 0; fa[buf][m] = ldsr<bf16x8>(va + m * 2048 + ks * 1024); }
    else { const int n = (i - 1) >> 1, t = (i - 1) & 1;
      fb[buf][n][t] = trd(CFG == 0 ? vb[n & 1] + (n >> 1) * 8192 + ks * 4096 + t * 2048 : (CFG == 1 && n == 6) ? vb[NB - 1] + ks * 2048 + t * 1024 : vb[n] + ks * 4096 + t * 2048); }
  };
  auto step = [&](int buf, int ks, bool doread, bool first, bool doupd, unsigned sa, unsigned sb) {
    UNR for (int i = 0; i < NM; i++) {
      const int m = i / NT, n = i % NT; const bf16x8 b = cat(fb[buf][n][0], fb[buf][n][1]);
      const f32x4 cin = first ? f32x4{0, 0, 0, 0} : acc[m][n];
      acc[m][n] = (CFG == 0 && n >= 4) ? mfma(fa[buf][m], b, cin) : mfma(b, fa[buf][m], cin);
      SB;
      if (doread && i < NR) { rd1(buf ^ 1, ks, i); SB; }
      if (doupd && (FREE > 0 ? i >= NR : i == NM - 1)) { const int u0 = FREE > 0 ? (i - NR) * UPG : 0;
        UNR for (int u = u0; u < u0 + UPG; u++) if (u < NU) upd(u, sa, sb);
        SB; }
    }
  };
  UNR for (int i = 0; i < NU; i++) upd(i, 0, 0);
  SB; lbar();
  UNR for (int i = 0; i < NR; i++) rd1(0, 0, i);
  SB;
  unsigned sa = 16384, sb = L::SBB;
  step(0, 1, true, true, true, sa, sb);
  for (int t = 0; t < KT - 1; t++) {
    waitlgkm0(); bar(); SB;
    sa += 16384; sb += L::SBB; if (sa == C::NS * 16384) { sa = 0; sb = 0; }
    step(1, 0, true, false, false, 0, 0);
    if (FREE >= 4) { waitlgkm0(); SB; }
    step(0, 1, true, false, true, sa, sb);
  }
  step(1, 0, false, false, false, 0, 0);
}

template <int CFG> struct Loader {
  using C = Cfg<CFG>; using L = Lay<CFG>; static constexpr int N = C::N, KT = C::KT, PER = L::PER, PB = L::PB, NS = C::NS;
  const char *ab, *bb, *hb; unsigned av, bv, hv, la, lb, lh; int ka, kb;
  DI void init(const void* A, int akt, const u16* W, int k0, int col0, int hcol, int rot, int tm, int lw, int lane, unsigned lds0) {
    const int r = lane >> 3, hr = lane >> 2;
    ab = (const char*)A + ((size_t)tm * akt + k0) * 16384 + lw * 4096; av = lane * 16;
    bb = (const char*)W + ((size_t)(k0 * 64 + lw * 16) * N + col0) * 2; bv = r * N * 2 + (((lane & 7) ^ (((r >> 1) & 3) << 1)) * 16);
    hb = (const char*)W + ((size_t)(lw * 16) * N + hcol) * 2; hv = hr * N * 2 + (((lane & 3) ^ (((hr >> 2) & 1) << 1)) * 16);
    if (CFG == 3) { const int t = lw & 1; bb -= (size_t)t * 16 * N * 2; hv = bv + (4 * t + 4 * (r >> 2)) * N * 2; bv += t * 16 * N * 2; }
    la = lds0 + lw * 4096; lb = lds0 + C::B0 + lw * 2048; lh = lds0 + C::B0 + C::NP * 8192 + lw * 1024; ka = kb = rot;
  }
  DI void issueA(int slot) { glds16x4(ab + (size_t)ka * 16384, av, la + slot * 16384); ka++; if (ka == KT) ka = 0; }
  template <bool NT> DI void issueB(int slot) { const char* b = bb + (size_t)kb * 64 * N * 2; const unsigned so = slot * L::SBB;
    const bool nat = CFG == 3 && ((0x1020408u >> kb) & 1);
    const unsigned v0 = nat ? hv : bv, v1 = v0 + (nat ? 16 : 8) * N * 2;
    UNR for (int i = 0; i < 2 * C::NP; i++) { const char* p = b + (i >> 1) * (CFG == 0 ? 2048 : 128) + (CFG == 3 ? 0 : (i & 1) * (8 * N * 2)); const unsigned l = lb + so + (i >> 1) * 8192 + (i & 1) * 1024, vo = CFG == 3 && (i & 1) ? v1 : v0;
      if (NT) glds16nt(p, vo, l); else glds16(p, vo, l); }
    if (C::HALF) { if (NT) glds16nt(hb + (size_t)kb * 64 * N * 2, hv, lh + so); else glds16(hb + (size_t)kb * 64 * N * 2, hv, lh + so); }
    kb++; if (kb == KT) kb = 0;
  }
  DI void issue(int slot) { issueA(slot); issueB<false>(slot); }
  template <bool PRE, int E1, class HK, class H1> DI void run(HK hk, H1 h1) {
    if (NS == 3) {
      int e0, slot = 0;
      if (PRE) { UNR for (int s = 0; s < NS; s++) issueA(s); waitvm<8>(); bar(); e0 = hk(0); waitx<4>(e0); lbar(); }
      else { issue(0); issue(1); waitvm<PER>(); bar(); issue(2); e0 = hk(0); waitx<PER>(e0); lbar(); }
      _Pragma("nounroll") for (int t = 1; t <= KT - 3; t++) { issue(slot); slot++; if (slot == NS) slot = 0; const int e1 = hk(t); waitx<PER>(e0 + e1); lbar(); e0 = e1; }
    } else {
      UNR for (int s = 0; s < NS; s++) issueA(s);
      waitvm<12>(); bar(); waitvm<8>(); bar();
      issue(0); waitvm<4 + PER>(); bar();
      int slot = 1;
      for (int t = 2; t <= KT - 4; t++) { issue(slot); slot = (slot + 1) & 3; waitvm<2 * PER>(); bar(); }
      waitvm<PER>(); bar();
    }
    h1(); waitvm<E1>(); bar();
  }
};
template <class H> DI void panel_sync(char* ws, int ph, const Blk& P, const Blk& C, int lw, int lane, H pre) {
  bar();
  unsigned* f = (unsigned*)(ws + WS_FLAG) + ph * 32;
  if (lw == 0 && lane < 16) __hip_atomic_store(f + (P.tm * 16 + lane) * 96 + P.tn, 1u, __ATOMIC_RELAXED, __HIP_MEMORY_SCOPE_AGENT);
  pre();
  if (lw == 0) {
    _Pragma("nounroll") for (int spin = 0; spin < SPIN_CAP; spin++) {
      if (__all(__hip_atomic_load(f + (C.tm * 16 + C.tn) * 96 + (lane & 15), __ATOMIC_RELAXED, __HIP_MEMORY_SCOPE_AGENT) != 0u)) break;
      asm volatile("s_sleep 1" ::: "memory");
    }
  }
  bar();
}
DI int modcol(int lane) { return 4 * (lane & 15) + 2 * (lane >> 5) + ((lane >> 4) & 1); }
DI float modvec(const char* part, int lane) {
  const rsrc_t r = mkrsrc(part, 4096); u32x4 p[4]; poll(p, [&](int i) { return ld_dev(r, lane * 16, i * 1024); });
  const f32x4 a = (asf(p[0]) + asf(p[1])) + (asf(p[2]) + asf(p[3]));
  return rbf(sw16_add(sw32_add(a[0], a[2]), sw32_add(a[1], a[3])));
}
DI void put_et(unsigned lds0, int vec, int k, float v) { ldsw<u16>(lds0 + L_ET + vec * 128 + fragpos(k), (u16)(__float_as_uint(v) >> 16)); }

template <int CFG, int NTL> struct UV {
  static DI void publish(rsrc_t r, int tm, int lw, int lane, unsigned lds0, int slot) {
    const bf16x8 e0 = ldsr<bf16x8>(lds0 + L_ET + (lane & 1) * 128 + (lane >> 4) * 16), e1 = ldsr<bf16x8>(lds0 + L_ET + (lane & 1) * 128 + 64 + (lane >> 4) * 16);
    f32x4 d[NTL];
    UNR for (int i = 0; i < NTL; i++) { const int tl = min(lw * NTL + i, 2 * Cfg<CFG>::NT - 1); const unsigned a = lds0 + slot * Lay<CFG>::SBB + btile<CFG>(tl, lane), ts = tl < 4 * Cfg<CFG>::NP ? 2048 : 1024;
      d[i] = mfma(wfrag(a + 2 * ts, ts), e1, mfma(wfrag(a, ts), e0, f32x4{0, 0, 0, 0})); }
    if ((lane & 14) == 0) UNR for (int i = 0; i < NTL; i++) st_dev(__builtin_bit_cast(u32x4, d[i]), r, (lane & 1) * 64 + (lane >> 4) * 16, (lw * NTL + i) * 2048 + tm * 128);
  }
  u32x4 v[2 * NTL];
  DI void fetch(const char* base, int lw, int lane) { UNR for (int i = 0; i < 2 * NTL; i++) gld_dev(v[i], base + lw * NTL * 2048 + i * 1024, lane * 16); }
  DI void finish(rsrc_t r, int lw, int lane, unsigned tab) {
    pin(v); unsigned mx = 0;
    UNR for (int i = 0; i < 2 * NTL; i++) mx = max(mx, umax4(v[i]));
    if (__any(mx == 0xffffffffu)) poll(v, [&](int i) { return ld_dev(r, lane * 16, lw * NTL * 2048 + i * 1024); });
    UNR for (int i = 0; i < NTL; i++) { const f32x4 a = asf(v[2 * i]) + asf(v[2 * i + 1]);
      float t = sw16_add(sw32_add(a[0], a[2]), sw32_add(a[1], a[3])); t += dpp<0x128>(t);
      ldsw<float>(tab + (lw * NTL + i) * 128 + (lane & 7) * 16 + (2 * (lane >> 5) + ((lane >> 4) & 1)) * 4, t); }
  }
};
template <int NPART> struct Stats {
  f32x2 v[NPART / 2];
  DI void fetch(const char* st, int tm, int lw, int lane) { UNR for (int i = 0; i < NPART / 2; i++) gld8(v[i], st + (size_t)(tm * NPART + i) * 1024 + lw * 256, (lane >> 5) * (NPART * 512) + (lane & 31) * 8); }
  DI void finish(int lw, int lane, unsigned tab) { pin(v); f32x2 s = v[0]; float q = 0;
    UNR for (int i = 1; i < NPART / 2; i++) s += v[i];
    const float mean = sw32_add(s[0], s[0]) * (1.f / NPART);
    UNR for (int i = 0; i < NPART / 2; i++) { const float d = v[i][0] - mean; q += d * d; }
    q = s[1] + (float)(D_ / NPART) * q;
    const float rstd = __builtin_amdgcn_rsqf(sw32_add(q, q) * (1.f / D_) + 1e-5f);
    ldsw<float>(tab + (lane >> 5) * 512 + (lw * 32 + (lane & 31)) * 4, lane < 32 ? rstd : -mean * rstd); }
};

template <bool MAX> DI float fold4(float a, float b, float c, float d) {
  auto op = [](unsigned x, unsigned y) { return MAX ? __builtin_elementwise_maximum(__uint_as_float(x), __uint_as_float(y)) : __uint_as_float(x) + __uint_as_float(y); };
  const auto r0 = __builtin_amdgcn_permlane32_swap(__float_as_uint(a), __float_as_uint(b), false, false), r1 = __builtin_amdgcn_permlane32_swap(__float_as_uint(c), __float_as_uint(d), false, false);
  const auto r2 = __builtin_amdgcn_permlane16_swap(__float_as_uint(op(r0[0], r0[1])), __float_as_uint(op(r1[0], r1[1])), false, false);
  return op(r2[0], r2[1]);
}
DI void spread4(float t, float& a, float& b, float& c, float& d) {
  const auto r2 = __builtin_amdgcn_permlane16_swap(__float_as_uint(t), __float_as_uint(t), false, false);
  const auto r0 = __builtin_amdgcn_permlane32_swap(r2[0], r2[0], false, false), r1 = __builtin_amdgcn_permlane32_swap(r2[1], r2[1], false, false);
  a = __uint_as_float(r0[0]); b = __uint_as_float(r0[1]); c = __uint_as_float(r1[0]); d = __uint_as_float(r1[1]);
}
DI void feat2(const bf16x8 (&wf)[2], bf16x8 x0, bf16x8 x1, bf16x8& fp0, bf16x8& fn0, bf16x8& fp1, bf16x8& fn1) {
  const f32x4 zero = {0, 0, 0, 0}; f32x4 p[2][2] = {{mfma(wf[0], x0, zero), mfma(wf[1], x0, zero)}, {mfma(wf[0], x1, zero), mfma(wf[1], x1, zero)}};
  float mx[2], nx[2], sp[2], sn[2]; f32x4 a[2][2], b[2][2];
  UNR for (int i = 0; i < 2; i++) { const f32x4 u = p[i][0], v = p[i][1];
    mx[i] = fmax3(fmax3(fmax3(u[0], u[1], u[2]), u[3], v[0]), fmax3(v[1], v[2], v[3]), v[3]); nx[i] = fmax3(fmax3(fmax3(-u[0], -u[1], -u[2]), -u[3], -v[0]), fmax3(-v[1], -v[2], -v[3]), -v[3]); }
  spread4(fold4<true>(mx[0], mx[1], nx[0], nx[1]), mx[0], mx[1], nx[0], nx[1]);
  UNR for (int i = 0; i < 2; i++) UNR for (int h = 0; h < 2; h++) UNR for (int j = 0; j < 4; j += 2) { const f32x2 t = {p[i][h][j], p[i][h][j + 1]};
    f32x2 d = t - f32x2{mx[i], mx[i]}, e = -t - f32x2{nx[i], nx[i]}; asm("" : "+v"(d), "+v"(e));
    a[i][h][j] = __builtin_amdgcn_exp2f(d[0]); a[i][h][j + 1] = __builtin_amdgcn_exp2f(d[1]); b[i][h][j] = __builtin_amdgcn_exp2f(e[0]); b[i][h][j + 1] = __builtin_amdgcn_exp2f(e[1]); }
  UNR for (int i = 0; i < 2; i++) { sp[i] = hsum4(a[i][0] + a[i][1]); sn[i] = hsum4(b[i][0] + b[i][1]); }
  spread4(__builtin_amdgcn_rcpf(fold4<false>(sp[0], sp[1], sn[0], sn[1])), sp[0], sp[1], sn[0], sn[1]);
  fp0 = pack8(a[0][0] * sp[0], a[0][1] * sp[0]); fn0 = pack8(b[0][0] * sn[0], b[0][1] * sn[0]); fp1 = pack8(a[1][0] * sp[1], a[1][1] * sp[1]); fn1 = pack8(b[1][0] * sn[1], b[1][1] * sn[1]);
}

struct Args { const u16 *x, *wqkv, *wphi, *wo, *w1, *b1, *w2, *b2, *wm; u16* out; char* ws; };

DI void loader_role(const Args& A, int lw, unsigned lds0) {
  char* ws = A.ws; const Blk B2 = blk(), B1 = blk1(); const f32x4 zero = {0, 0, 0, 0};
  int lane = threadIdx.x & 63;
  Loader<2> G2;
  if (EV & 8) __builtin_amdgcn_s_setprio(3);
  {
    const Blk& B = B1; const int g = lane >> 4, r = lane & 15, q = r >> 2, p = lane & 3, r8 = lane >> 3;
    Loader<0> G; G.init(ws + WS_Y1, 16, A.wqkv, 0, B.tn * 64, 0, B.rot, B.tm, lw, lane, lds0);
    const char* uvb = ws + WS_UV1 + (size_t)B.tn * 12 * 2048; const rsrc_t ruv = mkrsrc(uvb, 12 * 2048);
    UV<0, 3> U; Stats<16> S; u32x4 sf[2];
    UNR for (int i = 0; i < 2; i++) gld(sf[i], ws + WS_SIL + B.tm * 128 + i * 64, g * 16);
    const int t1 = 2 + (B.tm & 7), t3 = 2 + ((B.tm + 4) & 7) + ((B.tm >> 3) & 1) * 2;
    G.run<false, 15>([&](int t) { int e = 0;
        if (t == 0) { pin(sf); UV<0, 3>::publish(ruv, B.tm, lw, lane, lds0, 0); e = 3; }
        if (t == t1 && !(EV & 1)) {
          const char* wb = opq((const char*)A.wm) + (size_t)B.tm * 128 * MODN + (lw + 2) * 2 * D_ + B.tn * 128; const unsigned wv = r8 * MODN * 2 + (((lane & 7) ^ (((r8 >> 1) & 3) << 1)) * 16);
          const unsigned l0 = opq(lds0 + L_REST) + lw * 8192;
          UNR for (int i = 0; i < 8; i++) glds16nt(wb + i * 16 * MODN, wv, l0 + i * 1024);
          e += 8; }
        if (t == t1 + 3) { int ln = lane; pin(ln); const f32x4 d = gemv64(lds0 + L_REST + lw * 8192, __builtin_bit_cast(bf16x8, sf[0]), __builtin_bit_cast(bf16x8, sf[1]), ln);
          if (r < 4) st_dev(__builtin_bit_cast(u32x4, d), mkrsrc(opq(ws) + WS_MODP + (size_t)((lw * 16 + B.tn) * 16 + B.tm) * 256, 256), r * 64 + g * 16, 0);
          e += 1; }
        if (t == t3 && !(EV & 2)) { touch(opq((const char*)A.w1) + (size_t)(blockIdx.x * 4 + lw) * 7168, min(lane, 55) * 128, lds0 + L_DUM); touch(opq((const char*)A.wo) + (size_t)(blockIdx.x * 4 + lw) * 2048, (lane & 15) * 128, lds0 + L_DUM); e += 2; }
        return e; },
      [&]() { U.fetch(uvb, lw, lane); S.fetch(ws + WS_ST1, B.tm, lw, lane); glds16nt((const char*)A.wphi + B.tn * 4096 + lw * 1024, lane * 16, lds0 + L_WPHI + lw * 1024); });
    G2.init(ws + WS_O, 16, A.wo, 0, B2.tn * 64, 0, B2.rot, B2.tm, lw, lane, lds0);
    { const unsigned rv0 = r8 * 2048 + (((lane & 7) ^ (r8 >> 1)) * 16);
      UNR for (int i = 0; i < 4; i++) glds16nt((const char*)A.x + ((size_t)(B2.tm * 128 + (lw * 4 + i) * 8) * D_ + B2.tn * 64) * 2, (i & 1) ? rv0 ^ 64 : rv0, lds0 + L_RES + (lw * 4 + i) * 1024); }
    G2.issueB<false>(0); G2.issueB<false>(1);
    __builtin_amdgcn_s_setprio(3);
    waitvm<8>(); U.finish(ruv, lw, lane, lds0 + L_UVT); S.finish(lw, lane, lds0 + L_STT);
    __builtin_amdgcn_s_setprio((EV & 8) ? 3 : 0);
    lbar();
    bf16x8 wf[2];
    UNR for (int c = 0; c < 2; c++) wf[c] = wfrag(lds0 + L_WPHI + (lw & 1) * 2048 + (4 * g + q) * 64 + c * 32 + p * 8, 1024);
    bar();
    { const unsigned trw = lds0 + L_TR + lw * 8192 + r * 64 + ((g ^ ((r >> 1) & 3)) * 16); bf16x8 a, b, c, d;
      feat2(wf, ldsr<bf16x8>(lds0 + L_KX + lw * 2048 + lane * 16), ldsr<bf16x8>(lds0 + L_KX + lw * 2048 + 1024 + lane * 16), a, b, c, d);
      ldsw<bf16x8>(trw + 2048, a); ldsw<bf16x8>(trw + 2048 + 4096, b); ldsw<bf16x8>(trw + 3072, c); ldsw<bf16x8>(trw + 3072 + 4096, d); }
    lbar();
    __builtin_amdgcn_s_setprio(3);
    const int hd = lw >> 1, sg = lw & 1;
    const u32x4 one4 = {0x3f803f80u, 0x3f803f80u, 0x3f803f80u, 0x3f803f80u}; const bf16x8 ones = __builtin_bit_cast(bf16x8, one4);
    const unsigned tra = lds0 + L_TR + hd * 8192 + sg * 4096 + (4 * g + q) * 64 + ((p ^ (((4 * g + q) >> 1) & 3)) * 16), vfa = lds0 + L_VF + hd * 4096 + lane * 16;
    f32x4 kv[2][2] = {{zero, zero}, {zero, zero}}, z[2] = {zero, zero};
    UNR for (int k = 0; k < 4; k++) { const bf16x8 v0 = ldsr<bf16x8>(vfa + (k >> 1) * 8192 + (k & 1) * 2048), v1 = ldsr<bf16x8>(vfa + (k >> 1) * 8192 + (k & 1) * 2048 + 1024);
      UNR for (int ft = 0; ft < 2; ft++) { const bf16x8 af = wfrag(tra + (k >> 1) * 16384 + (k & 1) * 2048 + ft * 8, 1024);
        kv[ft][0] = mfma(af, v0, kv[ft][0]); kv[ft][1] = mfma(af, v1, kv[ft][1]); z[ft] = mfma(af, ones, z[ft]); } }
    const rsrc_t rk = mkrsrc(ws + WS_KV1 + (size_t)(B.tn * 4 + lw) * 32768, 32768), rz = mkrsrc(ws + WS_ZP + (size_t)(B.tn * 4 + lw) * 2048, 2048), r2 = mkrsrc(ws + WS_KV2 + (size_t)B.tn * 131072, 131072);
    UNR for (int c = 0; c < 2; c++) st_dev(pack8u(kv[0][c], kv[1][c]), rk, lane * 16, B.tm * 2048 + c * 1024);
    { const bool o = lane & 1; const f32x4 zs = {o ? z[1][0] : z[0][0], o ? z[1][1] : z[0][1], o ? z[1][2] : z[0][2], o ? z[1][3] : z[0][3]};
      if ((lane & 14) == 0) st_dev(__builtin_bit_cast(u32x4, zs), rz, (lane & 1) * 64 + g * 16, B.tm * 128); }
    STAMP(1, 16 + lw);
    u32x4 h[2]; poll(h, [&](int i) { return ld_dev(rk, (lane >> 3) * 2048 + (lane & 7) * 16, B.tm * 128 + i * 16384); });
    { float e[4], o[4];
      UNR for (int k = 0; k < 4; k++) { e[k] = fadd(blo(h[0][k]), blo(h[1][k])); o[k] = fadd(bhi(h[0][k]), bhi(h[1][k])); }
      float te = fold4<false>(e[0], e[1], e[2], e[3]), to = fold4<false>(o[0], o[1], o[2], o[3]); te += dpp<0x128>(te); to += dpp<0x128>(to);
      spread4(__uint_as_float(cvtpk(te, to)), e[0], e[1], e[2], e[3]);
      const u32x4 sum = {__float_as_uint(e[0]), __float_as_uint(e[1]), __float_as_uint(e[2]), __float_as_uint(e[3])};
      UNR for (int i = 0; i < 2; i++) st_dev(sum, r2, (lane >> 3) * 8192 + (lane & 7) * 16, i * 65536 + lw * 2048 + B.tm * 128); }
    STAMP(1, 20 + lw);
    poll(h, [&](int i) { return ld_dev(rz, r * 128 + g * 16, i * 64); });
    ldsw<bf16x8>(lds0 + L_KX + lw * 1024 + lane * 16, pack8(row16_sum4(asf(h[0])), row16_sum4(asf(h[1]))));
    lbar();
    __builtin_amdgcn_s_setprio((EV & 8) ? 3 : 0);
  }
  const Blk& B = B2; const int odd = B.tn & 1, q4 = (B.tm >> 3) + 2 * ((B.tm >> 2) & 1);
  Loader<1> G3;
  {
    G2.issueB<false>(2); G2.issueB<false>(3);
    panel_sync(ws, 0, B1, B, lw, lane, [&]() {});
    STAMP(2, 4 + lw);
    G3.init(ws + WS_Y2, 16, A.w1, 0, B.tn * 224 + odd * 32, B.tn * 224 + (odd ? 0 : 192), q4 * 4, B.tm, lw, lane, lds0);
    G2.run<true, 7>([&](int) { return 0; }, [&]() { G3.issueB<false>(0); });
  }
  Loader<3> G4;
  {
    panel_sync(ws, 1, B, B, lw, lane, [&]() { G3.issueB<false>(1); G3.issueB<false>(2); });
    STAMP(3, 4 + lw);
    const char* uvb = ws + WS_UV2 + (size_t)B.tn * 16 * 2048; const rsrc_t ruv = mkrsrc(uvb, 16 * 2048);
    UV<1, 4> U; Stats<32> S; const int tu = B.tm & 3, t3 = 5 + (B.j & 7);
    G3.run<true, 8>([&](int t) { int e = 0;
        if (t == tu) { int ln = lane; pin(ln); UV<1, 4>::publish(ruv, B.tm, opq(lw), ln, lds0, tu == 3 ? 0 : tu); e = 4; }
        if (t == t3 && !(EV & 4)) { touch(opq((const char*)A.w2) + (size_t)(blockIdx.x * 4 + lw) * 7168, min(lane, 55) * 128, lds0 + L_DUM); e += 1; }
        return e; },
      [&]() { U.fetch(uvb, lw, lane); });
    S.fetch(ws + WS_ST2, B.tm, lw, lane);
    G4.init(ws + WS_H1, 56, A.w2, (B.tn >> 3) * 28, (B.tn & 7) * 128, 0, (B.rot * 28) >> 4, B.tm, lw, lane, lds0);
    G4.issueB<false>(0); G4.issueB<false>(1);
    __builtin_amdgcn_s_setprio(3);
    waitvm<24>(); U.finish(ruv, lw, lane, lds0 + L_UVT); waitvm<8>(); S.finish(lw, lane, lds0 + L_STT);
    __builtin_amdgcn_s_setprio((EV & 8) ? 3 : 0);
    lbar();
  }
  {
    bar();
    G4.issueB<false>(2); G4.issueB<false>(3);
    panel_sync(ws, 2, B, B, lw, lane, [&]() {});
    STAMP(4, 4 + lw);
    G4.run<true, 0>([&](int) { return 0; }, [&]() {});
  }
}

DI void compute_role(const Args& A, int w, unsigned lds0) {
  char* ws = A.ws; const Blk B = blk(); const f32x4 zero = {0, 0, 0, 0}; const u32x4 mark = {~0u, ~0u, ~0u, ~0u};
  const int wm = w >> 1, wn = w & 1; int lane = threadIdx.x & 63;
  {
    const Blk B = blk1(); const int g = lane >> 4, r = lane & 15, q = r >> 2, p = lane & 3;
    if (w == 0) put_et(lds0, 0, lane, ((const float*)(ws + WS_S1))[B.rot * 64 + lane]);
    if (w == 1) put_et(lds0, 1, modcol(lane), modvec(ws + WS_SAP + B.rot * 4096, lane));
    f32x4 acc[4][6];
    compute_loop<0>(acc, lds0, wm, wn, lane, ws);
    STAMP(1, 8 + w);
    bar();
    STAMP(2, 20 + w);
    constexpr float L2E = 1.4426950408889634f;
    bf16x8 wf[2]; RowSt st[4];
    UNR for (int m = 0; m < 4; m++) st[m].set(lds0 + L_STT + (wm * 64 + m * 16 + r) * 4, L2E);
    const unsigned uva = lds0 + L_UVT + wn * 256;
    bf16x8 kf[4];
    { const f32x4 u0 = ldsr<f32x4>(uva + 512 + g * 16), v0 = ldsr<f32x4>(uva + 576 + g * 16) * L2E, u1 = ldsr<f32x4>(uva + 640 + g * 16), v1 = ldsr<f32x4>(uva + 704 + g * 16) * L2E;
      UNR for (int c = 0; c < 2; c++) wf[c] = wfrag(lds0 + L_WPHI + wn * 2048 + (4 * g + q) * 64 + c * 32 + p * 8, 1024);
      UNR for (int mm = 0; mm < 4; mm++) { const int m = mm ^ 2; kf[m] = pack8(lnf(acc[m][2], st[m], u0, v0), lnf(acc[m][3], st[m], u1, v1));
        if (m >= 2) ldsw<bf16x8>(lds0 + L_KX + w * 2048 + (m - 2) * 1024 + lane * 16, kf[m]);
        if (m == 3) { lbar(); asm volatile("" : "+v"(acc[0][2]), "+v"(acc[0][3]), "+v"(acc[1][2]), "+v"(acc[1][3])); } } }
    { const unsigned trw = lds0 + L_TR + w * 8192 + r * 64 + ((g ^ ((r >> 1) & 3)) * 16); bf16x8 a, b, c, d; feat2(wf, kf[0], kf[1], a, b, c, d);
      ldsw<bf16x8>(trw, a); ldsw<bf16x8>(trw + 4096, b); ldsw<bf16x8>(trw + 1024, c); ldsw<bf16x8>(trw + 1024 + 4096, d); }
    { float u[2], v[2];
      UNR for (int c = 0; c < 2; c++) { u[c] = ldsr<float>(uva + 1024 + c * 128 + r * 4); v[c] = ldsr<float>(uva + 1088 + c * 128 + r * 4); asm("" : "+v"(u[c])); asm("" : "+v"(v[c])); }
      UNR for (int m = 0; m < 4; m++) { const unsigned a = lds0 + L_STT + (wm * 64 + m * 16 + 4 * g) * 4; const f32x4 rs = ldsr<f32x4>(a), nm = ldsr<f32x4>(a + 512);
        UNR for (int c = 0; c < 2; c++) acc[m][4 + c] = fma4(acc[m][4 + c], rs, fma4(nm, spl(u[c]), spl(v[c]))); }
      UNR for (int uu = 0; uu < 2; uu++) UNR for (int c = 0; c < 2; c++) ldsw<bf16x8>(lds0 + L_VF + w * 4096 + uu * 2048 + c * 1024 + lane * 16, pack8(acc[2 * uu][4 + c], acc[2 * uu + 1][4 + c])); }
    lbar();
    STAMP(1, 12 + w);
    asm volatile("" : "+v"(acc[0][0]), "+v"(acc[0][1]), "+v"(acc[1][0]), "+v"(acc[1][1]), "+v"(acc[2][0]), "+v"(acc[2][1]), "+v"(acc[3][0]), "+v"(acc[3][1]));
    bf16x8 qp[4], qn[4];
    { const f32x4 u0 = ldsr<f32x4>(uva + g * 16), v0 = ldsr<f32x4>(uva + 64 + g * 16) * L2E, u1 = ldsr<f32x4>(uva + 128 + g * 16), v1 = ldsr<f32x4>(uva + 192 + g * 16) * L2E;
      UNR for (int m = 0; m < 4; m += 2) feat2(wf, pack8(lnf(acc[m][0], st[m], u0, v0), lnf(acc[m][1], st[m], u1, v1)),
                                               pack8(lnf(acc[m + 1][0], st[m + 1], u0, v0), lnf(acc[m + 1][1], st[m + 1], u1, v1)), qp[m], qn[m], qp[m + 1], qn[m + 1]); }
    asm volatile("" : "+v"(qp[0]), "+v"(qp[1]), "+v"(qp[2]), "+v"(qp[3]), "+v"(qn[0]), "+v"(qn[1]), "+v"(qn[2]), "+v"(qn[3]) :: "memory");
    STAMP(1, 24 + w);
    bar();
    STAMP(2, 24 + w);
    const rsrc_t r2 = mkrsrc(ws + WS_KV2 + (size_t)(B.tn * 16 + B.tm) * 8192 + wn * 4096, 4096);
    u32x4 kk[4]; UNR for (int i = 0; i < 4; i++) kk[i] = ld_dev(r2, lane * 16, i * 1024);
    const bf16x8 z0 = ldsr<bf16x8>(lds0 + L_KX + wn * 2048 + lane * 16), z1 = ldsr<bf16x8>(lds0 + L_KX + wn * 2048 + 1024 + lane * 16);
    float rd[4];
    UNR for (int m = 0; m < 4; m++) rd[m] = __builtin_amdgcn_rcpf(mfma(z1, qn[m], mfma(z0, qp[m], zero))[0] + 1e-6f);
    asm volatile("" : "+v"(rd[0]), "+v"(rd[1]), "+v"(rd[2]), "+v"(rd[3]));
    if (__any(max(max(umax4(kk[0]), umax4(kk[1])), max(umax4(kk[2]), umax4(kk[3]))) == 0xffffffffu)) poll(kk, [&](int i) { return ld_dev(r2, lane * 16, i * 1024); });
    const rsrc_t ro = mkrsrc(ws + WS_O + (size_t)(B.tm * 16 + B.tn) * 16384, 16384);
    UNR for (int m = 0; m < 4; m++) {
      const f32x4 n0 = mfma(__builtin_bit_cast(bf16x8, kk[2]), qn[m], mfma(__builtin_bit_cast(bf16x8, kk[0]), qp[m], zero)), n1 = mfma(__builtin_bit_cast(bf16x8, kk[3]), qn[m], mfma(__builtin_bit_cast(bf16x8, kk[1]), qp[m], zero));
      st_dev(pack8u(n0 * rd[m], n1 * rd[m]), ro, lane * 16, (wm * 8 + wn) * 1024 + m * 2048);
    }
    STAMP(1, 28 + w);
    waitvm<0>(); bar();
    STAMP(2, w);
  }
  pin(lane);
  u32x4 o[4];
  {
    const int g = lane >> 4, r = lane & 15;
    { const float v = modvec(ws + WS_MODP + (size_t)((w == 0 ? 0 : w == 3 ? 1 : 2) * 16 + (w < 2 ? B.tn : ((B.tm >> 3) + 2 * ((B.tm >> 2) & 1)) * 4 + (B.tm & 3))) * 4096, lane), s = (w == 1 || w == 2) ? rbf(1.f + v) : v;
      if (w < 2) ldsw<float>(lds0 + L_GT + w * 256 + modcol(lane) * 4, s); else put_et(lds0, w - 2, modcol(lane), s); }
    { const rsrc_t rx = mkrsrc(ws + WS_XCH + (size_t)(B.tm * 16 + B.tn) * 16384, 16384);
      UNR for (int i = 0; i < 4; i++) st_dev(mark, rx, lane * 16, w * 4096 + i * 1024); }
    f32x4 acc[4][2];
    bar();
    compute_loop<2>(acc, lds0, wm, wn, lane, ws);
    STAMP(2, 8 + w);
    f32x4 gate[2], sc[2]; u32x2 xr[4][2];
    UNR for (int n = 0; n < 2; n++) { gate[n] = ldsr<f32x4>(lds0 + L_GT + wn * 128 + n * 64 + g * 16); sc[n] = ldsr<f32x4>(lds0 + L_GT + 256 + wn * 128 + n * 64 + g * 16); }
    UNR for (int m = 0; m < 4; m++) UNR for (int n = 0; n < 2; n++) xr[m][n] = ldsr<u32x2>(lds0 + L_RES + (wm * 64 + m * 16 + r) * 128 + (((wn * 4 + n * 2 + (g >> 1)) ^ (r >> 1)) * 16) + (g & 1) * 8);
    const size_t tb = (size_t)(B.tm * 16 + B.tn) * 16384; const rsrc_t ry = mkrsrc(ws + WS_Y2 + tb, 16384), rst = mkrsrc(ws + WS_ST2 + (size_t)(B.tm * 32 + B.tn * 2 + wn) * 1024, 1024);
    f32x4 v[4][2]; float mean[4], m2[4];
    UNR for (int m = 0; m < 4; m++) {
      o[m] = pack8u(fma4(gate[0], acc[m][0], unp(xr[m][0][0], xr[m][0][1])), fma4(gate[1], acc[m][1], unp(xr[m][1][0], xr[m][1][1]))); v[m][0] = unp(o[m][0], o[m][1]); v[m][1] = unp(o[m][2], o[m][3]);
      st_dev(pack8u(v[m][0] * sc[0], v[m][1] * sc[1]), ry, lane * 16, (wm * 8 + wn) * 1024 + m * 2048); }
    UNR for (int m = 0; m < 4; m++) mean[m] = hsum4(v[m][0] + v[m][1]);
    const float mf = fold4<false>(mean[0], mean[1], mean[2], mean[3]) * (1.f / 32); spread4(mf, mean[0], mean[1], mean[2], mean[3]);
    UNR for (int m = 0; m < 4; m++) { const f32x4 d0 = v[m][0] - mean[m], d1 = v[m][1] - mean[m]; m2[m] = hsum4(fma4(d0, d0, d1 * d1)); }
    st_dev8(__builtin_bit_cast(u32x2, f32x2{mf, fold4<false>(m2[0], m2[1], m2[2], m2[3])}), rst, (wm * 64 + ((g >> 1) + 2 * (g & 1)) * 16 + r) * 8, 0);
    STAMP(2, 16 + w);
    waitvm<0>(); bar();
    STAMP(3, w);
  }
  pin(lane);
  {
    const int g = lane >> 4, r = lane & 15, odd = B.tn & 1;
    const int fp0 = B.tn * 7 + odd + wn * 3, fph = odd ? B.tn * 7 : B.tn * 7 + 6;
    if (w < 2) ldsw<float>(lds0 + L_GT + w * 256 + modcol(lane) * 4, modvec(ws + WS_MODP + (size_t)(48 + (B.tn & 7) * 2 + w) * 4096, lane));
    u32x2 bb[7];
    UNR for (int n = 0; n < 7; n++) bb[n] = *(const u32x2*)(A.b1 + (n < 6 ? fp0 * 32 + n * 16 : fph * 32 + wn * 16) + 4 * g);
    pin(bb);
    f32x4 acc[4][7];
    bar();
    { const rsrc_t rx1 = mkrsrc(ws + WS_X1 + (size_t)(B.tm * 16 + B.tn) * 16384, 16384);
      UNR for (int m = 0; m < 4; m++) st_dev(o[m], rx1, lane * 16, (wm * 8 + wn) * 1024 + m * 2048); }
    compute_loop<1>(acc, lds0, wm, wn, lane, ws);
    STAMP(3, 8 + w);
    bar();
    STAMP(3, 12 + w);
    f32x4 u[7], v[7];
    UNR for (int n = 0; n < 7; n++) { const unsigned a = lds0 + L_UVT + (n < 6 ? wn * 6 + n : 12 + wn) * 128 + g * 16; u[n] = ldsr<f32x4>(a); v[n] = ldsr<f32x4>(a + 64) + unp(bb[n][0], bb[n][1]); }
    const rsrc_t rh = mkrsrc(ws + WS_H1 + (size_t)B.tm * 56 * 16384, 56 * 16384);
    RowSt st[4]; u32x2 h6[4];
    UNR for (int m = 0; m < 4; m++) st[m].set(lds0 + L_STT + (wm * 64 + m * 16 + r) * 4, 1.f);
    auto act = [&](int m, int n) { const f32x4 t = __builtin_elementwise_max(lnf(acc[m][n], st[m], u[n], v[n]), zero); return t * t; };
    UNR for (int n = 0; n < 6; n += 2) { const int fp = fp0 + (n >> 1);
      UNR for (int m = 0; m < 4; m++) st_dev(pack8u(act(m, n), act(m, n + 1)), rh, lane * 16, (fp >> 1) * 16384 + (fp & 1) * 1024 + wm * 8192 + m * 2048); }
    UNR for (int m = 0; m < 4; m++) { const f32x4 t = act(m, 6); h6[m] = u32x2{cvtpk(t[0], t[1]), cvtpk(t[2], t[3])}; }
    UNR for (int m = 0; m < 4; m += 2) { const auto a = __builtin_amdgcn_permlane16_swap(h6[m][0], h6[m + 1][0], false, false), b = __builtin_amdgcn_permlane16_swap(h6[m][1], h6[m + 1][1], false, false);
      st_dev(u32x4{a[0], b[0], a[1], b[1]}, rh, (g & 1) * 2048 + (g >> 1) * 256 + r * 16, (fph >> 1) * 16384 + (fph & 1) * 1024 + wm * 8192 + m * 2048 + wn * 512); }
    STAMP(3, 16 + w);
    bar(); waitvm<0>(); bar();
    STAMP(4, w);
  }
  pin(lane);
  {
    const int g = lane >> 4, r = lane & 15, kh = B.tn >> 3, cg = (B.tn & 7) * 2 + wn;
    u32x2 bb[4]; u32x4 xr[2][2]; const rsrc_t rx1 = mkrsrc(ws + WS_X1 + (size_t)(B.tm * 16 + cg) * 16384, 16384);
    UNR for (int n = 0; n < 4; n++) bb[n] = *(const u32x2*)(A.b2 + cg * 64 + n * 16 + 4 * g);
    pin(bb);
    f32x4 acc[4][4];
    bar();
    UNR for (int mm = 0; mm < 2; mm++) UNR for (int ks = 0; ks < 2; ks++) xr[mm][ks] = ld_dev(rx1, lane * 16, ((wm * 4 + kh * 2 + mm) * 2 + ks) * 1024);
    compute_loop<3>(acc, lds0, wm, wn, lane, ws);
    STAMP(4, 8 + w);
    pin(xr[0]); pin(xr[1]);
    const rsrc_t rx = mkrsrc(ws + WS_XCH + (size_t)B.tm * 16 * 16384, 16 * 16384);
    f32x4 keep[2][4], gate[4];
    if (kh) { UNR for (int i = 0; i < 4; i++) st_dev(pack8u(acc[i >> 1][(i & 1) * 2], acc[i >> 1][(i & 1) * 2 + 1]), rx, lane * 16, B.tn * 16384 + w * 4096 + i * 1024);
              UNR for (int n = 0; n < 4; n++) { keep[0][n] = acc[2][n]; keep[1][n] = acc[3][n]; } }
    else    { UNR for (int i = 0; i < 4; i++) st_dev(pack8u(acc[2 + (i >> 1)][(i & 1) * 2], acc[2 + (i >> 1)][(i & 1) * 2 + 1]), rx, lane * 16, B.tn * 16384 + w * 4096 + i * 1024);
              UNR for (int n = 0; n < 4; n++) { keep[0][n] = acc[0][n]; keep[1][n] = acc[1][n]; } }
    UNR for (int n = 0; n < 4; n++) gate[n] = ldsr<f32x4>(lds0 + L_GT + wn * 256 + n * 64 + g * 16);
    UNR for (int mm = 0; mm < 2; mm++) UNR for (int n = 0; n < 4; n++) keep[mm][n] = fma4(gate[n], keep[mm][n] + unp(bb[n][0], bb[n][1]), unp(xr[mm][n >> 1][(n & 1) * 2], xr[mm][n >> 1][(n & 1) * 2 + 1]));
    pin(keep[0]); pin(keep[1]);
    u32x4 pp[4]; poll(pp, [&](int i) { return ld_dev(rx, lane * 16, (B.tn ^ 8) * 16384 + w * 4096 + i * 1024); });
    STAMP(4, 12 + w);
    const unsigned ot = lds0 + L_OT + w * 4096;
    UNR for (int mm = 0; mm < 2; mm++) UNR for (int n = 0; n < 4; n++) { const u32x4 t = pp[mm * 2 + (n >> 1)]; const f32x4 y = fma4(gate[n], unp(t[(n & 1) * 2], t[(n & 1) * 2 + 1]), keep[mm][n]);
      ldsw<u32x2>(ot + (mm * 16 + r) * 128 + (((n * 2 + (g >> 1)) ^ (r >> 1)) * 16) + (g & 1) * 8, u32x2{cvtpk(y[0], y[1]), cvtpk(y[2], y[3])}); }
    lwait();
    const int r8 = lane >> 3; const unsigned rv0 = r8 * 2048 + (((lane & 7) ^ (r8 >> 1)) * 16);
    char* op = (char*)A.out + ((size_t)(B.tm * 128 + wm * 64 + kh * 32) * D_ + cg * 64) * 2;
    UNR for (int i = 0; i < 4; i++) *(u32x4*)(op + i * 16384 + ((i & 1) ? rv0 ^ 64 : rv0)) = ldsr<u32x4>(ot + i * 1024 + lane * 16);
    STAMP(4, 16 + w);
  }
}

__global__ __launch_bounds__(512, 2) void k1_main(Args A) {
  __shared__ __attribute__((aligned(1024))) char smem[L_TOT];
  const int w = __builtin_amdgcn_readfirstlane(threadIdx.x >> 6);
  const unsigned lds0 = (unsigned)(size_t)(LAS char*)smem;
  if (w >= 4) loader_role(A, w - 4, lds0); else compute_role(A, w, lds0);
}

extern "C" void kernel_launch(void* const* d_in, const int* in_sizes, int n_in, void* d_out, int out_size, void* d_ws, size_t ws_size, hipStream_t stream) {
  Args A; A.x = (const u16*)d_in[0]; A.wqkv = (const u16*)d_in[1]; A.wphi = (const u16*)d_in[2]; A.wo = (const u16*)d_in[3]; A.w1 = (const u16*)d_in[5]; A.b1 = (const u16*)d_in[6];
  A.w2 = (const u16*)d_in[7]; A.b2 = (const u16*)d_in[8]; A.wm = (const u16*)d_in[10]; A.out = (u16*)d_out; A.ws = (char*)d_ws;
  k0_mod<<<256, 512, 0, stream>>>(A.x, (const u16*)d_in[9], (const u16*)d_in[10], A.wqkv, A.ws);
  k1_main<<<256, 512, 0, stream>>>(A);
}
```

```cpp
#include <hip/hip_runtime.h>
#include <hip/hip_bf16.h>

typedef unsigned short u16;
typedef unsigned u32x4 __attribute__((ext_vector_type(4)));
typedef unsigned u32x2 __attribute__((ext_vector_type(2)));
typedef short s16x4 __attribute__((ext_vector_type(4)));
typedef __bf16 bf16x8 __attribute__((ext_vector_type(8)));
typedef __bf16 bf16x2 __attribute__((ext_vector_type(2)));
typedef float f32x2 __attribute__((ext_vector_type(2)));
typedef float f32x4 __attribute__((ext_vector_type(4)));
#define LAS __attribute__((address_space(3)))
#define DI __device__ __forceinline__
#define SB __builtin_amdgcn_sched_barrier(0)
#define UNR _Pragma("unroll")

constexpr int D_ = 1024, F_ = 3584, MODN = 6 * D_;
constexpr size_t WS_EPOCH = 0;
constexpr size_t WS_S1    = 4096;
constexpr size_t WS_FLAG  = 1 << 16;
constexpr size_t WS_ST1   = 1 << 19;
constexpr size_t WS_ST2   = WS_ST1 + (1 << 18);
constexpr size_t WS_MODP  = 3 << 19;
constexpr size_t WS_UV1   = WS_MODP + 5 * 65536;
constexpr size_t WS_ZP    = WS_UV1 + 16 * 12 * 2048;
constexpr size_t WS_UV2   = WS_ZP + (1 << 17);
constexpr size_t WS_KV1   = 10 << 20;
constexpr size_t WS_KV2   = WS_KV1 + (2 << 20);
constexpr size_t WS_O     = 14 << 20;
constexpr size_t WS_X1    = 18 << 20;
constexpr size_t WS_H1    = 22 << 20;
constexpr size_t WS_STAMP = 40 << 20;
constexpr int SPIN_CAP = 1 << 14;
typedef unsigned long long u64;

#define STAMP(i) do { } while (0)

DI void glds16(const void* sbase, unsigned voff, unsigned ldsaddr) {
  asm volatile("s_mov_b32 m0, %2\n\ts_nop 0\n\tglobal_load_lds_dwordx4 %0, %1" :: "v"(voff), "s"(sbase), "s"(ldsaddr) : "memory");
}
DI void glds16nt(const void* sbase, unsigned voff, unsigned ldsaddr) {
  asm volatile("s_mov_b32 m0, %2\n\ts_nop 0\n\tglobal_load_lds_dwordx4 %0, %1 nt" :: "v"(voff), "s"(sbase), "s"(ldsaddr) : "memory");
}
DI void glds16x4(const void* sbase, unsigned voff, unsigned ldsaddr) {
  asm volatile("s_mov_b32 m0, %2\n\ts_nop 0\n\tglobal_load_lds_dwordx4 %0, %1 sc1\n\tglobal_load_lds_dwordx4 %0, %1 offset:1024 sc1\n\t"
               "global_load_lds_dwordx4 %0, %1 offset:2048 sc1\n\tglobal_load_lds_dwordx4 %0, %1 offset:3072 sc1" :: "v"(voff), "s"(sbase), "s"(ldsaddr) : "memory");
}
DI void touch(const void* sbase, unsigned voff, unsigned ldsdummy) {
  asm volatile("s_mov_b32 m0, %2\n\ts_nop 0\n\tglobal_load_lds_dword %0, %1" :: "v"(voff), "s"(sbase), "s"(ldsdummy) : "memory");
}
DI void touch_any(const void* p, unsigned ldsdummy) {
  asm volatile("s_mov_b32 m0, %1\n\ts_nop 0\n\tglobal_load_lds_dword %0, off" :: "v"(p), "s"(ldsdummy) : "memory");
}
DI void touch_nt(const void* sbase, unsigned voff, unsigned ldsdummy) {
  asm volatile("s_mov_b32 m0, %2\n\ts_nop 0\n\tglobal_load_lds_dword %0, %1 nt" :: "v"(voff), "s"(sbase), "s"(ldsdummy) : "memory");
}
DI void gld(u32x4& r, const void* sb, unsigned vo) { asm volatile("global_load_dwordx4 %0, %1, %2" : "=v"(r) : "v"(vo), "s"(sb) : "memory"); }
DI void gld_nt(u32x4& r, const void* sb, unsigned vo) { asm volatile("global_load_dwordx4 %0, %1, %2 nt" : "=v"(r) : "v"(vo), "s"(sb) : "memory"); }
DI void gld_dev(u32x4& r, const void* sb, unsigned vo) { asm volatile("global_load_dwordx4 %0, %1, %2 sc1" : "=v"(r) : "v"(vo), "s"(sb) : "memory"); }
DI void gld8(f32x2& r, const void* sb, unsigned vo) { asm volatile("global_load_dwordx2 %0, %1, %2 sc1" : "=v"(r) : "v"(vo), "s"(sb) : "memory"); }
DI void gld4_dev(unsigned& r, const void* sb, unsigned vo) { asm volatile("global_load_dword %0, %1, %2 sc1" : "=v"(r) : "v"(vo), "s"(sb) : "memory"); }
DI void gld4(unsigned& r, const void* sb, unsigned vo) { asm volatile("global_load_dword %0, %1, %2 nt" : "=v"(r) : "v"(vo), "s"(sb) : "memory"); }
template <class T> DI void pin(T& r) { asm volatile("" : "+v"(r)); }
template <class T, int N> DI void pin(T (&r)[N]) { UNR for (int i = 0; i < N; i++) pin(r[i]); }
template <class T> DI T opq(T v) { asm volatile("" : "+s"(v)); return v; }
DI f32x4 asf(u32x4 v) { return __builtin_bit_cast(f32x4, v); }
template <int N> DI void waitvm() { asm volatile("s_waitcnt vmcnt(%0)" :: "i"(N) : "memory"); }
template <int B, int LO = 0, int HI = 12> DI void waitx(int e) {
  if constexpr (LO == HI) waitvm<B + LO>();
  else if constexpr (LO == 0) { if (e == 0) waitvm<B>(); else waitx<B, 1, HI>(e); }
  else { constexpr int MID = (LO + HI + 1) / 2; if (e >= MID) waitx<B, MID, HI>(e); else waitx<B, LO, MID - 1>(e); }
}
DI void waitlgkm0() { __builtin_amdgcn_s_waitcnt(0xC07F); }
DI void lwait() { asm volatile("s_waitcnt lgkmcnt(0)" ::: "memory"); }
DI void bar() { asm volatile("s_barrier" ::: "memory"); }
DI void lbar() { asm volatile("s_waitcnt lgkmcnt(0)\n\ts_barrier" ::: "memory"); }
DI unsigned cvtpk(float a, float b) { f32x2 v = {a, b}; return __builtin_bit_cast(unsigned, __builtin_convertvector(v, bf16x2)); }
DI float rbf(float a) { return __uint_as_float(cvtpk(a, a) << 16); }
DI f32x4 rbf4(f32x4 s) { return f32x4{rbf(s[0]), rbf(s[1]), rbf(s[2]), rbf(s[3])}; }
DI float blo(unsigned u) { return __uint_as_float(u << 16); }
DI float bhi(unsigned u) { return __uint_as_float(u & 0xffff0000u); }
DI f32x4 unp(unsigned a, unsigned b) { return f32x4{blo(a), bhi(a), blo(b), bhi(b)}; }
DI u32x4 pack8u(const f32x4& a, const f32x4& b) { return u32x4{cvtpk(a[0], a[1]), cvtpk(a[2], a[3]), cvtpk(b[0], b[1]), cvtpk(b[2], b[3])}; }
DI bf16x8 pack8(const f32x4& a, const f32x4& b) { return __builtin_bit_cast(bf16x8, pack8u(a, b)); }
DI f32x4 mfma(bf16x8 a, bf16x8 b, f32x4 c) { return __builtin_amdgcn_mfma_f32_16x16x32_bf16(a, b, c, 0, 0, 0); }
DI s16x4 trd(unsigned a) { return __builtin_amdgcn_ds_read_tr16_b64_v4i16((LAS s16x4*)(size_t)a); }
DI bf16x8 cat(s16x4 lo, s16x4 hi) { return __builtin_bit_cast(bf16x8, __builtin_shufflevector(lo, hi, 0, 1, 2, 3, 4, 5, 6, 7)); }
template <class T> DI T ldsr(unsigned a) { return *(const LAS T*)(size_t)a; }
template <class T> DI void ldsw(unsigned a, T v) { *(LAS T*)(size_t)a = v; }
template <int C> DI float dpp(float x) { return __builtin_bit_cast(float, __builtin_amdgcn_update_dpp(0, __builtin_bit_cast(int, x), C, 0xf, 0xf, true)); }
DI float vmax(float a, float b) { float r; asm("v_max_f32_e32 %0, %1, %2" : "=v"(r) : "v"(a), "v"(b)); return r; }
DI float sw32_add(float a, float b) { auto r = __builtin_amdgcn_permlane32_swap(__float_as_uint(a), __float_as_uint(b), false, false); return __uint_as_float(r[0]) + __uint_as_float(r[1]); }
DI float sw16_add(float a, float b) { auto r = __builtin_amdgcn_permlane16_swap(__float_as_uint(a), __float_as_uint(b), false, false); return __uint_as_float(r[0]) + __uint_as_float(r[1]); }
DI float xg_sum(float a) { a = sw32_add(a, a); return sw16_add(a, a); }
DI float xg_max(float a) {
  auto r = __builtin_amdgcn_permlane32_swap(__float_as_uint(a), __float_as_uint(a), false, false); a = vmax(__uint_as_float(r[0]), __uint_as_float(r[1]));
  auto q = __builtin_amdgcn_permlane16_swap(__float_as_uint(a), __float_as_uint(a), false, false); return vmax(__uint_as_float(q[0]), __uint_as_float(q[1]));
}
DI float row16_sum(float a) { a += dpp<0x128>(a); a += dpp<0x124>(a); a += dpp<0x122>(a); a += dpp<0x121>(a); return a; }
DI f32x4 row16_sum4(f32x4 a) { return f32x4{row16_sum(a[0]), row16_sum(a[1]), row16_sum(a[2]), row16_sum(a[3])}; }
DI f32x4 spl(float a) { return f32x4{a, a, a, a}; }
DI f32x4 fma4(f32x4 a, f32x4 b, f32x4 c) { return __builtin_elementwise_fma(a, b, c); }
struct RowSt { float rs, nm; DI void set(unsigned a, float k) { rs = ldsr<float>(a) * k; nm = ldsr<float>(a + 512) * k; asm("" : "+v"(rs)); asm("" : "+v"(nm)); } };
DI f32x4 lnf(f32x4 acc, const RowSt& st, f32x4 u, f32x4 v) { return fma4(acc, spl(st.rs), fma4(u, spl(st.nm), v)); }
DI float fadd(float a, float b) { float r; asm("v_add_f32_e32 %0, %1, %2" : "=v"(r) : "v"(a), "v"(b)); return r; }
DI float hsum4(f32x4 a) { const f32x2 t = f32x2{a[0], a[1]} + f32x2{a[2], a[3]}; float r; asm("v_add_f32_e32 %0, %1, %2" : "=v"(r) : "v"(t[0]), "v"(t[1])); return r; }
struct rsrc_t { __amdgpu_buffer_rsrc_t r; unsigned base; };
DI rsrc_t mkrsrc(const char* ws, size_t off) { return rsrc_t{__builtin_amdgcn_make_buffer_rsrc((void*)ws, 0, 64 << 20, 0x00020000), (unsigned)off}; }
DI rsrc_t outrsrc(const void* out, size_t off) { return rsrc_t{__builtin_amdgcn_make_buffer_rsrc((void*)out, 0, 2048 * D_ * 2, 0x00020000), (unsigned)off}; }
DI unsigned xchoff(int tm, int tn, int w) { return WS_KV1 + (w >> 1) * (2 << 20) + (tn * 16 + tm) * 8192 + (w & 1) * 4096; }
DI u32x4 ld_dev(const rsrc_t& r, unsigned voff, unsigned soff) { return __builtin_amdgcn_raw_buffer_load_b128(r.r, voff, r.base + soff, 16); }
DI void st_dev(u32x4 v, const rsrc_t& r, unsigned voff, unsigned soff) { __builtin_amdgcn_raw_buffer_store_b128(v, r.r, voff + (r.base + soff), 0, 16); }
DI void st_dev8(u32x2 v, const rsrc_t& r, unsigned voff, unsigned soff) { __builtin_amdgcn_raw_buffer_store_b64(v, r.r, voff + (r.base + soff), 0, 16); }
DI unsigned umax4(u32x4 v) { return max(max(v[0], v[1]), max(v[2], v[3])); }
template <int N, class F> DI void poll(u32x4 (&v)[N], F ld) {
  _Pragma("nounroll") for (int spin = 0; spin < SPIN_CAP; spin++) { unsigned m = 0;
    UNR for (int i = 0; i < N; i++) v[i] = ld(i);
    UNR for (int i = 0; i < N; i++) m = max(m, umax4(v[i]));
    if (!__any(m == 0xffffffffu)) break;
    asm volatile("s_sleep 2" ::: "memory");
  }
}
#define EV 16
#define PT 7
#define PG 0
#define K0V 0
DI float fmax3(float a, float b, float c) { return __builtin_elementwise_maximum(__builtin_elementwise_maximum(a, b), c); }
struct Blk { int tm, tn, rot, j, xq; };
DI Blk blk1() { const int xcd = blockIdx.x & 7, j = blockIdx.x >> 3; Blk b; b.tm = j >> 1; b.tn = xcd * 2 + (j & 1); b.rot = b.tm; b.j = j; b.xq = xcd; return b; }
DI Blk blk() { const int xcd = blockIdx.x & 7, j = blockIdx.x >> 3; Blk b; b.tm = (xcd >> 2) * 8 + (j >> 2); b.tn = (xcd & 3) * 4 + (j & 3); b.rot = (b.tm & 7) * 2 + (b.tm >> 3); b.j = j; b.xq = xcd & 3; return b; }
DI unsigned ptile(int c, int lane) { const int row = 4 * (lane >> 4) + ((lane & 15) >> 2); return row * 128 + ((c ^ ((row >> 1) & 3)) * 32) + (lane & 3) * 8; }
DI unsigned htile(int c, int lane) { const int row = 4 * (lane >> 4) + ((lane & 15) >> 2); return row * 64 + ((c ^ ((row >> 2) & 1)) * 32) + (lane & 3) * 8; }
DI bf16x8 wfrag(unsigned a, unsigned ts) { return cat(trd(a), trd(a + ts)); }

DI unsigned silu2(unsigned cw) { const float c0 = blo(cw), c1 = bhi(cw); return cvtpk(c0 * __builtin_amdgcn_rcpf(1.f + __expf(-c0)), c1 * __builtin_amdgcn_rcpf(1.f + __expf(-c1))); }
DI unsigned fragpos(int k) { return (k >> 5) * 64 + ((k >> 2) & 3) * 16 + ((k & 3) + 4 * ((k >> 4) & 1)) * 2; }
DI f32x4 gemv64(unsigned a, bf16x8 s0, bf16x8 s1, int lane) { f32x4 d[4];
  UNR for (int cc = 0; cc < 4; cc++) d[cc] = mfma(wfrag(a + 4096 + ptile(cc, lane), 2048), s1, mfma(wfrag(a + ptile(cc, lane), 2048), s0, f32x4{0, 0, 0, 0}));
  const bool b0 = lane & 1, b1 = lane & 2; f32x4 o;
  UNR for (int k = 0; k < 4; k++) { const float lo = b0 ? d[1][k] : d[0][k], hi = b0 ? d[3][k] : d[2][k]; o[k] = b1 ? hi : lo; }
  return o;
}

template <int CFG> struct Cfg;
template <> struct Cfg<0> { static constexpr int NT = 6, NP = 3, HALF = 0, NB = 2, NS = 3, N = 3 * D_, KT = 16, B0 = 49152, BS0 = 0; };
template <> struct Cfg<1> { static constexpr int NT = 7, NP = 3, HALF = 1, NB = 7, NS = 3, N = F_, KT = 16, B0 = 65536, BS0 = 2; };
template <> struct Cfg<2> { static constexpr int NT = 2, NP = 1, HALF = 0, NB = 2, NS = 4, N = D_, KT = 16, B0 = 73728, BS0 = 0; };
template <> struct Cfg<3> { static constexpr int NT = 4, NP = 2, HALF = 0, NB = 4, NS = 4, N = D_, KT = 28, B0 = 65536, BS0 = 0; };
template <int CFG> struct Lay { using C = Cfg<CFG>; static constexpr int SBB = C::NP * 8192 + C::HALF * 4096, PB = 2 * C::NP + C::HALF, PER = 4 + PB; };
template <int CFG> DI unsigned btile(int tl, int lane) { using C = Cfg<CFG>; return C::B0 + (tl < 4 * C::NP ? (tl >> 2) * 8192 + ptile(tl & 3, lane) : C::NP * 8192 + htile(tl & 1, lane)); }
constexpr int L_RES = 106496, L_WPHI = 155648, L_UVT = L_WPHI + 4096, L_STT = L_UVT + 2048, L_ET = L_STT + 1024, L_GT = L_ET + 256, L_DUM = L_GT + 512, L_TOT = 163840, L_REST = 122880, L_OT = 131072;
constexpr int L_XT = 131072, L_SAU = 147456, L_SIL = L_WPHI, L_RED = L_SIL + 2048, L_SROW = L_RED + 2048, L_SIL2 = L_GT;
constexpr int L_TR = 0, L_VF = 32768, L_KX = 49152;

template <int CFG> DI void compute_loop(f32x4 (&acc)[4][Cfg<CFG>::NT], unsigned lds0, int wm, int wn, int lane, unsigned& ts) {
  using C = Cfg<CFG>; using L = Lay<CFG>; constexpr int MT = 4, NT = C::NT, NB = C::NB, KT = C::KT;
  const unsigned aoff = lds0 + wm * 8192 + lane * 16; unsigned boff[NB];
  UNR for (int i = 0; i < NB; i++) boff[i] = lds0 + btile<CFG>(CFG == 1 ? (i < 6 ? wn * 6 + i : 12 + wn) : wn * NB + i, lane);
  bf16x8 fa[2][MT]; s16x4 fb[2][NT][2]; unsigned va, vb[NB];
  constexpr int NR = MT + 2 * NT, NM = MT * NT, NU = 1 + NB, FREE = NM - NR, UPG = FREE > 0 ? (NU + FREE - 1) / FREE : NU;
  auto upd = [&](int i, unsigned sa, unsigned sb) { if (i == 0) { va = aoff + sa; pin(va); } else { vb[i - 1] = boff[i - 1] + sb; pin(vb[i - 1]); } };
  auto rd1 = [&](int buf, int ks, int i) {
    if (i == 0 || i > 2 * NT) { const int m = i ? i - 2 * NT : 0; fa[buf][m] = ldsr<bf16x8>(va + m * 2048 + ks * 1024); }
    else { const int n = (i - 1) >> 1, t = (i - 1) & 1;
      fb[buf][n][t] = trd(CFG == 0 ? vb[n & 1] + (n >> 1) * 8192 + ks * 4096 + t * 2048 : (CFG == 1 && n == 6) ? vb[NB - 1] + ks * 2048 + t * 1024 : vb[n] + ks * 4096 + t * 2048); }
  };
  auto step = [&](int buf, int ks, bool doread, bool first, bool doupd, unsigned sa, unsigned sb) {
    UNR for (int i = 0; i < NM; i++) {
      const int m = i / NT, n = i % NT; const bf16x8 b = cat(fb[buf][n][0], fb[buf][n][1]);
      const f32x4 cin = first ? f32x4{0, 0, 0, 0} : acc[m][n];
      acc[m][n] = (CFG == 0 && n >= 4) ? mfma(fa[buf][m], b, cin) : mfma(b, fa[buf][m], cin);
      SB;
      if (doread && i < NR) { rd1(buf ^ 1, ks, i); SB; }
      if (doupd && (FREE > 0 ? i >= NR : i == NM - 1)) { const int u0 = FREE > 0 ? (i - NR) * UPG : 0;
        UNR for (int u = u0; u < u0 + UPG; u++) if (u < NU) upd(u, sa, sb);
        SB; }
    }
  };
  UNR for (int i = 0; i < NU; i++) upd(i, 0, C::BS0 * L::SBB);
  SB; lbar();
  STAMP(CFG == 0 ? 12 : CFG == 2 ? 22 : CFG == 1 ? 28 : 36);
  UNR for (int i = 0; i < NR; i++) rd1(0, 0, i);
  SB;
  unsigned sa = 16384, sb = ((C::BS0 + 1) % C::NS) * L::SBB;
  step(0, 1, true, true, true, sa, sb);
  for (int t = 0; t < KT - 1; t++) {
    waitlgkm0(); bar(); SB;
    sa += 16384; if (sa == C::NS * 16384) sa = 0;
    sb += L::SBB; if (sb == C::NS * L::SBB) sb = 0;
    step(1, 0, true, false, false, 0, 0);
    if (FREE >= 4) { waitlgkm0(); SB; }
    step(0, 1, true, false, true, sa, sb);
  }
  step(1, 0, false, false, false, 0, 0);
}

template <int CFG> struct Loader {
  using C = Cfg<CFG>; using L = Lay<CFG>; static constexpr int N = C::N, KT = C::KT, PER = L::PER, PB = L::PB, NS = C::NS;
  const char *ab, *bb, *hb; unsigned av, bv, hv, la, lb, lh; int ka, kb;
  DI void init(const void* A, int akt, const u16* W, int k0, int col0, int hcol, int rot, int tm, int lw, int lane, unsigned lds0) {
    const int r = lane >> 3, hr = lane >> 2;
    ab = (const char*)A + ((size_t)tm * akt + k0) * 16384 + lw * 4096; av = lane * 16;
    bb = (const char*)W + ((size_t)(k0 * 64 + lw * 16) * N + col0) * 2; bv = r * N * 2 + (((lane & 7) ^ (((r >> 1) & 3) << 1)) * 16);
    hb = (const char*)W + ((size_t)(lw * 16) * N + hcol) * 2; hv = hr * N * 2 + (((lane & 3) ^ (((hr >> 2) & 1) << 1)) * 16);
    if (CFG == 3) { const int t = lw & 1; bb -= (size_t)t * 16 * N * 2; hv = bv + (4 * t + 4 * (r >> 2)) * N * 2; bv += t * 16 * N * 2; }
    la = lds0 + lw * 4096; lb = lds0 + C::B0 + lw * 2048; lh = lds0 + C::B0 + C::NP * 8192 + lw * 1024; ka = kb = rot;
  }
  DI void issueA(int slot) { glds16x4(ab + (size_t)ka * 16384, av, la + slot * 16384); ka++; if (ka == KT) ka = 0; }
  template <bool NT> DI void issueB(int slot) { const char* b = bb + (size_t)kb * 64 * N * 2; slot += C::BS0; if (slot >= NS) slot -= NS; const unsigned so = slot * L::SBB;
    const bool nat = CFG == 3 && ((0x1020408u >> kb) & 1);
    const unsigned v0 = nat ? hv : bv, v1 = v0 + (nat ? 16 : 8) * N * 2;
    UNR for (int i = 0; i < 2 * C::NP; i++) { const char* p = b + (i >> 1) * (CFG == 0 ? 2048 : 128) + (CFG == 3 ? 0 : (i & 1) * (8 * N * 2)); const unsigned l = lb + so + (i >> 1) * 8192 + (i & 1) * 1024, vo = CFG == 3 && (i & 1) ? v1 : v0;
      if (NT) glds16nt(p, vo, l); else glds16(p, vo, l); }
    if (C::HALF) { if (NT) glds16nt(hb + (size_t)kb * 64 * N * 2, hv, lh + so); else glds16(hb + (size_t)kb * 64 * N * 2, hv, lh + so); }
    kb++; if (kb == KT) kb = 0;
  }
  DI void issue(int slot) { issueA(slot); issueB<false>(slot); }
  template <int E1, class HF, class HK, class H1> DI void run(HF hf, HK hk, H1 h1) {
    UNR for (int s = 0; s < NS; s++) issueA(s);
    if (NS == 3) {
      waitvm<8>(); bar(); int e0 = hf(0); waitx<4>(e0); lbar();
      UNR for (int t = 1; t <= 5; t++) { issue((t - 1) % 3); int e1 = hf(t); if (t >= 3) e1 += hk(t); waitx<PER>(e0 + e1); lbar(); e0 = e1; }
      int slot = 2;
      _Pragma("nounroll") for (int t = 6; t <= KT - 3; t++) { issue(slot); slot++; if (slot == NS) slot = 0; const int e1 = hk(t); waitx<PER>(e0 + e1); lbar(); e0 = e1; }
    } else {
      waitvm<12>(); bar(); waitvm<8>(); bar();
      issue(0); waitvm<4 + PER>(); bar();
      int slot = 1;
      for (int t = 2; t <= KT - 4; t++) { issue(slot); slot = (slot + 1) & 3; waitvm<2 * PER>(); bar(); }
      waitvm<PER>(); bar();
    }
    h1(); waitvm<E1>(); bar();
  }
};
DI u64* flagp(char* ws, int tile, int line, int word) { return (u64*)(ws + WS_FLAG) + (tile * 6 + line) * 16 + word; }
DI u64 wait_flags(u64* f, u64 tag, u64 need) {
  u64 got = 0;
  _Pragma("nounroll") for (int spin = 0; spin < SPIN_CAP; spin++) {
    got = __ballot(__hip_atomic_load(f, __ATOMIC_RELAXED, __HIP_MEMORY_SCOPE_AGENT) == tag);
    if ((got & need) == need) break;
    asm volatile("s_sleep 1" ::: "memory");
  }
  return got;
}
template <class H> DI bool panel_sync(char* ws, int line, const Blk& P, const Blk& C, int lw, int lane, u64 tag, H pre) {
  bar();
  if (lw == 0) { const int i = lane & 15, grp = lane >> 4;
    if (line == 0) { if (grp < 3) __hip_atomic_store(grp == 0 ? flagp(ws, P.tm * 16 + i, 0, P.tn) : grp == 1 ? flagp(ws, i * 16 + P.tn, 1, P.tm) : flagp(ws, P.tn * 16 + i, 2, P.tm), tag, __ATOMIC_RELAXED, __HIP_MEMORY_SCOPE_AGENT); }
    else if (grp == 0) __hip_atomic_store(flagp(ws, P.tm * 16 + i, line, P.tn), tag, __ATOMIC_RELAXED, __HIP_MEMORY_SCOPE_AGENT); }
  pre();
  bool all = true;
  if (lw == 0) all = wait_flags(flagp(ws, C.tm * 16 + C.tn, line, line == 0 ? min(lane, 47) : lane & 15), tag, 0xffff) == ~0ull;
  bar();
  return all;
}
DI int modcol(int lane) { return 4 * (lane & 15) + 2 * (lane >> 5) + ((lane >> 4) & 1); }
DI float modvec(const char* ws, unsigned part, int lane, u32x4 (&p)[4], bool have) {
  if (!have || __any(max(max(umax4(p[0]), umax4(p[1])), max(umax4(p[2]), umax4(p[3]))) == 0xffffffffu)) { const rsrc_t r = mkrsrc(ws, part); poll(p, [&](int i) { return ld_dev(r, lane * 16, i * 1024); }); }
  const f32x4 a = (asf(p[0]) + asf(p[1])) + (asf(p[2]) + asf(p[3]));
  return rbf(sw16_add(sw32_add(a[0], a[2]), sw32_add(a[1], a[3])));
}
DI float modvec(const char* ws, unsigned part, int lane) { u32x4 p[4]; return modvec(ws, part, lane, p, false); }
DI void put_et(unsigned lds0, int vec, int k, float v) { ldsw<u16>(lds0 + L_ET + vec * 128 + fragpos(k), (u16)(__float_as_uint(v) >> 16)); }

template <int CFG, int NTL> struct UV {
  bf16x8 f[NTL][2]; unsigned ad[NTL], ts[NTL];
  DI void prep(int lw, int lane, unsigned base) { UNR for (int i = 0; i < NTL; i++) { const int tl = min(lw * NTL + i, 2 * Cfg<CFG>::NT - 1); ad[i] = base + btile<CFG>(tl, lane); ts[i] = tl < 4 * Cfg<CFG>::NP ? 2048 : 1024; pin(ad[i]); } }
  DI void read() { UNR for (int i = 0; i < NTL; i++) { f[i][0] = wfrag(ad[i], ts[i]); f[i][1] = wfrag(ad[i] + 2 * ts[i], ts[i]); } }
  DI void publish(const rsrc_t& r, int tm, int lw, int lane, unsigned lds0) {
    const bf16x8 e0 = ldsr<bf16x8>(lds0 + L_ET + (lane & 1) * 128 + (lane >> 4) * 16), e1 = ldsr<bf16x8>(lds0 + L_ET + (lane & 1) * 128 + 64 + (lane >> 4) * 16);
    f32x4 d[NTL];
    UNR for (int i = 0; i < NTL; i++) d[i] = mfma(f[i][1], e1, mfma(f[i][0], e0, f32x4{0, 0, 0, 0}));
    if ((lane & 14) == 0) UNR for (int i = 0; i < NTL; i++) st_dev(__builtin_bit_cast(u32x4, d[i]), r, (lane & 1) * 64 + (lane >> 4) * 16, (lw * NTL + i) * 2048 + tm * 128);
  }
  u32x4 v[2 * NTL];
  DI void fetch(const char* base, int lw, int lane) { UNR for (int i = 0; i < 2 * NTL; i++) gld_dev(v[i], base + lw * NTL * 2048 + i * 1024, lane * 16); }
  DI void finish(const rsrc_t& r, int lw, int lane, unsigned tab) {
    pin(v); unsigned mx = 0;
    UNR for (int i = 0; i < 2 * NTL; i++) mx = max(mx, umax4(v[i]));
    if (__any(mx == 0xffffffffu)) poll(v, [&](int i) { return ld_dev(r, lane * 16, lw * NTL * 2048 + i * 1024); });
    UNR for (int i = 0; i < NTL; i++) { const f32x4 a = asf(v[2 * i]) + asf(v[2 * i + 1]);
      float t = sw16_add(sw32_add(a[0], a[2]), sw32_add(a[1], a[3])); t += dpp<0x128>(t);
      ldsw<float>(tab + (lw * NTL + i) * 128 + (lane & 7) * 16 + (2 * (lane >> 5) + ((lane >> 4) & 1)) * 4, t); }
  }
};
template <int NPART> struct Stats {
  f32x2 v[NPART / 2];
  DI void fetch(const char* st, int tm, int lw, int lane) { UNR for (int i = 0; i < NPART / 2; i++) gld8(v[i], st + (size_t)(tm * NPART + i) * 1024 + lw * 256, (lane >> 5) * (NPART * 512) + (lane & 31) * 8); }
  DI void finish(int lw, int lane, unsigned tab) { pin(v); f32x2 s = v[0]; float q = 0;
    UNR for (int i = 1; i < NPART / 2; i++) s += v[i];
    const float mean = sw32_add(s[0], s[0]) * (1.f / NPART);
    UNR for (int i = 0; i < NPART / 2; i++) { const float d = v[i][0] - mean; q += d * d; }
    q = s[1] + (float)(D_ / NPART) * q;
    const float rstd = __builtin_amdgcn_rsqf(sw32_add(q, q) * (1.f / D_) + 1e-5f);
    ldsw<float>(tab + (lane >> 5) * 512 + (lw * 32 + (lane & 31)) * 4, lane < 32 ? rstd : -mean * rstd); }
};

template <bool MAX> DI float fold4(float a, float b, float c, float d) {
  auto op = [](unsigned x, unsigned y) { return MAX ? __builtin_elementwise_maximum(__uint_as_float(x), __uint_as_float(y)) : __uint_as_float(x) + __uint_as_float(y); };
  const auto r0 = __builtin_amdgcn_permlane32_swap(__float_as_uint(a), __float_as_uint(b), false, false), r1 = __builtin_amdgcn_permlane32_swap(__float_as_uint(c), __float_as_uint(d), false, false);
  const auto r2 = __builtin_amdgcn_permlane16_swap(__float_as_uint(op(r0[0], r0[1])), __float_as_uint(op(r1[0], r1[1])), false, false);
  return op(r2[0], r2[1]);
}
DI void spread4(float t, float& a, float& b, float& c, float& d) {
  const auto r2 = __builtin_amdgcn_permlane16_swap(__float_as_uint(t), __float_as_uint(t), false, false);
  const auto r0 = __builtin_amdgcn_permlane32_swap(r2[0], r2[0], false, false), r1 = __builtin_amdgcn_permlane32_swap(r2[1], r2[1], false, false);
  a = __uint_as_float(r0[0]); b = __uint_as_float(r0[1]); c = __uint_as_float(r1[0]); d = __uint_as_float(r1[1]);
}
DI void feat2(const bf16x8 (&wf)[2], bf16x8 x0, bf16x8 x1, bf16x8& fp0, bf16x8& fn0, bf16x8& fp1, bf16x8& fn1) {
  const f32x4 zero = {0, 0, 0, 0}; f32x4 p[2][2] = {{mfma(wf[0], x0, zero), mfma(wf[1], x0, zero)}, {mfma(wf[0], x1, zero), mfma(wf[1], x1, zero)}};
  float mx[2], nx[2], sp[2], sn[2]; f32x4 a[2][2], b[2][2];
  UNR for (int i = 0; i < 2; i++) { const f32x4 u = p[i][0], v = p[i][1];
    mx[i] = fmax3(fmax3(fmax3(u[0], u[1], u[2]), u[3], v[0]), fmax3(v[1], v[2], v[3]), v[3]); nx[i] = fmax3(fmax3(fmax3(-u[0], -u[1], -u[2]), -u[3], -v[0]), fmax3(-v[1], -v[2], -v[3]), -v[3]); }
  spread4(fold4<true>(mx[0], mx[1], nx[0], nx[1]), mx[0], mx[1], nx[0], nx[1]);
  UNR for (int i = 0; i < 2; i++) UNR for (int h = 0; h < 2; h++) UNR for (int j = 0; j < 4; j += 2) { const f32x2 t = {p[i][h][j], p[i][h][j + 1]};
    f32x2 d = t - f32x2{mx[i], mx[i]}, e = -t - f32x2{nx[i], nx[i]}; asm("" : "+v"(d), "+v"(e));
    a[i][h][j] = __builtin_amdgcn_exp2f(d[0]); a[i][h][j + 1] = __builtin_amdgcn_exp2f(d[1]); b[i][h][j] = __builtin_amdgcn_exp2f(e[0]); b[i][h][j + 1] = __builtin_amdgcn_exp2f(e[1]); }
  UNR for (int i = 0; i < 2; i++) { sp[i] = hsum4(a[i][0] + a[i][1]); sn[i] = hsum4(b[i][0] + b[i][1]); }
  spread4(__builtin_amdgcn_rcpf(fold4<false>(sp[0], sp[1], sn[0], sn[1])), sp[0], sp[1], sn[0], sn[1]);
  fp0 = pack8(a[0][0] * sp[0], a[0][1] * sp[0]); fn0 = pack8(b[0][0] * sn[0], b[0][1] * sn[0]); fp1 = pack8(a[1][0] * sp[1], a[1][1] * sp[1]); fn1 = pack8(b[1][0] * sn[1], b[1][1] * sn[1]);
}

DI void touch_code(int lane, unsigned ldsdummy) {
  const char* pc; unsigned len, back;
  asm volatile("s_getpc_b64 %0\n.Lhere%=:\n\ts_mov_b32 %2, .Lhere%=-.Lcode_begin\n\ts_mov_b32 %1, .Lcode_end-.Lcode_begin" : "=s"(pc), "=s"(len), "=s"(back));
  pc -= back;
  const unsigned o = ((blockIdx.x >> 3) * 16 + (lane & 15)) * 128;
  touch(pc, min(o, len), ldsdummy);
}
struct Args { const u16 *x, *wqkv, *wphi, *wo, *w1, *b1, *w2, *b2, *c, *wm; u16* out; char* ws; };

DI u64 phase0(const Args& A, int w, unsigned lds0, unsigned& ts) {
  char* ws = A.ws; const Blk B = blk1(); const f32x4 zero = {0, 0, 0, 0};
  const int lane = threadIdx.x & 63, r8 = lane >> 3, g = lane >> 4, r = lane & 15;
  unsigned cw, cw2; gld4(cw, A.c, w * 256 + ((B.tm * 16 + lane * 4) & 255)); gld4(cw2, A.c, B.tm * 128 + (lane & 31) * 4);
  const char* wb = (const char*)A.wm + B.tn * 128; const unsigned wv = r8 * MODN * 2 + (((lane & 7) ^ (((r8 >> 1) & 3) << 1)) * 16);
  auto piece = [&](int i) { glds16(wb + (size_t)(w * 16 + ((B.tm + i) & 15)) * 16 * MODN + 2 * D_, wv, lds0 + (w * 16 + i) * 1024); };
  glds16nt(wb + (size_t)(w * 16 + B.tm) * 16 * MODN + 2 * D_, wv, lds0 + w * 16384);
  const unsigned rv0 = r8 * 2048 + (((lane & 7) ^ (r8 >> 1)) * 16);
  UNR for (int i = 0; i < 2; i++) glds16nt((const char*)A.x + ((size_t)(B.tm * 128 + w * 16 + i * 8) * D_ + B.tn * 64) * 2, i ? rv0 ^ 64 : rv0, lds0 + L_XT + (w * 2 + i) * 1024);
  bar();
  piece(1); piece(2);
  if (w == 7 && (PT & 1)) { const Blk C = blk(); if (lane < 6) touch_any(flagp(ws, lane < 3 ? B.tm * 16 + B.tn : C.tm * 16 + C.tn, lane, 0), lds0 + L_DUM); }
  if (w < 3) {
    const int l = w * 8 + r8;
    if (l < 22) st_dev(u32x4{~0u, ~0u, ~0u, ~0u}, mkrsrc(ws, WS_MODP), (l < 10 ? (((l >> 1) * 16 + B.tn) * 16 + B.tm) * 256 + (l & 1) * 128 : 5 * 65536 + (B.tn * 12 + l - 10) * 2048 + B.tm * 128) + (lane & 7) * 16, 0); }
  waitvm<5>(); pin(cw); pin(cw2);
  ldsw<unsigned>(lds0 + L_SIL + w * 256 + fragpos(2 * lane), silu2(cw));
  if (w == 7) ldsw<unsigned>(lds0 + L_SIL2 + fragpos(2 * (lane & 31)), silu2(cw2));
  unsigned pa[4]; UNR for (int cc = 0; cc < 4; cc++) { pa[cc] = lds0 + w * 16384 + ptile(cc, lane); pin(pa[cc]); }
  STAMP(1);
  f32x4 a[4] = {zero, zero, zero, zero};
  auto kstep = [&](int ks) { const bf16x8 sf = ldsr<bf16x8>(lds0 + L_SIL + (w * 4 + ks) * 64 + g * 16);
    UNR for (int cc = 0; cc < 4; cc++) a[cc] = mfma(wfrag(pa[cc] + ks * 4096, 2048), sf, a[cc]); };
  UNR for (int i = 3; i < 10; i++) piece(i);
  waitvm<6>();
  u32x2 xt[4];
  UNR for (int cc = 0; cc < 4; cc++) xt[cc] = ldsr<u32x2>(lds0 + L_XT + (w * 16 + r) * 128 + (((cc * 2 + (g >> 1)) ^ (r >> 1)) * 16) + (g & 1) * 8);
  kstep(0);
  UNR for (int i = 10; i < 16; i++) piece(i);
  STAMP(2);
  bar();
  glds16nt(wb + (size_t)(B.tm * 8 + w) * 16 * MODN, wv, lds0 + L_SAU + w * 1024);
  { const int e = min(w * 24 + lane, 191), row = e / 6, l = e - row * 6;
    if (lane < 24) touch_nt((const char*)A.wqkv + (size_t)B.j * 32 * 6144 + B.xq * 256, row * 6144 + (l >> 1) * 2048 + (l & 1) * 128, lds0 + L_DUM); }
  f32x2 ep; gld8(ep, ws + WS_EPOCH, 0);
  waitvm<11>(); kstep(1); waitvm<7>(); kstep(2); waitvm<3>(); kstep(3);
  if (r == 0) UNR for (int cc = 0; cc < 4; cc++) ldsw<f32x4>(lds0 + L_RED + w * 256 + cc * 64 + g * 16, a[cc]);
  STAMP(3);
  lbar();
  f32x4 xv[4]; UNR for (int cc = 0; cc < 4; cc++) xv[cc] = unp(xt[cc][0], xt[cc][1]);
  float s = 0;
  UNR for (int i = 0; i < 8; i++) s += ldsr<float>(lds0 + L_RED + i * 256 + lane * 4);
  s = rbf(1.f + rbf(s)); ldsw<float>(lds0 + L_SROW + w * 256 + lane * 4, s);
  asm volatile("" ::: "memory");
  const rsrc_t ry = outrsrc(A.out, (B.tm * 16 + B.tn) * 16384);
  UNR for (int ks = 0; ks < 2; ks++)
    st_dev(pack8u(xv[2 * ks] * ldsr<f32x4>(lds0 + L_SROW + w * 256 + ks * 128 + g * 16), xv[2 * ks + 1] * ldsr<f32x4>(lds0 + L_SROW + w * 256 + ks * 128 + 64 + g * 16)), ry, lane * 16, w * 2048 + ks * 1024);
  STAMP(4);
  if (w == 6 && B.tm == B.tn) __hip_atomic_store((float*)(ws + WS_S1) + B.tn * 64 + lane, s, __ATOMIC_RELAXED, __HIP_MEMORY_SCOPE_AGENT);
  { const float mean = xg_sum(hsum4((xv[0] + xv[1]) + (xv[2] + xv[3]))) * (1.f / 64); f32x4 q = zero;
    UNR for (int cc = 0; cc < 4; cc++) { const f32x4 d = xv[cc] - mean; q = fma4(d, d, q); }
    const float m2 = xg_sum(hsum4(q));
    if (g == 0) st_dev8(__builtin_bit_cast(u32x2, f32x2{mean, m2}), mkrsrc(ws, WS_ST1 + (size_t)(B.tm * 16 + B.tn) * 1024), r * 8, w * 128); }
  waitvm<0>(); pin(ep);
  STAMP(5);
  return __builtin_bit_cast(u64, ep) + 0x9E3779B97F4A7C15ull;
}

DI void loader_role(const Args& A, int lw, unsigned lds0, u64 tag, unsigned& ts) {
  char* ws = A.ws; const Blk B2 = blk(), B1 = blk1(); const f32x4 zero = {0, 0, 0, 0}; const u32x4 mark = {~0u, ~0u, ~0u, ~0u};
  int lane = threadIdx.x & 63; pin(lane);
  Loader<2> G2;
  {
    const Blk& B = B1; const int g = lane >> 4, r = lane & 15, q = r >> 2, p = lane & 3, r8 = lane >> 3;
    Loader<0> G; G.init(A.out, 16, A.wqkv, 0, B.tn * 64, 0, B.rot, B.tm, lw, lane, lds0);
    const rsrc_t ruv = mkrsrc(ws, WS_UV1 + B.tn * 12 * 2048);
    UV<0, 3> U; Stats<16> S; U.prep(lw, lane, lds0);
    const bool all0 = panel_sync(ws, 0, B, B, lw, lane, tag, [&]() { UNR for (int s = 0; s < 3; s++) G.issueB<false>(s); });
    STAMP(11);
    const int t1 = 3 + (B.tm * 5 >> 3), t3 = 3 + (((B.tm + 8) & 15) * 5 >> 3);
    const char* sap = ws + WS_MODP + (size_t)(64 + B.rot) * 4096; u32x4 sp[4]; unsigned s1;
    G.run<15>([&](int t) { int e = 0;
        if (t == 0) { U.read(); if (!all0) wait_flags(flagp(ws, B.tm * 16 + B.tn, 1, lane & 31), tag, ~0ull); }
        if (t == 1) { UNR for (int i = 0; i < 4; i++) gld_dev(sp[i], sap + i * 1024, lw == 1 ? lane * 16 : 0); gld4_dev(s1, ws + WS_S1 + B.rot * 256, lane * 4);
          UNR for (int c = 0; c < 2; c++) st_dev(mark, mkrsrc(ws, WS_KV1 + (size_t)(B.tn * 4 + lw) * 32768), lane * 16, B.tm * 2048 + c * 1024);
          e = 7; }
        if (t == 2) { UNR for (int c = 0; c < 2; c++) st_dev(mark, mkrsrc(ws, WS_KV2 + (size_t)(B.tn * 16 + B.tm) * 8192), lane * 16, lw * 2048 + c * 1024);
          if (r8 < 5) st_dev(mark, mkrsrc(ws, WS_ZP), (r8 == 0 ? (B.tn * 4 + lw) * 2048 : (1 << 17) + (B.tn * 16 + lw * 4 + r8 - 1) * 2048) + (lane & 7) * 16, B.tm * 128);
          e = 3; }
        if (t == 3 && PG) { const unsigned i = (B.j * 64 + lane) << PG;
          const char* p = lw == 0 ? ws + WS_KV1 + min(i, 27u << 20) : lw == 1 ? (const char*)A.w1 + min(i, D_ * F_ * 2u - 4) : lw == 2 ? (const char*)A.w2 + min(i, D_ * F_ * 2u - 4) : i < (2u << 20) ? (const char*)A.wo + i : ws + min(i - (2u << 20), 3u << 20);
          touch_any(p, lds0 + L_DUM); e = 1; }
        if (t == 4) { pin(sp); pin(s1); if (lw == 0) put_et(lds0, 0, lane, __uint_as_float(s1)); if (lw == 1) put_et(lds0, 1, modcol(lane), modvec(ws, WS_MODP + (64 + B.rot) * 4096, lane, sp, true)); }
        if (t == 5) { U.publish(ruv, B.tm, lw, lane, lds0); e = 3; }
        return e; },
      [&](int t) { int e = 0;
        if (t == t1 && !(EV & 1)) {
          const char* wb = opq((const char*)A.wm) + (size_t)B.tm * 128 * MODN + (lw + 2) * 2 * D_ + B.tn * 128; const unsigned wv = r8 * MODN * 2 + (((lane & 7) ^ (((r8 >> 1) & 3) << 1)) * 16);
          const unsigned l0 = opq(lds0 + L_REST) + lw * 8192;
          UNR for (int i = 0; i < 8; i++) glds16nt(wb + i * 16 * MODN, wv, l0 + i * 1024);
          e += 8; }
        if (t == t3 && !(EV & 2)) { touch(opq((const char*)A.w1) + (size_t)(blockIdx.x * 4 + lw) * 7168, min(lane, 55) * 128, lds0 + L_DUM); touch(opq((const char*)A.wo) + (size_t)(blockIdx.x * 4 + lw) * 2048, (lane & 15) * 128, lds0 + L_DUM); e += 2; }
        if ((PT & 4) && t == 11 && lw == 0) { touch_code(lane, lds0 + L_DUM); e += 1; }
        return e; },
      [&]() { U.fetch(opq(ws) + ruv.base, lw, lane); S.fetch(opq(ws) + WS_ST1, B.tm, lw, lane); glds16nt((const char*)A.wphi + B.tn * 4096 + lw * 1024, lane * 16, lds0 + L_WPHI + lw * 1024); });
    G2.init(ws + WS_O, 16, A.wo, 0, B2.tn * 64, 0, B2.rot, B2.tm, lw, lane, lds0);
    { const unsigned rv0 = r8 * 2048 + (((lane & 7) ^ (r8 >> 1)) * 16);
      UNR for (int i = 0; i < 4; i++) glds16nt((const char*)A.x + ((size_t)(B2.tm * 128 + (lw * 4 + i) * 8) * D_ + B2.tn * 64) * 2, (i & 1) ? rv0 ^ 64 : rv0, lds0 + L_RES + (lw * 4 + i) * 1024); }
    G2.issueB<false>(0); G2.issueB<false>(1);
    __builtin_amdgcn_s_setprio(3);
    waitvm<8>(); U.finish(ruv, lw, lane, lds0 + L_UVT); S.finish(lw, lane, lds0 + L_STT);
    __builtin_amdgcn_s_setprio((EV & 8) ? 3 : 0);
    lbar();
    bf16x8 wf[2];
    UNR for (int c = 0; c < 2; c++) wf[c] = wfrag(lds0 + L_WPHI + (lw & 1) * 2048 + (4 * g + q) * 64 + c * 32 + p * 8, 1024);
    bar();
    { const unsigned trw = lds0 + L_TR + lw * 8192 + r * 64 + ((g ^ ((r >> 1) & 3)) * 16); bf16x8 a, b, c, d;
      feat2(wf, ldsr<bf16x8>(lds0 + L_KX + lw * 2048 + lane * 16), ldsr<bf16x8>(lds0 + L_KX + lw * 2048 + 1024 + lane * 16), a, b, c, d);
      ldsw<bf16x8>(trw + 2048, a); ldsw<bf16x8>(trw + 2048 + 4096, b); ldsw<bf16x8>(trw + 3072, c); ldsw<bf16x8>(trw + 3072 + 4096, d); }
    lbar();
    __builtin_amdgcn_s_setprio(3);
    const int hd = lw >> 1, sg = lw & 1;
    const u32x4 one4 = {0x3f803f80u, 0x3f803f80u, 0x3f803f80u, 0x3f803f80u}; const bf16x8 ones = __builtin_bit_cast(bf16x8, one4);
    const unsigned tra = lds0 + L_TR + hd * 8192 + sg * 4096 + (4 * g + q) * 64 + ((p ^ (((4 * g + q) >> 1) & 3)) * 16), vfa = lds0 + L_VF + hd * 4096 + lane * 16;
    f32x4 kv[2][2] = {{zero, zero}, {zero, zero}}, z[2] = {zero, zero};
    UNR for (int k = 0; k < 4; k++) { const bf16x8 v0 = ldsr<bf16x8>(vfa + (k >> 1) * 8192 + (k & 1) * 2048), v1 = ldsr<bf16x8>(vfa + (k >> 1) * 8192 + (k & 1) * 2048 + 1024);
      UNR for (int ft = 0; ft < 2; ft++) { const bf16x8 af = wfrag(tra + (k >> 1) * 16384 + (k & 1) * 2048 + ft * 8, 1024);
        kv[ft][0] = mfma(af, v0, kv[ft][0]); kv[ft][1] = mfma(af, v1, kv[ft][1]); z[ft] = mfma(af, ones, z[ft]); } }
    const rsrc_t rk = mkrsrc(ws, WS_KV1 + (size_t)(B.tn * 4 + lw) * 32768), rz = mkrsrc(ws, WS_ZP + (size_t)(B.tn * 4 + lw) * 2048), r2 = mkrsrc(ws, WS_KV2 + (size_t)B.tn * 131072);
    UNR for (int c = 0; c < 2; c++) st_dev(pack8u(kv[0][c], kv[1][c]), rk, lane * 16, B.tm * 2048 + c * 1024);
    { const bool o = lane & 1; const f32x4 zs = {o ? z[1][0] : z[0][0], o ? z[1][1] : z[0][1], o ? z[1][2] : z[0][2], o ? z[1][3] : z[0][3]};
      if ((lane & 14) == 0) st_dev(__builtin_bit_cast(u32x4, zs), rz, (lane & 1) * 64 + g * 16, B.tm * 128); }
    STAMP(16);
    { const f32x4 d = gemv64(lds0 + L_REST + lw * 8192, ldsr<bf16x8>(lds0 + L_SIL2 + g * 16), ldsr<bf16x8>(lds0 + L_SIL2 + 64 + g * 16), lane);
      if (r < 4) st_dev(__builtin_bit_cast(u32x4, d), mkrsrc(ws, WS_MODP + (size_t)((lw * 16 + B.tn) * 16 + B.tm) * 256), r * 64 + g * 16, 0); }
    u32x4 h[4]; poll(h, [&](int i) { return i < 2 ? ld_dev(rk, (lane >> 3) * 2048 + (lane & 7) * 16, B.tm * 128 + i * 16384) : ld_dev(rz, r * 128 + g * 16, (i - 2) * 64); });
    { float e[4], o[4];
      UNR for (int k = 0; k < 4; k++) { e[k] = fadd(blo(h[0][k]), blo(h[1][k])); o[k] = fadd(bhi(h[0][k]), bhi(h[1][k])); }
      float te = fold4<false>(e[0], e[1], e[2], e[3]), to = fold4<false>(o[0], o[1], o[2], o[3]); te += dpp<0x128>(te); to += dpp<0x128>(to);
      spread4(__uint_as_float(cvtpk(te, to)), e[0], e[1], e[2], e[3]);
      const u32x4 sum = {__float_as_uint(e[0]), __float_as_uint(e[1]), __float_as_uint(e[2]), __float_as_uint(e[3])};
      UNR for (int i = 0; i < 2; i++) st_dev(sum, r2, (lane >> 3) * 8192 + (lane & 7) * 16, i * 65536 + lw * 2048 + B.tm * 128); }
    STAMP(17);
    ldsw<bf16x8>(lds0 + L_KX + lw * 1024 + lane * 16, pack8(row16_sum4(asf(h[2])), row16_sum4(asf(h[3]))));
    lbar();
    __builtin_amdgcn_s_setprio((EV & 8) ? 3 : 0);
  }
  const Blk& B = B2; const int odd = B.tn & 1, q4 = (B.tm >> 3) + 2 * ((B.tm >> 2) & 1);
  Loader<1> G3;
  {
    G2.issueB<false>(2); G2.issueB<false>(3);
    panel_sync(ws, 3, B1, B, lw, lane, tag, [&]() {});
    if (blockIdx.x == 0 && lw == 0 && lane == 0) __hip_atomic_store((u64*)(ws + WS_EPOCH), tag, __ATOMIC_RELAXED, __HIP_MEMORY_SCOPE_AGENT);
    STAMP(21);
    G3.init(A.out, 16, A.w1, 0, B.tn * 224 + odd * 32, B.tn * 224 + (odd ? 0 : 192), q4 * 4, B.tm, lw, lane, lds0);
    G2.run<7>([&](int) { return 0; }, [&](int) { return 0; }, [&]() { G3.issueB<false>(0); });
  }
  Loader<3> G4;
  {
    const rsrc_t ruv = mkrsrc(ws, WS_UV2 + B.tn * 16 * 2048);
    UV<1, 4> U; Stats<32> S; const int tu = B.tm & 3, t3 = 5 + (B.j & 7);
    U.prep(lw, lane, lds0 + (tu == 1 ? 0 : tu == 2 ? 1 : 2) * Lay<1>::SBB);
    panel_sync(ws, 4, B, B, lw, lane, tag, [&]() { G3.issueB<false>(1); G3.issueB<false>(2); });
    STAMP(27);
    auto h1pre = [&](int t) {
      if (EV & 16) { const int fp = B.tn * 7 + (t >> 1); st_dev(mark, mkrsrc(ws, WS_H1 + (B.tm * 56 + (fp >> 1)) * 16384), lane * 16, (fp & 1) * 1024 + (lw * 2 + (t & 1)) * 2048); return 1; } return 0; };
    G3.run<8>([&](int t) { int e = 0;
        if (t == 0 || (t < 4 && t == tu)) U.read();
        if (t == 4) { U.publish(ruv, B.tm, lw, lane, lds0); e = 4; }
        if (t < 3) e += h1pre(t);
        return e; },
      [&](int t) { int e = h1pre(t);
        if (t == t3 && !(EV & 4)) { touch(opq((const char*)A.w2) + (size_t)(blockIdx.x * 4 + lw) * 7168, min(lane, 55) * 128, lds0 + L_DUM); e += 1; }
        if ((PT & 4) && t == 11 && lw == 0) { touch_code(lane, lds0 + L_DUM); e += 1; }
        return e; },
      [&]() { U.fetch(opq(ws) + ruv.base, lw, lane); });
    S.fetch(opq(ws) + WS_ST2, B.tm, lw, lane);
    G4.init(ws + WS_H1, 56, A.w2, (B.tn >> 3) * 28, (B.tn & 7) * 128, 0, (B.rot * 28) >> 4, B.tm, lw, lane, lds0);
    G4.issueB<false>(0); G4.issueB<false>(1);
    __builtin_amdgcn_s_setprio(3);
    waitvm<24>(); U.finish(ruv, lw, lane, lds0 + L_UVT); waitvm<8>(); S.finish(lw, lane, lds0 + L_STT);
    __builtin_amdgcn_s_setprio((EV & 8) ? 3 : 0);
    lbar();
  }
  {
    bar();
    G4.issueB<false>(2); G4.issueB<false>(3);
    panel_sync(ws, 5, B, B, lw, lane, tag, [&]() {});
    STAMP(35);
    G4.run<0>([&](int) { return 0; }, [&](int) { return 0; }, [&]() {});
    asm volatile(".Lcode_end:");
  }
}

DI void compute_role(const Args& A, int w, unsigned lds0, unsigned& ts) {
  char* ws = A.ws; const Blk B = blk(); const f32x4 zero = {0, 0, 0, 0}; const u32x4 mark = {~0u, ~0u, ~0u, ~0u};
  const int wm = w >> 1, wn = w & 1; int lane = threadIdx.x & 63; pin(lane);
  {
    const Blk B = blk1(); const int g = lane >> 4, r = lane & 15, q = r >> 2, p = lane & 3;
    bar();
    if (w == 0) { const f32x4 d = gemv64(lds0 + L_SAU, ldsr<bf16x8>(lds0 + L_SIL2 + g * 16), ldsr<bf16x8>(lds0 + L_SIL2 + 64 + g * 16), lane);
      if (r < 4) st_dev(__builtin_bit_cast(u32x4, d), mkrsrc(ws, WS_MODP + (size_t)((64 + B.tn) * 16 + B.tm) * 256), r * 64 + g * 16, 0); }
    bar();
    f32x4 acc[4][6];
    compute_loop<0>(acc, lds0, wm, wn, lane, ts);
    STAMP(13);
    bar();
    STAMP(14);
    constexpr float L2E = 1.4426950408889634f;
    bf16x8 wf[2]; RowSt st[4];
    UNR for (int m = 0; m < 4; m++) st[m].set(lds0 + L_STT + (wm * 64 + m * 16 + r) * 4, L2E);
    const unsigned uva = lds0 + L_UVT + wn * 256;
    bf16x8 kf[4];
    { const f32x4 u0 = ldsr<f32x4>(uva + 512 + g * 16), v0 = ldsr<f32x4>(uva + 576 + g * 16) * L2E, u1 = ldsr<f32x4>(uva + 640 + g * 16), v1 = ldsr<f32x4>(uva + 704 + g * 16) * L2E;
      UNR for (int c = 0; c < 2; c++) wf[c] = wfrag(lds0 + L_WPHI + wn * 2048 + (4 * g + q) * 64 + c * 32 + p * 8, 1024);
      UNR for (int mm = 0; mm < 4; mm++) { const int m = mm ^ 2; kf[m] = pack8(lnf(acc[m][2], st[m], u0, v0), lnf(acc[m][3], st[m], u1, v1));
        if (m >= 2) ldsw<bf16x8>(lds0 + L_KX + w * 2048 + (m - 2) * 1024 + lane * 16, kf[m]);
        if (m == 3) { lbar(); asm volatile("" : "+v"(acc[0][2]), "+v"(acc[0][3]), "+v"(acc[1][2]), "+v"(acc[1][3])); } } }
    { const unsigned trw = lds0 + L_TR + w * 8192 + r * 64 + ((g ^ ((r >> 1) & 3)) * 16); bf16x8 a, b, c, d; feat2(wf, kf[0], kf[1], a, b, c, d);
      ldsw<bf16x8>(trw, a); ldsw<bf16x8>(trw + 4096, b); ldsw<bf16x8>(trw + 1024, c); ldsw<bf16x8>(trw + 1024 + 4096, d); }
    { float u[2], v[2];
      UNR for (int c = 0; c < 2; c++) { u[c] = ldsr<float>(uva + 1024 + c * 128 + r * 4); v[c] = ldsr<float>(uva + 1088 + c * 128 + r * 4); asm("" : "+v"(u[c])); asm("" : "+v"(v[c])); }
      UNR for (int m = 0; m < 4; m++) { const unsigned a = lds0 + L_STT + (wm * 64 + m * 16 + 4 * g) * 4; const f32x4 rs = ldsr<f32x4>(a), nm = ldsr<f32x4>(a + 512);
        UNR for (int c = 0; c < 2; c++) acc[m][4 + c] = fma4(acc[m][4 + c], rs, fma4(nm, spl(u[c]), spl(v[c]))); }
      UNR for (int uu = 0; uu < 2; uu++) UNR for (int c = 0; c < 2; c++) ldsw<bf16x8>(lds0 + L_VF + w * 4096 + uu * 2048 + c * 1024 + lane * 16, pack8(acc[2 * uu][4 + c], acc[2 * uu + 1][4 + c])); }
    lbar();
    STAMP(15);
    asm volatile("" : "+v"(acc[0][0]), "+v"(acc[0][1]), "+v"(acc[1][0]), "+v"(acc[1][1]), "+v"(acc[2][0]), "+v"(acc[2][1]), "+v"(acc[3][0]), "+v"(acc[3][1]));
    bf16x8 qp[4], qn[4];
    { const f32x4 u0 = ldsr<f32x4>(uva + g * 16), v0 = ldsr<f32x4>(uva + 64 + g * 16) * L2E, u1 = ldsr<f32x4>(uva + 128 + g * 16), v1 = ldsr<f32x4>(uva + 192 + g * 16) * L2E;
      UNR for (int m = 0; m < 4; m += 2) feat2(wf, pack8(lnf(acc[m][0], st[m], u0, v0), lnf(acc[m][1], st[m], u1, v1)),
                                               pack8(lnf(acc[m + 1][0], st[m + 1], u0, v0), lnf(acc[m + 1][1], st[m + 1], u1, v1)), qp[m], qn[m], qp[m + 1], qn[m + 1]); }
    asm volatile("" : "+v"(qp[0]), "+v"(qp[1]), "+v"(qp[2]), "+v"(qp[3]), "+v"(qn[0]), "+v"(qn[1]), "+v"(qn[2]), "+v"(qn[3]) :: "memory");
    STAMP(16);
    bar();
    STAMP(17);
    const rsrc_t r2 = mkrsrc(ws, WS_KV2 + (size_t)(B.tn * 16 + B.tm) * 8192 + wn * 4096);
    u32x4 kk[4]; UNR for (int i = 0; i < 4; i++) kk[i] = ld_dev(r2, lane * 16, i * 1024);
    const bf16x8 z0 = ldsr<bf16x8>(lds0 + L_KX + wn * 2048 + lane * 16), z1 = ldsr<bf16x8>(lds0 + L_KX + wn * 2048 + 1024 + lane * 16);
    float rd[4];
    UNR for (int m = 0; m < 4; m++) rd[m] = __builtin_amdgcn_rcpf(mfma(z1, qn[m], mfma(z0, qp[m], zero))[0] + 1e-6f);
    asm volatile("" : "+v"(rd[0]), "+v"(rd[1]), "+v"(rd[2]), "+v"(rd[3]));
    if (__any(max(max(umax4(kk[0]), umax4(kk[1])), max(umax4(kk[2]), umax4(kk[3]))) == 0xffffffffu)) poll(kk, [&](int i) { return ld_dev(r2, lane * 16, i * 1024); });
    const rsrc_t ro = mkrsrc(ws, WS_O + (size_t)(B.tm * 16 + B.tn) * 16384);
    UNR for (int m = 0; m < 4; m++) {
      const f32x4 n0 = mfma(__builtin_bit_cast(bf16x8, kk[2]), qn[m], mfma(__builtin_bit_cast(bf16x8, kk[0]), qp[m], zero)), n1 = mfma(__builtin_bit_cast(bf16x8, kk[3]), qn[m], mfma(__builtin_bit_cast(bf16x8, kk[1]), qp[m], zero));
      st_dev(pack8u(n0 * rd[m], n1 * rd[m]), ro, lane * 16, (wm * 8 + wn) * 1024 + m * 2048);
    }
    STAMP(18);
    waitvm<0>(); bar();
    STAMP(19);
  }
  pin(lane);
  u32x4 o[4];
  {
    const int g = lane >> 4, r = lane & 15;
    { const float v = modvec(ws, WS_MODP + (size_t)((w == 0 ? 0 : w == 3 ? 1 : 2) * 16 + (w < 2 ? B.tn : ((B.tm >> 3) + 2 * ((B.tm >> 2) & 1)) * 4 + (B.tm & 3))) * 4096, lane), s = (w == 1 || w == 2) ? rbf(1.f + v) : v;
      if (w < 2) ldsw<float>(lds0 + L_GT + w * 256 + modcol(lane) * 4, s); else put_et(lds0, w - 2, modcol(lane), s); }
    f32x4 acc[4][2];
    bar();
    compute_loop<2>(acc, lds0, wm, wn, lane, ts);
    STAMP(23);
    f32x4 gate[2], sc[2]; u32x2 xr[4][2];
    UNR for (int n = 0; n < 2; n++) { gate[n] = ldsr<f32x4>(lds0 + L_GT + wn * 128 + n * 64 + g * 16); sc[n] = ldsr<f32x4>(lds0 + L_GT + 256 + wn * 128 + n * 64 + g * 16); }
    UNR for (int m = 0; m < 4; m++) UNR for (int n = 0; n < 2; n++) xr[m][n] = ldsr<u32x2>(lds0 + L_RES + (wm * 64 + m * 16 + r) * 128 + (((wn * 4 + n * 2 + (g >> 1)) ^ (r >> 1)) * 16) + (g & 1) * 8);
    const size_t tb = (size_t)(B.tm * 16 + B.tn) * 16384; const rsrc_t ry = outrsrc(A.out, tb), rst = mkrsrc(ws, WS_ST2 + (size_t)(B.tm * 32 + B.tn * 2 + wn) * 1024);
    f32x4 v[4][2]; float mean[4], m2[4];
    UNR for (int m = 0; m < 4; m++) {
      o[m] = pack8u(fma4(gate[0], acc[m][0], unp(xr[m][0][0], xr[m][0][1])), fma4(gate[1], acc[m][1], unp(xr[m][1][0], xr[m][1][1]))); v[m][0] = unp(o[m][0], o[m][1]); v[m][1] = unp(o[m][2], o[m][3]);
      st_dev(pack8u(v[m][0] * sc[0], v[m][1] * sc[1]), ry, lane * 16, (wm * 8 + wn) * 1024 + m * 2048); }
    UNR for (int m = 0; m < 4; m++) mean[m] = hsum4(v[m][0] + v[m][1]);
    const float mf = fold4<false>(mean[0], mean[1], mean[2], mean[3]) * (1.f / 32); spread4(mf, mean[0], mean[1], mean[2], mean[3]);
    UNR for (int m = 0; m < 4; m++) { const f32x4 d0 = v[m][0] - mean[m], d1 = v[m][1] - mean[m]; m2[m] = hsum4(fma4(d0, d0, d1 * d1)); }
    st_dev8(__builtin_bit_cast(u32x2, f32x2{mf, fold4<false>(m2[0], m2[1], m2[2], m2[3])}), rst, (wm * 64 + ((g >> 1) + 2 * (g & 1)) * 16 + r) * 8, 0);
    STAMP(24);
    waitvm<0>(); bar();
    STAMP(25);
  }
  pin(lane);
  {
    const int g = lane >> 4, r = lane & 15, odd = B.tn & 1;
    const int fp0 = B.tn * 7 + odd + wn * 3, fph = odd ? B.tn * 7 : B.tn * 7 + 6;
    if (w < 2) ldsw<float>(lds0 + L_GT + w * 256 + modcol(lane) * 4, modvec(ws, WS_MODP + (size_t)(48 + (B.tn & 7) * 2 + w) * 4096, lane));
    u32x2 bb[7];
    UNR for (int n = 0; n < 7; n++) bb[n] = *(const u32x2*)(A.b1 + (n < 6 ? fp0 * 32 + n * 16 : fph * 32 + wn * 16) + 4 * g);
    pin(bb);
    UNR for (int i = 0; i < 4; i++) st_dev(mark, mkrsrc(ws, xchoff(B.tm, B.tn, w)), lane * 16, i * 1024);
    f32x4 acc[4][7];
    bar();
    { const rsrc_t rx1 = mkrsrc(ws, WS_X1 + (size_t)(B.tm * 16 + B.tn) * 16384);
      UNR for (int m = 0; m < 4; m++) st_dev(o[m], rx1, lane * 16, (wm * 8 + wn) * 1024 + m * 2048); }
    compute_loop<1>(acc, lds0, wm, wn, lane, ts);
    STAMP(29);
    bar();
    STAMP(30);
    f32x4 u[7], v[7];
    UNR for (int n = 0; n < 7; n++) { const unsigned a = lds0 + L_UVT + (n < 6 ? wn * 6 + n : 12 + wn) * 128 + g * 16; u[n] = ldsr<f32x4>(a); v[n] = ldsr<f32x4>(a + 64) + unp(bb[n][0], bb[n][1]); }
    const rsrc_t rh = mkrsrc(ws, WS_H1 + (size_t)B.tm * 56 * 16384);
    RowSt st[4]; u32x2 h6[4];
    UNR for (int m = 0; m < 4; m++) st[m].set(lds0 + L_STT + (wm * 64 + m * 16 + r) * 4, 1.f);
    auto act = [&](int m, int n) { const f32x4 t = __builtin_elementwise_max(lnf(acc[m][n], st[m], u[n], v[n]), zero); return t * t; };
    UNR for (int n = 0; n < 6; n += 2) { const int fp = fp0 + (n >> 1);
      UNR for (int m = 0; m < 4; m++) st_dev(pack8u(act(m, n), act(m, n + 1)), rh, lane * 16, (fp >> 1) * 16384 + (fp & 1) * 1024 + wm * 8192 + m * 2048); }
    UNR for (int m = 0; m < 4; m++) { const f32x4 t = act(m, 6); h6[m] = u32x2{cvtpk(t[0], t[1]), cvtpk(t[2], t[3])}; }
    UNR for (int m = 0; m < 4; m += 2) { const auto a = __builtin_amdgcn_permlane16_swap(h6[m][0], h6[m + 1][0], false, false), b = __builtin_amdgcn_permlane16_swap(h6[m][1], h6[m + 1][1], false, false);
      st_dev(u32x4{a[0], b[0], a[1], b[1]}, rh, (g & 1) * 2048 + (g >> 1) * 256 + r * 16, (fph >> 1) * 16384 + (fph & 1) * 1024 + wm * 8192 + m * 2048 + wn * 512); }
    STAMP(31);
    bar(); waitvm<0>(); bar();
    STAMP(32);
  }
  pin(lane);
  {
    const int g = lane >> 4, r = lane & 15, kh = B.tn >> 3, cg = (B.tn & 7) * 2 + wn;
    u32x2 bb[4]; u32x4 xr[2][2]; const rsrc_t rx1 = mkrsrc(ws, WS_X1 + (size_t)(B.tm * 16 + cg) * 16384);
    UNR for (int n = 0; n < 4; n++) bb[n] = *(const u32x2*)(A.b2 + cg * 64 + n * 16 + 4 * g);
    pin(bb);
    f32x4 acc[4][4];
    bar();
    UNR for (int mm = 0; mm < 2; mm++) UNR for (int ks = 0; ks < 2; ks++) xr[mm][ks] = ld_dev(rx1, lane * 16, ((wm * 4 + kh * 2 + mm) * 2 + ks) * 1024);
    compute_loop<3>(acc, lds0, wm, wn, lane, ts);
    STAMP(37);
    pin(xr[0]); pin(xr[1]);
    const rsrc_t rx = mkrsrc(ws, xchoff(B.tm, B.tn, w)), rp = mkrsrc(ws, xchoff(B.tm, B.tn ^ 8, w));
    f32x4 keep[2][4], gate[4];
    if (kh) { UNR for (int i = 0; i < 4; i++) st_dev(pack8u(acc[i >> 1][(i & 1) * 2], acc[i >> 1][(i & 1) * 2 + 1]), rx, lane * 16, i * 1024);
              UNR for (int n = 0; n < 4; n++) { keep[0][n] = acc[2][n]; keep[1][n] = acc[3][n]; } }
    else    { UNR for (int i = 0; i < 4; i++) st_dev(pack8u(acc[2 + (i >> 1)][(i & 1) * 2], acc[2 + (i >> 1)][(i & 1) * 2 + 1]), rx, lane * 16, i * 1024);
              UNR for (int n = 0; n < 4; n++) { keep[0][n] = acc[0][n]; keep[1][n] = acc[1][n]; } }
    UNR for (int n = 0; n < 4; n++) gate[n] = ldsr<f32x4>(lds0 + L_GT + wn * 256 + n * 64 + g * 16);
    UNR for (int mm = 0; mm < 2; mm++) UNR for (int n = 0; n < 4; n++) keep[mm][n] = fma4(gate[n], keep[mm][n] + unp(bb[n][0], bb[n][1]), unp(xr[mm][n >> 1][(n & 1) * 2], xr[mm][n >> 1][(n & 1) * 2 + 1]));
    pin(keep[0]); pin(keep[1]);
    u32x4 pp[4]; poll(pp, [&](int i) { return ld_dev(rp, lane * 16, i * 1024); });
    STAMP(38);
    const unsigned ot = lds0 + L_OT + w * 4096;
    UNR for (int mm = 0; mm < 2; mm++) UNR for (int n = 0; n < 4; n++) { const u32x4 t = pp[mm * 2 + (n >> 1)]; const f32x4 y = fma4(gate[n], unp(t[(n & 1) * 2], t[(n & 1) * 2 + 1]), keep[mm][n]);
      ldsw<u32x2>(ot + (mm * 16 + r) * 128 + (((n * 2 + (g >> 1)) ^ (r >> 1)) * 16) + (g & 1) * 8, u32x2{cvtpk(y[0], y[1]), cvtpk(y[2], y[3])}); }
    lwait();
    const int r8 = lane >> 3; const unsigned rv0 = r8 * 2048 + (((lane & 7) ^ (r8 >> 1)) * 16);
    char* op = (char*)A.out + ((size_t)(B.tm * 128 + wm * 64 + kh * 32) * D_ + cg * 64) * 2;
    UNR for (int i = 0; i < 4; i++) *(u32x4*)(op + i * 16384 + ((i & 1) ? rv0 ^ 64 : rv0)) = ldsr<u32x4>(ot + i * 1024 + lane * 16);
    STAMP(39);
  }
}

__global__ __launch_bounds__(512, 2) void k_block(Args A) {
  __shared__ __attribute__((aligned(1024))) char smem[L_TOT];
  const int w = __builtin_amdgcn_readfirstlane(threadIdx.x >> 6);
  const unsigned lds0 = (unsigned)(size_t)(LAS char*)smem;
  asm volatile(".Lcode_begin:");
  if ((PT & 2) && w == 3) touch_code(threadIdx.x & 63, lds0 + L_DUM);
  unsigned ts = 0; STAMP(0);
  const u64 tag = phase0(A, w, lds0, ts);
  if (w >= 4) loader_role(A, w - 4, lds0, tag, ts); else compute_role(A, w, lds0, ts);
}

extern "C" void kernel_launch(void* const* d_in, const int* in_sizes, int n_in, void* d_out, int out_size, void* d_ws, size_t ws_size, hipStream_t stream) {
  Args A; A.x = (const u16*)d_in[0]; A.wqkv = (const u16*)d_in[1]; A.wphi = (const u16*)d_in[2]; A.wo = (const u16*)d_in[3]; A.w1 = (const u16*)d_in[5]; A.b1 = (const u16*)d_in[6];
  A.w2 = (const u16*)d_in[7]; A.b2 = (const u16*)d_in[8]; A.c = (const u16*)d_in[9]; A.wm = (const u16*)d_in[10]; A.out = (u16*)d_out; A.ws = (char*)d_ws;
  k_block<<<256, 512, 0, stream>>>(A);
}
```

```cpp
#define STAMPS
#define DIAGB 77
#define DIAG0 13
#define DIAG 22
#include <hip/hip_runtime.h>
#include <hip/hip_bf16.h>

typedef unsigned short u16;
typedef unsigned u32x4 __attribute__((ext_vector_type(4)));
typedef unsigned u32x2 __attribute__((ext_vector_type(2)));
typedef short s16x4 __attribute__((ext_vector_type(4)));
typedef __bf16 bf16x8 __attribute__((ext_vector_type(8)));
typedef __bf16 bf16x2 __attribute__((ext_vector_type(2)));
typedef float f32x2 __attribute__((ext_vector_type(2)));
typedef float f32x4 __attribute__((ext_vector_type(4)));
#define LAS __attribute__((address_space(3)))
#define DI __device__ __forceinline__
#define SB __builtin_amdgcn_sched_barrier(0)
#define UNR _Pragma("unroll")

constexpr int D_ = 1024, F_ = 3584, MODN = 6 * D_;
constexpr size_t WS_EPOCH = 0;
constexpr size_t WS_S1    = 4096;
constexpr size_t WS_FLAG  = 1 << 16;
constexpr size_t WS_ST1   = 1 << 19;
constexpr size_t WS_ST2   = WS_ST1 + (1 << 18);
constexpr size_t WS_MODP  = 3 << 19;
constexpr size_t WS_UV1   = WS_MODP + 5 * 65536;
constexpr size_t WS_ZP    = WS_UV1 + 16 * 12 * 2048;
constexpr size_t WS_UV2   = WS_ZP + (1 << 17);
constexpr size_t WS_KV1   = 10 << 20;
constexpr size_t WS_KV2   = WS_KV1 + (2 << 20);
constexpr size_t WS_O     = 14 << 20;
constexpr size_t WS_X1    = 18 << 20;
constexpr size_t WS_H1    = 22 << 20;
constexpr size_t WS_STAMP = 40 << 20;
constexpr int SPIN_CAP = 1 << 14;
typedef unsigned long long u64;

#define STAMP(i) do { u64 t_; asm volatile("s_memrealtime %0\n\ts_waitcnt lgkmcnt(0)" : "=s"(t_) :: "memory"); ts = (int)(threadIdx.x & 63) == (i) ? (unsigned)t_ : ts; } while (0)

DI void glds16(const void* sbase, unsigned voff, unsigned ldsaddr) {
  asm volatile("s_mov_b32 m0, %2\n\ts_nop 0\n\tglobal_load_lds_dwordx4 %0, %1" :: "v"(voff), "s"(sbase), "s"(ldsaddr) : "memory");
}
DI void glds16nt(const void* sbase, unsigned voff, unsigned ldsaddr) {
  asm volatile("s_mov_b32 m0, %2\n\ts_nop 0\n\tglobal_load_lds_dwordx4 %0, %1 nt" :: "v"(voff), "s"(sbase), "s"(ldsaddr) : "memory");
}
DI void glds16x4(const void* sbase, unsigned voff, unsigned ldsaddr) {
  asm volatile("s_mov_b32 m0, %2\n\ts_nop 0\n\tglobal_load_lds_dwordx4 %0, %1 sc1\n\tglobal_load_lds_dwordx4 %0, %1 offset:1024 sc1\n\t"
               "global_load_lds_dwordx4 %0, %1 offset:2048 sc1\n\tglobal_load_lds_dwordx4 %0, %1 offset:3072 sc1" :: "v"(voff), "s"(sbase), "s"(ldsaddr) : "memory");
}
DI void touch(const void* sbase, unsigned voff, unsigned ldsdummy) {
  asm volatile("s_mov_b32 m0, %2\n\ts_nop 0\n\tglobal_load_lds_dword %0, %1" :: "v"(voff), "s"(sbase), "s"(ldsdummy) : "memory");
}
DI void touch_any(const void* p, unsigned ldsdummy) {
  asm volatile("s_mov_b32 m0, %1\n\ts_nop 0\n\tglobal_load_lds_dword %0, off" :: "v"(p), "s"(ldsdummy) : "memory");
}
DI void touch_nt(const void* sbase, unsigned voff, unsigned ldsdummy) {
  asm volatile("s_mov_b32 m0, %2\n\ts_nop 0\n\tglobal_load_lds_dword %0, %1 nt" :: "v"(voff), "s"(sbase), "s"(ldsdummy) : "memory");
}
DI void gld(u32x4& r, const void* sb, unsigned vo) { asm volatile("global_load_dwordx4 %0, %1, %2" : "=v"(r) : "v"(vo), "s"(sb) : "memory"); }
DI void gld_nt(u32x4& r, const void* sb, unsigned vo) { asm volatile("global_load_dwordx4 %0, %1, %2 nt" : "=v"(r) : "v"(vo), "s"(sb) : "memory"); }
DI void gld_dev(u32x4& r, const void* sb, unsigned vo) { asm volatile("global_load_dwordx4 %0, %1, %2 sc1" : "=v"(r) : "v"(vo), "s"(sb) : "memory"); }
DI void gld8(f32x2& r, const void* sb, unsigned vo) { asm volatile("global_load_dwordx2 %0, %1, %2 sc1" : "=v"(r) : "v"(vo), "s"(sb) : "memory"); }
DI void gld4_dev(unsigned& r, const void* sb, unsigned vo) { asm volatile("global_load_dword %0, %1, %2 sc1" : "=v"(r) : "v"(vo), "s"(sb) : "memory"); }
DI void gld4(unsigned& r, const void* sb, unsigned vo) { asm volatile("global_load_dword %0, %1, %2 nt" : "=v"(r) : "v"(vo), "s"(sb) : "memory"); }
template <class T> DI void pin(T& r) { asm volatile("" : "+v"(r)); }
template <class T, int N> DI void pin(T (&r)[N]) { UNR for (int i = 0; i < N; i++) pin(r[i]); }
template <class T> DI T opq(T v) { asm volatile("" : "+s"(v)); return v; }
DI f32x4 asf(u32x4 v) { return __builtin_bit_cast(f32x4, v); }
template <int N> DI void waitvm() { asm volatile("s_waitcnt vmcnt(%0)" :: "i"(N) : "memory"); }
template <int B, int LO = 0, int HI = 12> DI void waitx(int e) {
  if constexpr (LO == HI) waitvm<B + LO>();
  else if constexpr (LO == 0) { if (e == 0) waitvm<B>(); else waitx<B, 1, HI>(e); }
  else { constexpr int MID = (LO + HI + 1) / 2; if (e >= MID) waitx<B, MID, HI>(e); else waitx<B, LO, MID - 1>(e); }
}
DI void waitlgkm0() { __builtin_amdgcn_s_waitcnt(0xC07F); }
DI void lwait() { asm volatile("s_waitcnt lgkmcnt(0)" ::: "memory"); }
DI void bar() { asm volatile("s_barrier" ::: "memory"); }
DI void lbar() { asm volatile("s_waitcnt lgkmcnt(0)\n\ts_barrier" ::: "memory"); }
DI unsigned cvtpk(float a, float b) { f32x2 v = {a, b}; return __builtin_bit_cast(unsigned, __builtin_convertvector(v, bf16x2)); }
DI float rbf(float a) { return __uint_as_float(cvtpk(a, a) << 16); }
DI f32x4 rbf4(f32x4 s) { return f32x4{rbf(s[0]), rbf(s[1]), rbf(s[2]), rbf(s[3])}; }
DI float blo(unsigned u) { return __uint_as_float(u << 16); }
DI float bhi(unsigned u) { return __uint_as_float(u & 0xffff0000u); }
DI f32x4 unp(unsigned a, unsigned b) { return f32x4{blo(a), bhi(a), blo(b), bhi(b)}; }
DI u32x4 pack8u(const f32x4& a, const f32x4& b) { return u32x4{cvtpk(a[0], a[1]), cvtpk(a[2], a[3]), cvtpk(b[0], b[1]), cvtpk(b[2], b[3])}; }
DI bf16x8 pack8(const f32x4& a, const f32x4& b) { return __builtin_bit_cast(bf16x8, pack8u(a, b)); }
DI f32x4 mfma(bf16x8 a, bf16x8 b, f32x4 c) { return __builtin_amdgcn_mfma_f32_16x16x32_bf16(a, b, c, 0, 0, 0); }
DI s16x4 trd(unsigned a) { return __builtin_amdgcn_ds_read_tr16_b64_v4i16((LAS s16x4*)(size_t)a); }
DI bf16x8 cat(s16x4 lo, s16x4 hi) { return __builtin_bit_cast(bf16x8, __builtin_shufflevector(lo, hi, 0, 1, 2, 3, 4, 5, 6, 7)); }
template <class T> DI T ldsr(unsigned a) { return *(const LAS T*)(size_t)a; }
template <class T> DI void ldsw(unsigned a, T v) { *(LAS T*)(size_t)a = v; }
template <int C> DI float dpp(float x) { return __builtin_bit_cast(float, __builtin_amdgcn_update_dpp(0, __builtin_bit_cast(int, x), C, 0xf, 0xf, true)); }
DI float vmax(float a, float b) { float r; asm("v_max_f32_e32 %0, %1, %2" : "=v"(r) : "v"(a), "v"(b)); return r; }
DI float sw32_add(float a, float b) { auto r = __builtin_amdgcn_permlane32_swap(__float_as_uint(a), __float_as_uint(b), false, false); return __uint_as_float(r[0]) + __uint_as_float(r[1]); }
DI float sw16_add(float a, float b) { auto r = __builtin_amdgcn_permlane16_swap(__float_as_uint(a), __float_as_uint(b), false, false); return __uint_as_float(r[0]) + __uint_as_float(r[1]); }
DI float xg_sum(float a) { a = sw32_add(a, a); return sw16_add(a, a); }
DI float xg_max(float a) {
  auto r = __builtin_amdgcn_permlane32_swap(__float_as_uint(a), __float_as_uint(a), false, false); a = vmax(__uint_as_float(r[0]), __uint_as_float(r[1]));
  auto q = __builtin_amdgcn_permlane16_swap(__float_as_uint(a), __float_as_uint(a), false, false); return vmax(__uint_as_float(q[0]), __uint_as_float(q[1]));
}
DI float row16_sum(float a) { a += dpp<0x128>(a); a += dpp<0x124>(a); a += dpp<0x122>(a); a += dpp<0x121>(a); return a; }
DI f32x4 row16_sum4(f32x4 a) { return f32x4{row16_sum(a[0]), row16_sum(a[1]), row16_sum(a[2]), row16_sum(a[3])}; }
DI f32x4 spl(float a) { return f32x4{a, a, a, a}; }
DI f32x4 fma4(f32x4 a, f32x4 b, f32x4 c) { return __builtin_elementwise_fma(a, b, c); }
struct RowSt { float rs, nm; DI void set(unsigned a, float k) { rs = ldsr<float>(a) * k; nm = ldsr<float>(a + 512) * k; asm("" : "+v"(rs)); asm("" : "+v"(nm)); } };
DI f32x4 lnf(f32x4 acc, const RowSt& st, f32x4 u, f32x4 v) { return fma4(acc, spl(st.rs), fma4(u, spl(st.nm), v)); }
DI float fadd(float a, float b) { float r; asm("v_add_f32_e32 %0, %1, %2" : "=v"(r) : "v"(a), "v"(b)); return r; }
DI float hsum4(f32x4 a) { const f32x2 t = f32x2{a[0], a[1]} + f32x2{a[2], a[3]}; float r; asm("v_add_f32_e32 %0, %1, %2" : "=v"(r) : "v"(t[0]), "v"(t[1])); return r; }
struct rsrc_t { __amdgpu_buffer_rsrc_t r; unsigned base; };
DI rsrc_t mkrsrc(const char* ws, size_t off) { return rsrc_t{__builtin_amdgcn_make_buffer_rsrc((void*)ws, 0, 64 << 20, 0x00020000), (unsigned)off}; }
DI rsrc_t outrsrc(const void* out, size_t off) { return rsrc_t{__builtin_amdgcn_make_buffer_rsrc((void*)out, 0, 2048 * D_ * 2, 0x00020000), (unsigned)off}; }
DI unsigned xchoff(int tm, int tn, int w) { return WS_KV1 + (w >> 1) * (2 << 20) + (tn * 16 + tm) * 8192 + (w & 1) * 4096; }
DI u32x4 ld_dev(const rsrc_t& r, unsigned voff, unsigned soff) { return __builtin_amdgcn_raw_buffer_load_b128(r.r, voff, r.base + soff, 16); }
DI void st_dev(u32x4 v, const rsrc_t& r, unsigned voff, unsigned soff) { __builtin_amdgcn_raw_buffer_store_b128(v, r.r, voff + (r.base + soff), 0, 16); }
DI void st_dev8(u32x2 v, const rsrc_t& r, unsigned voff, unsigned soff) { __builtin_amdgcn_raw_buffer_store_b64(v, r.r, voff + (r.base + soff), 0, 16); }
DI unsigned umax4(u32x4 v) { return max(max(v[0], v[1]), max(v[2], v[3])); }
template <int N, class F> DI void poll(u32x4 (&v)[N], F ld) {
  _Pragma("nounroll") for (int spin = 0; spin < SPIN_CAP; spin++) { unsigned m = 0;
    UNR for (int i = 0; i < N; i++) v[i] = ld(i);
    UNR for (int i = 0; i < N; i++) m = max(m, umax4(v[i]));
    if (!__any(m == 0xffffffffu)) break;
    asm volatile("s_sleep 2" ::: "memory");
  }
}
#define EV 0
#define PT 7
#define PG 0
#define K0V 0
DI float fmax3(float a, float b, float c) { return __builtin_elementwise_maximum(__builtin_elementwise_maximum(a, b), c); }
struct Blk { int tm, tn, rot, j, xq; };
DI Blk blk1() { const int xcd = blockIdx.x & 7, j = blockIdx.x >> 3; Blk b; b.tm = j >> 1; b.tn = xcd * 2 + (j & 1); b.rot = b.tm; b.j = j; b.xq = xcd; return b; }
DI Blk blk() { const int xcd = blockIdx.x & 7, j = blockIdx.x >> 3; Blk b; b.tm = (xcd >> 2) * 8 + (j >> 2); b.tn = (xcd & 3) * 4 + (j & 3); b.rot = (b.tm & 7) * 2 + (b.tm >> 3); b.j = j; b.xq = xcd & 3; return b; }
DI unsigned ptile(int c, int lane) { const int row = 4 * (lane >> 4) + ((lane & 15) >> 2); return row * 128 + ((c ^ ((row >> 1) & 3)) * 32) + (lane & 3) * 8; }
DI unsigned htile(int c, int lane) { const int row = 4 * (lane >> 4) + ((lane & 15) >> 2); return row * 64 + ((c ^ ((row >> 2) & 1)) * 32) + (lane & 3) * 8; }
DI bf16x8 wfrag(unsigned a, unsigned ts) { return cat(trd(a), trd(a + ts)); }

DI unsigned silu2(unsigned cw) { const float c0 = blo(cw), c1 = bhi(cw); return cvtpk(c0 * __builtin_amdgcn_rcpf(1.f + __expf(-c0)), c1 * __builtin_amdgcn_rcpf(1.f + __expf(-c1))); }
DI unsigned fragpos(int k) { return (k >> 5) * 64 + ((k >> 2) & 3) * 16 + ((k & 3) + 4 * ((k >> 4) & 1)) * 2; }
DI f32x4 gemv64(unsigned a, bf16x8 s0, bf16x8 s1, int lane) { f32x4 d[4];
  UNR for (int cc = 0; cc < 4; cc++) d[cc] = mfma(wfrag(a + 4096 + ptile(cc, lane), 2048), s1, mfma(wfrag(a + ptile(cc, lane), 2048), s0, f32x4{0, 0, 0, 0}));
  const bool b0 = lane & 1, b1 = lane & 2; f32x4 o;
  UNR for (int k = 0; k < 4; k++) { const float lo = b0 ? d[1][k] : d[0][k], hi = b0 ? d[3][k] : d[2][k]; o[k] = b1 ? hi : lo; }
  return o;
}

template <int CFG> struct Cfg;
template <> struct Cfg<0> { static constexpr int NT = 6, NP = 3, HALF = 0, NB = 2, NS = 3, N = 3 * D_, KT = 16, B0 = 49152, BS0 = 0; };
template <> struct Cfg<1> { static constexpr int NT = 7, NP = 3, HALF = 1, NB = 7, NS = 3, N = F_, KT = 16, B0 = 65536, BS0 = 2; };
template <> struct Cfg<2> { static constexpr int NT = 2, NP = 1, HALF = 0, NB = 2, NS = 4, N = D_, KT = 16, B0 = 73728, BS0 = 0; };
template <> struct Cfg<3> { static constexpr int NT = 4, NP = 2, HALF = 0, NB = 4, NS = 4, N = D_, KT = 28, B0 = 65536, BS0 = 0; };
template <int CFG> struct Lay { using C = Cfg<CFG>; static constexpr int SBB = C::NP * 8192 + C::HALF * 4096, PB = 2 * C::NP + C::HALF, PER = 4 + PB; };
template <int CFG> DI unsigned btile(int tl, int lane) { using C = Cfg<CFG>; return C::B0 + (tl < 4 * C::NP ? (tl >> 2) * 8192 + ptile(tl & 3, lane) : C::NP * 8192 + htile(tl & 1, lane)); }
constexpr int L_RES = 106496, L_WPHI = 155648, L_UVT = L_WPHI + 4096, L_STT = L_UVT + 2048, L_ET = L_STT + 1024, L_GT = L_ET + 256, L_DUM = L_GT + 512, L_TOT = 163840, L_REST = 122880, L_OT = 131072;
constexpr int L_XT = 131072, L_SAU = 147456, L_SIL = L_WPHI, L_RED = L_SIL + 2048, L_SROW = L_RED + 2048, L_SIL2 = L_GT;
constexpr int L_TR = 0, L_VF = 32768, L_KX = 49152;

template <int CFG> DI void compute_loop(f32x4 (&acc)[4][Cfg<CFG>::NT], unsigned lds0, int wm, int wn, int lane, unsigned& ts) {
  using C = Cfg<CFG>; using L = Lay<CFG>; constexpr int MT = 4, NT = C::NT, NB = C::NB, KT = C::KT;
  const unsigned aoff = lds0 + wm * 8192 + lane * 16; unsigned boff[NB];
  UNR for (int i = 0; i < NB; i++) boff[i] = lds0 + btile<CFG>(CFG == 1 ? (i < 6 ? wn * 6 + i : 12 + wn) : wn * NB + i, lane);
  bf16x8 fa[2][MT]; s16x4 fb[2][NT][2]; unsigned va, vb[NB];
  constexpr int NR = MT + 2 * NT, NM = MT * NT, NU = 1 + NB, FREE = NM - NR, UPG = FREE > 0 ? (NU + FREE - 1) / FREE : NU;
  auto upd = [&](int i, unsigned sa, unsigned sb) { if (i == 0) { va = aoff + sa; pin(va); } else { vb[i - 1] = boff[i - 1] + sb; pin(vb[i - 1]); } };
  auto rd1 = [&](int buf, int ks, int i) {
    if (i == 0 || i > 2 * NT) { const int m = i ? i - 2 * NT : 0; fa[buf][m] = ldsr<bf16x8>(va + m * 2048 + ks * 1024); }
    else { const int n = (i - 1) >> 1, t = (i - 1) & 1;
      fb[buf][n][t] = trd(CFG == 0 ? vb[n & 1] + (n >> 1) * 8192 + ks * 4096 + t * 2048 : (CFG == 1 && n == 6) ? vb[NB - 1] + ks * 2048 + t * 1024 : vb[n] + ks * 4096 + t * 2048); }
  };
  auto step = [&](int buf, int ks, bool doread, bool first, bool doupd, unsigned sa, unsigned sb) {
    UNR for (int i = 0; i < NM; i++) {
      const int m = i / NT, n = i % NT; const bf16x8 b = cat(fb[buf][n][0], fb[buf][n][1]);
      const f32x4 cin = first ? f32x4{0, 0, 0, 0} : acc[m][n];
      acc[m][n] = (CFG == 0 && n >= 4) ? mfma(fa[buf][m], b, cin) : mfma(b, fa[buf][m], cin);
      SB;
      if (doread && i < NR) { rd1(buf ^ 1, ks, i); SB; }
      if (doupd && (FREE > 0 ? i >= NR : i == NM - 1)) { const int u0 = FREE > 0 ? (i - NR) * UPG : 0;
        UNR for (int u = u0; u < u0 + UPG; u++) if (u < NU) upd(u, sa, sb);
        SB; }
    }
  };
  UNR for (int i = 0; i < NU; i++) upd(i, 0, C::BS0 * L::SBB);
  SB; lbar();
  STAMP(CFG == 0 ? 12 : CFG == 2 ? 22 : CFG == 1 ? 28 : 36);
  UNR for (int i = 0; i < NR; i++) rd1(0, 0, i);
  SB;
  unsigned sa = 16384, sb = ((C::BS0 + 1) % C::NS) * L::SBB;
  step(0, 1, true, true, true, sa, sb);
  for (int t = 0; t < KT - 1; t++) {
    waitlgkm0(); bar(); SB;
    sa += 16384; if (sa == C::NS * 16384) sa = 0;
    sb += L::SBB; if (sb == C::NS * L::SBB) sb = 0;
    step(1, 0, true, false, false, 0, 0);
    if (FREE >= 4) { waitlgkm0(); SB; }
    step(0, 1, true, false, true, sa, sb);
  }
  step(1, 0, false, false, false, 0, 0);
}

template <int CFG> struct Loader {
  using C = Cfg<CFG>; using L = Lay<CFG>; static constexpr int N = C::N, KT = C::KT, PER = L::PER, PB = L::PB, NS = C::NS;
  const char *ab, *bb, *hb; unsigned av, bv, hv, la, lb, lh; int ka, kb;
  DI void init(const void* A, int akt, const u16* W, int k0, int col0, int hcol, int rot, int tm, int lw, int lane, unsigned lds0) {
    const int r = lane >> 3, hr = lane >> 2;
    ab = (const char*)A + ((size_t)tm * akt + k0) * 16384 + lw * 4096; av = lane * 16;
    bb = (const char*)W + ((size_t)(k0 * 64 + lw * 16) * N + col0) * 2; bv = r * N * 2 + (((lane & 7) ^ (((r >> 1) & 3) << 1)) * 16);
    hb = (const char*)W + ((size_t)(lw * 16) * N + hcol) * 2; hv = hr * N * 2 + (((lane & 3) ^ (((hr >> 2) & 1) << 1)) * 16);
    if (CFG == 3) { const int t = lw & 1; bb -= (size_t)t * 16 * N * 2; hv = bv + (4 * t + 4 * (r >> 2)) * N * 2; bv += t * 16 * N * 2; }
    la = lds0 + lw * 4096; lb = lds0 + C::B0 + lw * 2048; lh = lds0 + C::B0 + C::NP * 8192 + lw * 1024; ka = kb = rot;
  }
  DI void issueA(int slot) { glds16x4(ab + (size_t)ka * 16384, av, la + slot * 16384); ka++; if (ka == KT) ka = 0; }
  template <bool NT> DI void issueB(int slot) { const char* b = bb + (size_t)kb * 64 * N * 2; slot += C::BS0; if (slot >= NS) slot -= NS; const unsigned so = slot * L::SBB;
    const bool nat = CFG == 3 && ((0x1020408u >> kb) & 1);
    const unsigned v0 = nat ? hv : bv, v1 = v0 + (nat ? 16 : 8) * N * 2;
    UNR for (int i = 0; i < 2 * C::NP; i++) { const char* p = b + (i >> 1) * (CFG == 0 ? 2048 : 128) + (CFG == 3 ? 0 : (i & 1) * (8 * N * 2)); const unsigned l = lb + so + (i >> 1) * 8192 + (i & 1) * 1024, vo = CFG == 3 && (i & 1) ? v1 : v0;
      if (NT) glds16nt(p, vo, l); else glds16(p, vo, l); }
    if (C::HALF) { if (NT) glds16nt(hb + (size_t)kb * 64 * N * 2, hv, lh + so); else glds16(hb + (size_t)kb * 64 * N * 2, hv, lh + so); }
    kb++; if (kb == KT) kb = 0;
  }
  DI void issue(int slot) { issueA(slot); issueB<false>(slot); }
  template <int E1, class HF, class HK, class H1> DI void run(HF hf, HK hk, H1 h1) {
    UNR for (int s = 0; s < NS; s++) issueA(s);
    if (NS == 3) {
      waitvm<8>(); bar(); int e0 = hf(0); waitx<4>(e0); lbar();
      UNR for (int t = 1; t <= 5; t++) { issue((t - 1) % 3); int e1 = hf(t); if (t >= 3) e1 += hk(t); waitx<PER>(e0 + e1); lbar(); e0 = e1; }
      int slot = 2;
      _Pragma("nounroll") for (int t = 6; t <= KT - 3; t++) { issue(slot); slot++; if (slot == NS) slot = 0; const int e1 = hk(t); waitx<PER>(e0 + e1); lbar(); e0 = e1; }
    } else {
      waitvm<12>(); bar(); waitvm<8>(); bar();
      issue(0); waitvm<4 + PER>(); bar();
      int slot = 1;
      for (int t = 2; t <= KT - 4; t++) { issue(slot); slot = (slot + 1) & 3; waitvm<2 * PER>(); bar(); }
      waitvm<PER>(); bar();
    }
    h1(); waitvm<E1>(); bar();
  }
};
DI u64* flagp(char* ws, int tile, int line, int word) { return (u64*)(ws + WS_FLAG) + (tile * 6 + line) * 16 + word; }
DI u64 wait_flags(u64* f, u64 tag, u64 need) {
  u64 got = 0;
  _Pragma("nounroll") for (int spin = 0; spin < SPIN_CAP; spin++) {
    got = __ballot(__hip_atomic_load(f, __ATOMIC_RELAXED, __HIP_MEMORY_SCOPE_AGENT) == tag);
    if ((got & need) == need) break;
    asm volatile("s_sleep 1" ::: "memory");
  }
  return got;
}
template <class H> DI bool panel_sync(char* ws, int line, const Blk& P, const Blk& C, int lw, int lane, u64 tag, H pre) {
  bar();
  if (lw == 0) { const int i = lane & 15, grp = lane >> 4;
    if (line == 0) { if (grp < 3) __hip_atomic_store(grp == 0 ? flagp(ws, P.tm * 16 + i, 0, P.tn) : grp == 1 ? flagp(ws, i * 16 + P.tn, 1, P.tm) : flagp(ws, P.tn * 16 + i, 2, P.tm), tag, __ATOMIC_RELAXED, __HIP_MEMORY_SCOPE_AGENT); }
    else if (grp == 0) __hip_atomic_store(flagp(ws, P.tm * 16 + i, line, P.tn), tag, __ATOMIC_RELAXED, __HIP_MEMORY_SCOPE_AGENT); }
  pre();
  bool all = true;
  if (lw == 0) all = wait_flags(flagp(ws, C.tm * 16 + C.tn, line, line == 0 ? min(lane, 47) : lane & 15), tag, 0xffff) == ~0ull;
  bar();
  return all;
}
DI int modcol(int lane) { return 4 * (lane & 15) + 2 * (lane >> 5) + ((lane >> 4) & 1); }
DI float modvec(const char* ws, unsigned part, int lane, u32x4 (&p)[4], bool have) {
  if (!have || __any(max(max(umax4(p[0]), umax4(p[1])), max(umax4(p[2]), umax4(p[3]))) == 0xffffffffu)) { const rsrc_t r = mkrsrc(ws, part); poll(p, [&](int i) { return ld_dev(r, lane * 16, i * 1024); }); }
  const f32x4 a = (asf(p[0]) + asf(p[1])) + (asf(p[2]) + asf(p[3]));
  return rbf(sw16_add(sw32_add(a[0], a[2]), sw32_add(a[1], a[3])));
}
DI float modvec(const char* ws, unsigned part, int lane) { u32x4 p[4]; return modvec(ws, part, lane, p, false); }
DI void put_et(unsigned lds0, int vec, int k, float v) { ldsw<u16>(lds0 + L_ET + vec * 128 + fragpos(k), (u16)(__float_as_uint(v) >> 16)); }

template <int CFG, int NTL> struct UV {
  bf16x8 f[NTL][2]; unsigned ad[NTL], ts[NTL];
  DI void prep(int lw, int lane, unsigned base) { UNR for (int i = 0; i < NTL; i++) { const int tl = min(lw * NTL + i, 2 * Cfg<CFG>::NT - 1); ad[i] = base + btile<CFG>(tl, lane); ts[i] = tl < 4 * Cfg<CFG>::NP ? 2048 : 1024; pin(ad[i]); } }
  DI void read() { UNR for (int i = 0; i < NTL; i++) { f[i][0] = wfrag(ad[i], ts[i]); f[i][1] = wfrag(ad[i] + 2 * ts[i], ts[i]); } }
  DI void publish(const rsrc_t& r, int tm, int lw, int lane, unsigned lds0) {
    const bf16x8 e0 = ldsr<bf16x8>(lds0 + L_ET + (lane & 1) * 128 + (lane >> 4) * 16), e1 = ldsr<bf16x8>(lds0 + L_ET + (lane & 1) * 128 + 64 + (lane >> 4) * 16);
    f32x4 d[NTL];
    UNR for (int i = 0; i < NTL; i++) d[i] = mfma(f[i][1], e1, mfma(f[i][0], e0, f32x4{0, 0, 0, 0}));
    if ((lane & 14) == 0) UNR for (int i = 0; i < NTL; i++) st_dev(__builtin_bit_cast(u32x4, d[i]), r, (lane & 1) * 64 + (lane >> 4) * 16, (lw * NTL + i) * 2048 + tm * 128);
  }
  u32x4 v[2 * NTL];
  DI void fetch(const char* base, int lw, int lane) { UNR for (int i = 0; i < 2 * NTL; i++) gld_dev(v[i], base + lw * NTL * 2048 + i * 1024, lane * 16); }
  DI void finish(const rsrc_t& r, int lw, int lane, unsigned tab) {
    pin(v); unsigned mx = 0;
    UNR for (int i = 0; i < 2 * NTL; i++) mx = max(mx, umax4(v[i]));
    if (__any(mx == 0xffffffffu)) poll(v, [&](int i) { return ld_dev(r, lane * 16, lw * NTL * 2048 + i * 1024); });
    UNR for (int i = 0; i < NTL; i++) { const f32x4 a = asf(v[2 * i]) + asf(v[2 * i + 1]);
      float t = sw16_add(sw32_add(a[0], a[2]), sw32_add(a[1], a[3])); t += dpp<0x128>(t);
      ldsw<float>(tab + (lw * NTL + i) * 128 + (lane & 7) * 16 + (2 * (lane >> 5) + ((lane >> 4) & 1)) * 4, t); }
  }
};
template <int NPART> struct Stats {
  f32x2 v[NPART / 2];
  DI void fetch(const char* st, int tm, int lw, int lane) { UNR for (int i = 0; i < NPART / 2; i++) gld8(v[i], st + (size_t)(tm * NPART + i) * 1024 + lw * 256, (lane >> 5) * (NPART * 512) + (lane & 31) * 8); }
  DI void finish(int lw, int lane, unsigned tab) { pin(v); f32x2 s = v[0]; float q = 0;
    UNR for (int i = 1; i < NPART / 2; i++) s += v[i];
    const float mean = sw32_add(s[0], s[0]) * (1.f / NPART);
    UNR for (int i = 0; i < NPART / 2; i++) { const float d = v[i][0] - mean; q += d * d; }
    q = s[1] + (float)(D_ / NPART) * q;
    const float rstd = __builtin_amdgcn_rsqf(sw32_add(q, q) * (1.f / D_) + 1e-5f);
    ldsw<float>(tab + (lane >> 5) * 512 + (lw * 32 + (lane & 31)) * 4, lane < 32 ? rstd : -mean * rstd); }
};

template <bool MAX> DI float fold4(float a, float b, float c, float d) {
  auto op = [](unsigned x, unsigned y) { return MAX ? __builtin_elementwise_maximum(__uint_as_float(x), __uint_as_float(y)) : __uint_as_float(x) + __uint_as_float(y); };
  const auto r0 = __builtin_amdgcn_permlane32_swap(__float_as_uint(a), __float_as_uint(b), false, false), r1 = __builtin_amdgcn_permlane32_swap(__float_as_uint(c), __float_as_uint(d), false, false);
  const auto r2 = __builtin_amdgcn_permlane16_swap(__float_as_uint(op(r0[0], r0[1])), __float_as_uint(op(r1[0], r1[1])), false, false);
  return op(r2[0], r2[1]);
}
DI void spread4(float t, float& a, float& b, float& c, float& d) {
  const auto r2 = __builtin_amdgcn_permlane16_swap(__float_as_uint(t), __float_as_uint(t), false, false);
  const auto r0 = __builtin_amdgcn_permlane32_swap(r2[0], r2[0], false, false), r1 = __builtin_amdgcn_permlane32_swap(r2[1], r2[1], false, false);
  a = __uint_as_float(r0[0]); b = __uint_as_float(r0[1]); c = __uint_as_float(r1[0]); d = __uint_as_float(r1[1]);
}
DI void feat2(const bf16x8 (&wf)[2], bf16x8 x0, bf16x8 x1, bf16x8& fp0, bf16x8& fn0, bf16x8& fp1, bf16x8& fn1) {
  const f32x4 zero = {0, 0, 0, 0}; f32x4 p[2][2] = {{mfma(wf[0], x0, zero), mfma(wf[1], x0, zero)}, {mfma(wf[0], x1, zero), mfma(wf[1], x1, zero)}};
  float mx[2], nx[2], sp[2], sn[2]; f32x4 a[2][2], b[2][2];
  UNR for (int i = 0; i < 2; i++) { const f32x4 u = p[i][0], v = p[i][1];
    mx[i] = fmax3(fmax3(fmax3(u[0], u[1], u[2]), u[3], v[0]), fmax3(v[1], v[2], v[3]), v[3]); nx[i] = fmax3(fmax3(fmax3(-u[0], -u[1], -u[2]), -u[3], -v[0]), fmax3(-v[1], -v[2], -v[3]), -v[3]); }
  spread4(fold4<true>(mx[0], mx[1], nx[0], nx[1]), mx[0], mx[1], nx[0], nx[1]);
  UNR for (int i = 0; i < 2; i++) UNR for (int h = 0; h < 2; h++) UNR for (int j = 0; j < 4; j += 2) { const f32x2 t = {p[i][h][j], p[i][h][j + 1]};
    f32x2 d = t - f32x2{mx[i], mx[i]}, e = -t - f32x2{nx[i], nx[i]}; asm("" : "+v"(d), "+v"(e));
    a[i][h][j] = __builtin_amdgcn_exp2f(d[0]); a[i][h][j + 1] = __builtin_amdgcn_exp2f(d[1]); b[i][h][j] = __builtin_amdgcn_exp2f(e[0]); b[i][h][j + 1] = __builtin_amdgcn_exp2f(e[1]); }
  UNR for (int i = 0; i < 2; i++) { sp[i] = hsum4(a[i][0] + a[i][1]); sn[i] = hsum4(b[i][0] + b[i][1]); }
  spread4(__builtin_amdgcn_rcpf(fold4<false>(sp[0], sp[1], sn[0], sn[1])), sp[0], sp[1], sn[0], sn[1]);
  fp0 = pack8(a[0][0] * sp[0], a[0][1] * sp[0]); fn0 = pack8(b[0][0] * sn[0], b[0][1] * sn[0]); fp1 = pack8(a[1][0] * sp[1], a[1][1] * sp[1]); fn1 = pack8(b[1][0] * sn[1], b[1][1] * sn[1]);
}

DI void touch_code(int lane, unsigned ldsdummy) {
  const char* pc; unsigned len, back;
  asm volatile("s_getpc_b64 %0\n.Lhere%=:\n\ts_mov_b32 %2, .Lhere%=-.Lcode_begin\n\ts_mov_b32 %1, .Lcode_end-.Lcode_begin" : "=s"(pc), "=s"(len), "=s"(back));
  pc -= back;
  const unsigned o = ((blockIdx.x >> 3) * 16 + (lane & 15)) * 128;
  touch(pc, min(o, len), ldsdummy);
}
struct Args { const u16 *x, *wqkv, *wphi, *wo, *w1, *b1, *w2, *b2, *c, *wm; u16* out; char* ws; };

DI u64 phase0(const Args& A, int w, unsigned lds0, unsigned& ts) {
  char* ws = A.ws; const Blk B = blk1(); const f32x4 zero = {0, 0, 0, 0};
  const int lane = threadIdx.x & 63, r8 = lane >> 3, g = lane >> 4, r = lane & 15;
  unsigned cw, cw2; gld4(cw, A.c, w * 256 + ((B.tm * 16 + lane * 4) & 255)); gld4(cw2, A.c, B.tm * 128 + (lane & 31) * 4);
  const char* wb = (const char*)A.wm + B.tn * 128; const unsigned wv = r8 * MODN * 2 + (((lane & 7) ^ (((r8 >> 1) & 3) << 1)) * 16);
  auto piece = [&](int i) { glds16(wb + (size_t)(w * 16 + ((B.tm + i) & 15)) * 16 * MODN + 2 * D_, wv, lds0 + (w * 16 + i) * 1024); };
  glds16nt(wb + (size_t)(w * 16 + B.tm) * 16 * MODN + 2 * D_, wv, lds0 + w * 16384);
  const unsigned rv0 = r8 * 2048 + (((lane & 7) ^ (r8 >> 1)) * 16);
  UNR for (int i = 0; i < 2; i++) glds16nt((const char*)A.x + ((size_t)(B.tm * 128 + w * 16 + i * 8) * D_ + B.tn * 64) * 2, i ? rv0 ^ 64 : rv0, lds0 + L_XT + (w * 2 + i) * 1024);
  bar();
  piece(1); piece(2);
  if (w == 7 && (PT & 1)) { const Blk C = blk(); if (lane < 6) touch_any(flagp(ws, lane < 3 ? B.tm * 16 + B.tn : C.tm * 16 + C.tn, lane, 0), lds0 + L_DUM); }
  if (w < 3) {
    const int l = w * 8 + r8;
    if (l < 22) st_dev(u32x4{~0u, ~0u, ~0u, ~0u}, mkrsrc(ws, WS_MODP), (l < 10 ? (((l >> 1) * 16 + B.tn) * 16 + B.tm) * 256 + (l & 1) * 128 : 5 * 65536 + (B.tn * 12 + l - 10) * 2048 + B.tm * 128) + (lane & 7) * 16, 0); }
  waitvm<5>(); pin(cw); pin(cw2);
  ldsw<unsigned>(lds0 + L_SIL + w * 256 + fragpos(2 * lane), silu2(cw));
  if (w == 7) ldsw<unsigned>(lds0 + L_SIL2 + fragpos(2 * (lane & 31)), silu2(cw2));
  unsigned pa[4]; UNR for (int cc = 0; cc < 4; cc++) { pa[cc] = lds0 + w * 16384 + ptile(cc, lane); pin(pa[cc]); }
  STAMP(1);
  f32x4 a[4] = {zero, zero, zero, zero};
  auto kstep = [&](int ks) { const bf16x8 sf = ldsr<bf16x8>(lds0 + L_SIL + (w * 4 + ks) * 64 + g * 16);
    UNR for (int cc = 0; cc < 4; cc++) a[cc] = mfma(wfrag(pa[cc] + ks * 4096, 2048), sf, a[cc]); };
  UNR for (int i = 3; i < 10; i++) piece(i);
  waitvm<6>();
  u32x2 xt[4];
  UNR for (int cc = 0; cc < 4; cc++) xt[cc] = ldsr<u32x2>(lds0 + L_XT + (w * 16 + r) * 128 + (((cc * 2 + (g >> 1)) ^ (r >> 1)) * 16) + (g & 1) * 8);
  kstep(0);
  UNR for (int i = 10; i < 16; i++) piece(i);
  STAMP(2);
  bar();
  glds16nt(wb + (size_t)(B.tm * 8 + w) * 16 * MODN, wv, lds0 + L_SAU + w * 1024);
  { const int e = min(w * 24 + lane, 191), row = e / 6, l = e - row * 6;
    if (lane < 24) touch_nt((const char*)A.wqkv + (size_t)B.j * 32 * 6144 + B.xq * 256, row * 6144 + (l >> 1) * 2048 + (l & 1) * 128, lds0 + L_DUM); }
  f32x2 ep; gld8(ep, ws + WS_EPOCH, 0);
  waitvm<11>(); kstep(1); waitvm<7>(); kstep(2); waitvm<3>(); kstep(3);
  if (r == 0) UNR for (int cc = 0; cc < 4; cc++) ldsw<f32x4>(lds0 + L_RED + w * 256 + cc * 64 + g * 16, a[cc]);
  STAMP(3);
  lbar();
  f32x4 xv[4]; UNR for (int cc = 0; cc < 4; cc++) xv[cc] = unp(xt[cc][0], xt[cc][1]);
  float s = 0;
  UNR for (int i = 0; i < 8; i++) s += ldsr<float>(lds0 + L_RED + i * 256 + lane * 4);
  s = rbf(1.f + rbf(s)); ldsw<float>(lds0 + L_SROW + w * 256 + lane * 4, s);
  asm volatile("" ::: "memory");
  const rsrc_t ry = outrsrc(A.out, (B.tm * 16 + B.tn) * 16384);
  UNR for (int ks = 0; ks < 2; ks++)
    st_dev(pack8u(xv[2 * ks] * ldsr<f32x4>(lds0 + L_SROW + w * 256 + ks * 128 + g * 16), xv[2 * ks + 1] * ldsr<f32x4>(lds0 + L_SROW + w * 256 + ks * 128 + 64 + g * 16)), ry, lane * 16, w * 2048 + ks * 1024);
  STAMP(4);
  if (w == 6 && B.tm == B.tn) __hip_atomic_store((float*)(ws + WS_S1) + B.tn * 64 + lane, s, __ATOMIC_RELAXED, __HIP_MEMORY_SCOPE_AGENT);
  { const float mean = xg_sum(hsum4((xv[0] + xv[1]) + (xv[2] + xv[3]))) * (1.f / 64); f32x4 q = zero;
    UNR for (int cc = 0; cc < 4; cc++) { const f32x4 d = xv[cc] - mean; q = fma4(d, d, q); }
    const float m2 = xg_sum(hsum4(q));
    if (g == 0) st_dev8(__builtin_bit_cast(u32x2, f32x2{mean, m2}), mkrsrc(ws, WS_ST1 + (size_t)(B.tm * 16 + B.tn) * 1024), r * 8, w * 128); }
  waitvm<0>(); pin(ep);
  STAMP(5);
  return __builtin_bit_cast(u64, ep) + 0x9E3779B97F4A7C15ull;
}

DI void loader_role(const Args& A, int lw, unsigned lds0, u64 tag, unsigned& ts) {
  char* ws = A.ws; const Blk B2 = blk(), B1 = blk1(); const f32x4 zero = {0, 0, 0, 0}; const u32x4 mark = {~0u, ~0u, ~0u, ~0u};
  int lane = threadIdx.x & 63; pin(lane);
  Loader<2> G2;
  {
    const Blk& B = B1; const int g = lane >> 4, r = lane & 15, q = r >> 2, p = lane & 3, r8 = lane >> 3;
    Loader<0> G; G.init(A.out, 16, A.wqkv, 0, B.tn * 64, 0, B.rot, B.tm, lw, lane, lds0);
    const rsrc_t ruv = mkrsrc(ws, WS_UV1 + B.tn * 12 * 2048);
    UV<0, 3> U; Stats<16> S; U.prep(lw, lane, lds0);
    const bool all0 = panel_sync(ws, 0, B, B, lw, lane, tag, [&]() { UNR for (int s = 0; s < 3; s++) G.issueB<false>(s); });
    STAMP(11);
    const int t1 = 3 + (B.tm * 5 >> 3), t3 = 3 + (((B.tm + 8) & 15) * 5 >> 3);
    const char* sap = ws + WS_MODP + (size_t)(64 + B.rot) * 4096; u32x4 sp[4]; unsigned s1;
    G.run<15>([&](int t) { int e = 0;
        if (t == 0) { U.read(); if (!all0) wait_flags(flagp(ws, B.tm * 16 + B.tn, 1, lane & 31), tag, ~0ull); }
        if (t == 1) { UNR for (int i = 0; i < 4; i++) gld_dev(sp[i], sap + i * 1024, lw == 1 ? lane * 16 : 0); gld4_dev(s1, ws + WS_S1 + B.rot * 256, lane * 4);
          UNR for (int c = 0; c < 2; c++) st_dev(mark, mkrsrc(ws, WS_KV1 + (size_t)(B.tn * 4 + lw) * 32768), lane * 16, B.tm * 2048 + c * 1024);
          e = 7; }
        if (t == 2) { UNR for (int c = 0; c < 2; c++) st_dev(mark, mkrsrc(ws, WS_KV2 + (size_t)(B.tn * 16 + B.tm) * 8192), lane * 16, lw * 2048 + c * 1024);
          if (r8 < 5) st_dev(mark, mkrsrc(ws, WS_ZP), (r8 == 0 ? (B.tn * 4 + lw) * 2048 : (1 << 17) + (B.tn * 16 + lw * 4 + r8 - 1) * 2048) + (lane & 7) * 16, B.tm * 128);
          e = 3; }
        if (t == 3 && PG) { const unsigned i = (B.j * 64 + lane) << PG;
          const char* p = lw == 0 ? ws + WS_KV1 + min(i, 27u << 20) : lw == 1 ? (const char*)A.w1 + min(i, D_ * F_ * 2u - 4) : lw == 2 ? (const char*)A.w2 + min(i, D_ * F_ * 2u - 4) : i < (2u << 20) ? (const char*)A.wo + i : ws + min(i - (2u << 20), 3u << 20);
          touch_any(p, lds0 + L_DUM); e = 1; }
        if (t == 4) { pin(sp); pin(s1); if (lw == 0) put_et(lds0, 0, lane, __uint_as_float(s1)); if (lw == 1) put_et(lds0, 1, modcol(lane), modvec(ws, WS_MODP + (64 + B.rot) * 4096, lane, sp, true)); }
        if (t == 5) { U.publish(ruv, B.tm, lw, lane, lds0); e = 3; }
        return e; },
      [&](int t) { int e = 0;
        if (t == t1 && !(EV & 1)) {
          const char* wb = opq((const char*)A.wm) + (size_t)B.tm * 128 * MODN + (lw + 2) * 2 * D_ + B.tn * 128; const unsigned wv = r8 * MODN * 2 + (((lane & 7) ^ (((r8 >> 1) & 3) << 1)) * 16);
          const unsigned l0 = opq(lds0 + L_REST) + lw * 8192;
          UNR for (int i = 0; i < 8; i++) glds16nt(wb + i * 16 * MODN, wv, l0 + i * 1024);
          e += 8; }
        if (t == t3 && !(EV & 2)) { touch(opq((const char*)A.w1) + (size_t)(blockIdx.x * 4 + lw) * 7168, min(lane, 55) * 128, lds0 + L_DUM); touch(opq((const char*)A.wo) + (size_t)(blockIdx.x * 4 + lw) * 2048, (lane & 15) * 128, lds0 + L_DUM); e += 2; }
        if ((PT & 4) && t == 11 && lw == 0) { touch_code(lane, lds0 + L_DUM); e += 1; }
        return e; },
      [&]() { U.fetch(opq(ws) + ruv.base, lw, lane); S.fetch(opq(ws) + WS_ST1, B.tm, lw, lane); glds16nt((const char*)A.wphi + B.tn * 4096 + lw * 1024, lane * 16, lds0 + L_WPHI + lw * 1024); });
    G2.init(ws + WS_O, 16, A.wo, 0, B2.tn * 64, 0, B2.rot, B2.tm, lw, lane, lds0);
    { const unsigned rv0 = r8 * 2048 + (((lane & 7) ^ (r8 >> 1)) * 16);
      UNR for (int i = 0; i < 4; i++) glds16nt((const char*)A.x + ((size_t)(B2.tm * 128 + (lw * 4 + i) * 8) * D_ + B2.tn * 64) * 2, (i & 1) ? rv0 ^ 64 : rv0, lds0 + L_RES + (lw * 4 + i) * 1024); }
    G2.issueB<false>(0); G2.issueB<false>(1);
    __builtin_amdgcn_s_setprio(3);
    waitvm<8>(); U.finish(ruv, lw, lane, lds0 + L_UVT); S.finish(lw, lane, lds0 + L_STT);
    __builtin_amdgcn_s_setprio((EV & 8) ? 3 : 0);
    lbar();
    bf16x8 wf[2];
    UNR for (int c = 0; c < 2; c++) wf[c] = wfrag(lds0 + L_WPHI + (lw & 1) * 2048 + (4 * g + q) * 64 + c * 32 + p * 8, 1024);
    bar();
    { const unsigned trw = lds0 + L_TR + lw * 8192 + r * 64 + ((g ^ ((r >> 1) & 3)) * 16); bf16x8 a, b, c, d;
      feat2(wf, ldsr<bf16x8>(lds0 + L_KX + lw * 2048 + lane * 16), ldsr<bf16x8>(lds0 + L_KX + lw * 2048 + 1024 + lane * 16), a, b, c, d);
      ldsw<bf16x8>(trw + 2048, a); ldsw<bf16x8>(trw + 2048 + 4096, b); ldsw<bf16x8>(trw + 3072, c); ldsw<bf16x8>(trw + 3072 + 4096, d); }
    lbar();
    __builtin_amdgcn_s_setprio(3);
    const int hd = lw >> 1, sg = lw & 1;
    const u32x4 one4 = {0x3f803f80u, 0x3f803f80u, 0x3f803f80u, 0x3f803f80u}; const bf16x8 ones = __builtin_bit_cast(bf16x8, one4);
    const unsigned tra = lds0 + L_TR + hd * 8192 + sg * 4096 + (4 * g + q) * 64 + ((p ^ (((4 * g + q) >> 1) & 3)) * 16), vfa = lds0 + L_VF + hd * 4096 + lane * 16;
    f32x4 kv[2][2] = {{zero, zero}, {zero, zero}}, z[2] = {zero, zero};
    UNR for (int k = 0; k < 4; k++) { const bf16x8 v0 = ldsr<bf16x8>(vfa + (k >> 1) * 8192 + (k & 1) * 2048), v1 = ldsr<bf16x8>(vfa + (k >> 1) * 8192 + (k & 1) * 2048 + 1024);
      UNR for (int ft = 0; ft < 2; ft++) { const bf16x8 af = wfrag(tra + (k >> 1) * 16384 + (k & 1) * 2048 + ft * 8, 1024);
        kv[ft][0] = mfma(af, v0, kv[ft][0]); kv[ft][1] = mfma(af, v1, kv[ft][1]); z[ft] = mfma(af, ones, z[ft]); } }
    const rsrc_t rk = mkrsrc(ws, WS_KV1 + (size_t)(B.tn * 4 + lw) * 32768), rz = mkrsrc(ws, WS_ZP + (size_t)(B.tn * 4 + lw) * 2048), r2 = mkrsrc(ws, WS_KV2 + (size_t)B.tn * 131072);
    UNR for (int c = 0; c < 2; c++) st_dev(pack8u(kv[0][c], kv[1][c]), rk, lane * 16, B.tm * 2048 + c * 1024);
    { const bool o = lane & 1; const f32x4 zs = {o ? z[1][0] : z[0][0], o ? z[1][1] : z[0][1], o ? z[1][2] : z[0][2], o ? z[1][3] : z[0][3]};
      if ((lane & 14) == 0) st_dev(__builtin_bit_cast(u32x4, zs), rz, (lane & 1) * 64 + g * 16, B.tm * 128); }
    STAMP(16);
    { const f32x4 d = gemv64(lds0 + L_REST + lw * 8192, ldsr<bf16x8>(lds0 + L_SIL2 + g * 16), ldsr<bf16x8>(lds0 + L_SIL2 + 64 + g * 16), lane);
      if (r < 4) st_dev(__builtin_bit_cast(u32x4, d), mkrsrc(ws, WS_MODP + (size_t)((lw * 16 + B.tn) * 16 + B.tm) * 256), r * 64 + g * 16, 0); }
    u32x4 h[4]; poll(h, [&](int i) { return i < 2 ? ld_dev(rk, (lane >> 3) * 2048 + (lane & 7) * 16, B.tm * 128 + i * 16384) : ld_dev(rz, r * 128 + g * 16, (i - 2) * 64); });
    { float e[4], o[4];
      UNR for (int k = 0; k < 4; k++) { e[k] = fadd(blo(h[0][k]), blo(h[1][k])); o[k] = fadd(bhi(h[0][k]), bhi(h[1][k])); }
      float te = fold4<false>(e[0], e[1], e[2], e[3]), to = fold4<false>(o[0], o[1], o[2], o[3]); te += dpp<0x128>(te); to += dpp<0x128>(to);
      spread4(__uint_as_float(cvtpk(te, to)), e[0], e[1], e[2], e[3]);
      const u32x4 sum = {__float_as_uint(e[0]), __float_as_uint(e[1]), __float_as_uint(e[2]), __float_as_uint(e[3])};
      UNR for (int i = 0; i < 2; i++) st_dev(sum, r2, (lane >> 3) * 8192 + (lane & 7) * 16, i * 65536 + lw * 2048 + B.tm * 128); }
    STAMP(17);
    ldsw<bf16x8>(lds0 + L_KX + lw * 1024 + lane * 16, pack8(row16_sum4(asf(h[2])), row16_sum4(asf(h[3]))));
    lbar();
    __builtin_amdgcn_s_setprio((EV & 8) ? 3 : 0);
  }
  const Blk& B = B2; const int odd = B.tn & 1, q4 = (B.tm >> 3) + 2 * ((B.tm >> 2) & 1);
  Loader<1> G3;
  {
    G2.issueB<false>(2); G2.issueB<false>(3);
    panel_sync(ws, 3, B1, B, lw, lane, tag, [&]() {});
    if (blockIdx.x == 0 && lw == 0 && lane == 0) __hip_atomic_store((u64*)(ws + WS_EPOCH), tag, __ATOMIC_RELAXED, __HIP_MEMORY_SCOPE_AGENT);
    STAMP(21);
    G3.init(A.out, 16, A.w1, 0, B.tn * 224 + odd * 32, B.tn * 224 + (odd ? 0 : 192), q4 * 4, B.tm, lw, lane, lds0);
    G2.run<7>([&](int) { return 0; }, [&](int) { return 0; }, [&]() { G3.issueB<false>(0); });
  }
  Loader<3> G4;
  {
    const rsrc_t ruv = mkrsrc(ws, WS_UV2 + B.tn * 16 * 2048);
    UV<1, 4> U; Stats<32> S; const int tu = B.tm & 3, t3 = 5 + (B.j & 7);
    U.prep(lw, lane, lds0 + (tu == 1 ? 0 : tu == 2 ? 1 : 2) * Lay<1>::SBB);
    panel_sync(ws, 4, B, B, lw, lane, tag, [&]() { G3.issueB<false>(1); G3.issueB<false>(2); });
    STAMP(27);
    auto h1pre = [&](int t) {
      if (EV & 16) { const int fp = B.tn * 7 + (t >> 1); st_dev(mark, mkrsrc(ws, WS_H1 + (B.tm * 56 + (fp >> 1)) * 16384), lane * 16, (fp & 1) * 1024 + (lw * 2 + (t & 1)) * 2048); return 1; } return 0; };
    G3.run<8>([&](int t) { int e = 0;
        if (t == 0 || (t < 4 && t == tu)) U.read();
        if (t == 4) { U.publish(ruv, B.tm, lw, lane, lds0); e = 4; }
        if (t < 3) e += h1pre(t);
        return e; },
      [&](int t) { int e = h1pre(t);
        if (t == t3 && !(EV & 4)) { touch(opq((const char*)A.w2) + (size_t)(blockIdx.x * 4 + lw) * 7168, min(lane, 55) * 128, lds0 + L_DUM); e += 1; }
        if ((PT & 4) && t == 11 && lw == 0) { touch_code(lane, lds0 + L_DUM); e += 1; }
        return e; },
      [&]() { U.fetch(opq(ws) + ruv.base, lw, lane); });
    S.fetch(opq(ws) + WS_ST2, B.tm, lw, lane);
    G4.init(ws + WS_H1, 56, A.w2, (B.tn >> 3) * 28, (B.tn & 7) * 128, 0, (B.rot * 28) >> 4, B.tm, lw, lane, lds0);
    G4.issueB<false>(0); G4.issueB<false>(1);
    __builtin_amdgcn_s_setprio(3);
    waitvm<24>(); U.finish(ruv, lw, lane, lds0 + L_UVT); waitvm<8>(); S.finish(lw, lane, lds0 + L_STT);
    __builtin_amdgcn_s_setprio((EV & 8) ? 3 : 0);
    lbar();
  }
  {
    bar();
    G4.issueB<false>(2); G4.issueB<false>(3);
    panel_sync(ws, 5, B, B, lw, lane, tag, [&]() {});
    STAMP(35);
    G4.run<0>([&](int) { return 0; }, [&](int) { return 0; }, [&]() {});
    asm volatile(".Lcode_end:");
  }
}

DI void compute_role(const Args& A, int w, unsigned lds0, unsigned& ts) {
  char* ws = A.ws; const Blk B = blk(); const f32x4 zero = {0, 0, 0, 0}; const u32x4 mark = {~0u, ~0u, ~0u, ~0u};
  const int wm = w >> 1, wn = w & 1; int lane = threadIdx.x & 63; pin(lane);
  {
    const Blk B = blk1(); const int g = lane >> 4, r = lane & 15, q = r >> 2, p = lane & 3;
    bar();
    if (w == 0) { const f32x4 d = gemv64(lds0 + L_SAU, ldsr<bf16x8>(lds0 + L_SIL2 + g * 16), ldsr<bf16x8>(lds0 + L_SIL2 + 64 + g * 16), lane);
      if (r < 4) st_dev(__builtin_bit_cast(u32x4, d), mkrsrc(ws, WS_MODP + (size_t)((64 + B.tn) * 16 + B.tm) * 256), r * 64 + g * 16, 0); }
    bar();
    f32x4 acc[4][6];
    compute_loop<0>(acc, lds0, wm, wn, lane, ts);
    STAMP(13);
    bar();
    STAMP(14);
    constexpr float L2E = 1.4426950408889634f;
    bf16x8 wf[2]; RowSt st[4];
    UNR for (int m = 0; m < 4; m++) st[m].set(lds0 + L_STT + (wm * 64 + m * 16 + r) * 4, L2E);
    const unsigned uva = lds0 + L_UVT + wn * 256;
    bf16x8 kf[4];
    { const f32x4 u0 = ldsr<f32x4>(uva + 512 + g * 16), v0 = ldsr<f32x4>(uva + 576 + g * 16) * L2E, u1 = ldsr<f32x4>(uva + 640 + g * 16), v1 = ldsr<f32x4>(uva + 704 + g * 16) * L2E;
      UNR for (int c = 0; c < 2; c++) wf[c] = wfrag(lds0 + L_WPHI + wn * 2048 + (4 * g + q) * 64 + c * 32 + p * 8, 1024);
      UNR for (int mm = 0; mm < 4; mm++) { const int m = mm ^ 2; kf[m] = pack8(lnf(acc[m][2], st[m], u0, v0), lnf(acc[m][3], st[m], u1, v1));
        if (m >= 2) ldsw<bf16x8>(lds0 + L_KX + w * 2048 + (m - 2) * 1024 + lane * 16, kf[m]);
        if (m == 3) { lbar(); asm volatile("" : "+v"(acc[0][2]), "+v"(acc[0][3]), "+v"(acc[1][2]), "+v"(acc[1][3])); } } }
    { const unsigned trw = lds0 + L_TR + w * 8192 + r * 64 + ((g ^ ((r >> 1) & 3)) * 16); bf16x8 a, b, c, d; feat2(wf, kf[0], kf[1], a, b, c, d);
      ldsw<bf16x8>(trw, a); ldsw<bf16x8>(trw + 4096, b); ldsw<bf16x8>(trw + 1024, c); ldsw<bf16x8>(trw + 1024 + 4096, d); }
    { float u[2], v[2];
      UNR for (int c = 0; c < 2; c++) { u[c] = ldsr<float>(uva + 1024 + c * 128 + r * 4); v[c] = ldsr<float>(uva + 1088 + c * 128 + r * 4); asm("" : "+v"(u[c])); asm("" : "+v"(v[c])); }
      UNR for (int m = 0; m < 4; m++) { const unsigned a = lds0 + L_STT + (wm * 64 + m * 16 + 4 * g) * 4; const f32x4 rs = ldsr<f32x4>(a), nm = ldsr<f32x4>(a + 512);
        UNR for (int c = 0; c < 2; c++) acc[m][4 + c] = fma4(acc[m][4 + c], rs, fma4(nm, spl(u[c]), spl(v[c]))); }
      UNR for (int uu = 0; uu < 2; uu++) UNR for (int c = 0; c < 2; c++) ldsw<bf16x8>(lds0 + L_VF + w * 4096 + uu * 2048 + c * 1024 + lane * 16, pack8(acc[2 * uu][4 + c], acc[2 * uu + 1][4 + c])); }
    lbar();
    STAMP(15);
    asm volatile("" : "+v"(acc[0][0]), "+v"(acc[0][1]), "+v"(acc[1][0]), "+v"(acc[1][1]), "+v"(acc[2][0]), "+v"(acc[2][1]), "+v"(acc[3][0]), "+v"(acc[3][1]));
    bf16x8 qp[4], qn[4];
    { const f32x4 u0 = ldsr<f32x4>(uva + g * 16), v0 = ldsr<f32x4>(uva + 64 + g * 16) * L2E, u1 = ldsr<f32x4>(uva + 128 + g * 16), v1 = ldsr<f32x4>(uva + 192 + g * 16) * L2E;
      UNR for (int m = 0; m < 4; m += 2) feat2(wf, pack8(lnf(acc[m][0], st[m], u0, v0), lnf(acc[m][1], st[m], u1, v1)),
                                               pack8(lnf(acc[m + 1][0], st[m + 1], u0, v0), lnf(acc[m + 1][1], st[m + 1], u1, v1)), qp[m], qn[m], qp[m + 1], qn[m + 1]); }
    asm volatile("" : "+v"(qp[0]), "+v"(qp[1]), "+v"(qp[2]), "+v"(qp[3]), "+v"(qn[0]), "+v"(qn[1]), "+v"(qn[2]), "+v"(qn[3]) :: "memory");
    STAMP(16);
    bar();
    STAMP(17);
    const rsrc_t r2 = mkrsrc(ws, WS_KV2 + (size_t)(B.tn * 16 + B.tm) * 8192 + wn * 4096);
    u32x4 kk[4]; UNR for (int i = 0; i < 4; i++) kk[i] = ld_dev(r2, lane * 16, i * 1024);
    const bf16x8 z0 = ldsr<bf16x8>(lds0 + L_KX + wn * 2048 + lane * 16), z1 = ldsr<bf16x8>(lds0 + L_KX + wn * 2048 + 1024 + lane * 16);
    float rd[4];
    UNR for (int m = 0; m < 4; m++) rd[m] = __builtin_amdgcn_rcpf(mfma(z1, qn[m], mfma(z0, qp[m], zero))[0] + 1e-6f);
    asm volatile("" : "+v"(rd[0]), "+v"(rd[1]), "+v"(rd[2]), "+v"(rd[3]));
    if (__any(max(max(umax4(kk[0]), umax4(kk[1])), max(umax4(kk[2]), umax4(kk[3]))) == 0xffffffffu)) poll(kk, [&](int i) { return ld_dev(r2, lane * 16, i * 1024); });
    const rsrc_t ro = mkrsrc(ws, WS_O + (size_t)(B.tm * 16 + B.tn) * 16384);
    UNR for (int m = 0; m < 4; m++) {
      const f32x4 n0 = mfma(__builtin_bit_cast(bf16x8, kk[2]), qn[m], mfma(__builtin_bit_cast(bf16x8, kk[0]), qp[m], zero)), n1 = mfma(__builtin_bit_cast(bf16x8, kk[3]), qn[m], mfma(__builtin_bit_cast(bf16x8, kk[1]), qp[m], zero));
      st_dev(pack8u(n0 * rd[m], n1 * rd[m]), ro, lane * 16, (wm * 8 + wn) * 1024 + m * 2048);
    }
    STAMP(18);
    waitvm<0>(); bar();
    STAMP(19);
  }
  pin(lane);
  u32x4 o[4];
  {
    const int g = lane >> 4, r = lane & 15;
    { const float v = modvec(ws, WS_MODP + (size_t)((w == 0 ? 0 : w == 3 ? 1 : 2) * 16 + (w < 2 ? B.tn : ((B.tm >> 3) + 2 * ((B.tm >> 2) & 1)) * 4 + (B.tm & 3))) * 4096, lane), s = (w == 1 || w == 2) ? rbf(1.f + v) : v;
      if (w < 2) ldsw<float>(lds0 + L_GT + w * 256 + modcol(lane) * 4, s); else put_et(lds0, w - 2, modcol(lane), s); }
    f32x4 acc[4][2];
    bar();
    compute_loop<2>(acc, lds0, wm, wn, lane, ts);
    STAMP(23);
    f32x4 gate[2], sc[2]; u32x2 xr[4][2];
    UNR for (int n = 0; n < 2; n++) { gate[n] = ldsr<f32x4>(lds0 + L_GT + wn * 128 + n * 64 + g * 16); sc[n] = ldsr<f32x4>(lds0 + L_GT + 256 + wn * 128 + n * 64 + g * 16); }
    UNR for (int m = 0; m < 4; m++) UNR for (int n = 0; n < 2; n++) xr[m][n] = ldsr<u32x2>(lds0 + L_RES + (wm * 64 + m * 16 + r) * 128 + (((wn * 4 + n * 2 + (g >> 1)) ^ (r >> 1)) * 16) + (g & 1) * 8);
    const size_t tb = (size_t)(B.tm * 16 + B.tn) * 16384; const rsrc_t ry = outrsrc(A.out, tb), rst = mkrsrc(ws, WS_ST2 + (size_t)(B.tm * 32 + B.tn * 2 + wn) * 1024);
    f32x4 v[4][2]; float mean[4], m2[4];
    UNR for (int m = 0; m < 4; m++) {
      o[m] = pack8u(fma4(gate[0], acc[m][0], unp(xr[m][0][0], xr[m][0][1])), fma4(gate[1], acc[m][1], unp(xr[m][1][0], xr[m][1][1]))); v[m][0] = unp(o[m][0], o[m][1]); v[m][1] = unp(o[m][2], o[m][3]);
      st_dev(pack8u(v[m][0] * sc[0], v[m][1] * sc[1]), ry, lane * 16, (wm * 8 + wn) * 1024 + m * 2048); }
    UNR for (int m = 0; m < 4; m++) mean[m] = hsum4(v[m][0] + v[m][1]);
    const float mf = fold4<false>(mean[0], mean[1], mean[2], mean[3]) * (1.f / 32); spread4(mf, mean[0], mean[1], mean[2], mean[3]);
    UNR for (int m = 0; m < 4; m++) { const f32x4 d0 = v[m][0] - mean[m], d1 = v[m][1] - mean[m]; m2[m] = hsum4(fma4(d0, d0, d1 * d1)); }
    st_dev8(__builtin_bit_cast(u32x2, f32x2{mf, fold4<false>(m2[0], m2[1], m2[2], m2[3])}), rst, (wm * 64 + ((g >> 1) + 2 * (g & 1)) * 16 + r) * 8, 0);
    STAMP(24);
    waitvm<0>(); bar();
    STAMP(25);
  }
  pin(lane);
  {
    const int g = lane >> 4, r = lane & 15, odd = B.tn & 1;
    const int fp0 = B.tn * 7 + odd + wn * 3, fph = odd ? B.tn * 7 : B.tn * 7 + 6;
    if (w < 2) ldsw<float>(lds0 + L_GT + w * 256 + modcol(lane) * 4, modvec(ws, WS_MODP + (size_t)(48 + (B.tn & 7) * 2 + w) * 4096, lane));
    u32x2 bb[7];
    UNR for (int n = 0; n < 7; n++) bb[n] = *(const u32x2*)(A.b1 + (n < 6 ? fp0 * 32 + n * 16 : fph * 32 + wn * 16) + 4 * g);
    pin(bb);
    UNR for (int i = 0; i < 4; i++) st_dev(mark, mkrsrc(ws, xchoff(B.tm, B.tn, w)), lane * 16, i * 1024);
    f32x4 acc[4][7];
    bar();
    { const rsrc_t rx1 = mkrsrc(ws, WS_X1 + (size_t)(B.tm * 16 + B.tn) * 16384);
      UNR for (int m = 0; m < 4; m++) st_dev(o[m], rx1, lane * 16, (wm * 8 + wn) * 1024 + m * 2048); }
    compute_loop<1>(acc, lds0, wm, wn, lane, ts);
    STAMP(29);
    bar();
    STAMP(30);
    f32x4 u[7], v[7];
    UNR for (int n = 0; n < 7; n++) { const unsigned a = lds0 + L_UVT + (n < 6 ? wn * 6 + n : 12 + wn) * 128 + g * 16; u[n] = ldsr<f32x4>(a); v[n] = ldsr<f32x4>(a + 64) + unp(bb[n][0], bb[n][1]); }
    const rsrc_t rh = mkrsrc(ws, WS_H1 + (size_t)B.tm * 56 * 16384);
    RowSt st[4]; u32x2 h6[4];
    UNR for (int m = 0; m < 4; m++) st[m].set(lds0 + L_STT + (wm * 64 + m * 16 + r) * 4, 1.f);
    auto act = [&](int m, int n) { const f32x4 t = __builtin_elementwise_max(lnf(acc[m][n], st[m], u[n], v[n]), zero); return t * t; };
    UNR for (int n = 0; n < 6; n += 2) { const int fp = fp0 + (n >> 1);
      UNR for (int m = 0; m < 4; m++) st_dev(pack8u(act(m, n), act(m, n + 1)), rh, lane * 16, (fp >> 1) * 16384 + (fp & 1) * 1024 + wm * 8192 + m * 2048); }
    UNR for (int m = 0; m < 4; m++) { const f32x4 t = act(m, 6); h6[m] = u32x2{cvtpk(t[0], t[1]), cvtpk(t[2], t[3])}; }
    UNR for (int m = 0; m < 4; m += 2) { const auto a = __builtin_amdgcn_permlane16_swap(h6[m][0], h6[m + 1][0], false, false), b = __builtin_amdgcn_permlane16_swap(h6[m][1], h6[m + 1][1], false, false);
      st_dev(u32x4{a[0], b[0], a[1], b[1]}, rh, (g & 1) * 2048 + (g >> 1) * 256 + r * 16, (fph >> 1) * 16384 + (fph & 1) * 1024 + wm * 8192 + m * 2048 + wn * 512); }
    STAMP(31);
    bar(); waitvm<0>(); bar();
    STAMP(32);
  }
  pin(lane);
  {
    const int g = lane >> 4, r = lane & 15, kh = B.tn >> 3, cg = (B.tn & 7) * 2 + wn;
    u32x2 bb[4]; u32x4 xr[2][2]; const rsrc_t rx1 = mkrsrc(ws, WS_X1 + (size_t)(B.tm * 16 + cg) * 16384);
    UNR for (int n = 0; n < 4; n++) bb[n] = *(const u32x2*)(A.b2 + cg * 64 + n * 16 + 4 * g);
    pin(bb);
    f32x4 acc[4][4];
    bar();
    UNR for (int mm = 0; mm < 2; mm++) UNR for (int ks = 0; ks < 2; ks++) xr[mm][ks] = ld_dev(rx1, lane * 16, ((wm * 4 + kh * 2 + mm) * 2 + ks) * 1024);
    compute_loop<3>(acc, lds0, wm, wn, lane, ts);
    STAMP(37);
    pin(xr[0]); pin(xr[1]);
    const rsrc_t rx = mkrsrc(ws, xchoff(B.tm, B.tn, w)), rp = mkrsrc(ws, xchoff(B.tm, B.tn ^ 8, w));
    f32x4 keep[2][4], gate[4];
    if (kh) { UNR for (int i = 0; i < 4; i++) st_dev(pack8u(acc[i >> 1][(i & 1) * 2], acc[i >> 1][(i & 1) * 2 + 1]), rx, lane * 16, i * 1024);
              UNR for (int n = 0; n < 4; n++) { keep[0][n] = acc[2][n]; keep[1][n] = acc[3][n]; } }
    else    { UNR for (int i = 0; i < 4; i++) st_dev(pack8u(acc[2 + (i >> 1)][(i & 1) * 2], acc[2 + (i >> 1)][(i & 1) * 2 + 1]), rx, lane * 16, i * 1024);
              UNR for (int n = 0; n < 4; n++) { keep[0][n] = acc[0][n]; keep[1][n] = acc[1][n]; } }
    UNR for (int n = 0; n < 4; n++) gate[n] = ldsr<f32x4>(lds0 + L_GT + wn * 256 + n * 64 + g * 16);
    UNR for (int mm = 0; mm < 2; mm++) UNR for (int n = 0; n < 4; n++) keep[mm][n] = fma4(gate[n], keep[mm][n] + unp(bb[n][0], bb[n][1]), unp(xr[mm][n >> 1][(n & 1) * 2], xr[mm][n >> 1][(n & 1) * 2 + 1]));
    pin(keep[0]); pin(keep[1]);
    u32x4 pp[4]; poll(pp, [&](int i) { return ld_dev(rp, lane * 16, i * 1024); });
    STAMP(38);
    const unsigned ot = lds0 + L_OT + w * 4096;
    UNR for (int mm = 0; mm < 2; mm++) UNR for (int n = 0; n < 4; n++) { const u32x4 t = pp[mm * 2 + (n >> 1)]; const f32x4 y = fma4(gate[n], unp(t[(n & 1) * 2], t[(n & 1) * 2 + 1]), keep[mm][n]);
      ldsw<u32x2>(ot + (mm * 16 + r) * 128 + (((n * 2 + (g >> 1)) ^ (r >> 1)) * 16) + (g & 1) * 8, u32x2{cvtpk(y[0], y[1]), cvtpk(y[2], y[3])}); }
    lwait();
    const int r8 = lane >> 3; const unsigned rv0 = r8 * 2048 + (((lane & 7) ^ (r8 >> 1)) * 16);
    char* op = (char*)A.out + ((size_t)(B.tm * 128 + wm * 64 + kh * 32) * D_ + cg * 64) * 2;
    UNR for (int i = 0; i < 4; i++) *(u32x4*)(op + i * 16384 + ((i & 1) ? rv0 ^ 64 : rv0)) = ldsr<u32x4>(ot + i * 1024 + lane * 16);
    STAMP(39);
  }
}

__global__ __launch_bounds__(512, 2) void k_block(Args A) {
  __shared__ __attribute__((aligned(1024))) char smem[L_TOT];
  const int w = __builtin_amdgcn_readfirstlane(threadIdx.x >> 6);
  const unsigned lds0 = (unsigned)(size_t)(LAS char*)smem;
  asm volatile(".Lcode_begin:");
  if ((PT & 2) && w == 3) touch_code(threadIdx.x & 63, lds0 + L_DUM);
  unsigned ts = 0; STAMP(0);
  const u64 tag = phase0(A, w, lds0, ts);
  if (w >= 4) loader_role(A, w - 4, lds0, tag, ts); else compute_role(A, w, lds0, ts);
  if (blockIdx.x == DIAGB && w == 0) { const unsigned a = __builtin_amdgcn_readlane(ts, DIAG0), b = __builtin_amdgcn_readlane(ts, DIAG), s0 = __builtin_amdgcn_readlane(ts, 0);
    const unsigned until = s0 + 10000 + 10 * (b - a);
    while ((int)(until - (unsigned)__builtin_amdgcn_s_memrealtime()) > 0) __builtin_amdgcn_s_sleep(1); }
}

extern "C" void kernel_launch(void* const* d_in, const int* in_sizes, int n_in, void* d_out, int out_size, void* d_ws, size_t ws_size, hipStream_t stream) {
  Args A; A.x = (const u16*)d_in[0]; A.wqkv = (const u16*)d_in[1]; A.wphi = (const u16*)d_in[2]; A.wo = (const u16*)d_in[3]; A.w1 = (const u16*)d_in[5]; A.b1 = (const u16*)d_in[6];
  A.w2 = (const u16*)d_in[7]; A.b2 = (const u16*)d_in[8]; A.c = (const u16*)d_in[9]; A.wm = (const u16*)d_in[10]; A.out = (u16*)d_out; A.ws = (char*)d_ws;
  k_block<<<256, 512, 0, stream>>>(A);
}
```

```cpp
#include <hip/hip_runtime.h>
#include <hip/hip_bf16.h>

typedef unsigned short u16;
typedef unsigned u32x4 __attribute__((ext_vector_type(4)));
typedef unsigned u32x2 __attribute__((ext_vector_type(2)));
typedef short s16x4 __attribute__((ext_vector_type(4)));
typedef __bf16 bf16x8 __attribute__((ext_vector_type(8)));
typedef __bf16 bf16x2 __attribute__((ext_vector_type(2)));
typedef float f32x2 __attribute__((ext_vector_type(2)));
typedef float f32x4 __attribute__((ext_vector_type(4)));
#define LAS __attribute__((address_space(3)))
#define DI __device__ __forceinline__
#define SB __builtin_amdgcn_sched_barrier(0)
#define UNR _Pragma("unroll")

constexpr int D_ = 1024, F_ = 3584, MODN = 6 * D_;
constexpr size_t WS_EPOCH = 0;
constexpr size_t WS_S1    = 4096;
constexpr size_t WS_FLAG  = 1 << 16;
constexpr size_t WS_ST1   = 1 << 19;
constexpr size_t WS_ST2   = WS_ST1 + (1 << 18);
constexpr size_t WS_MODP  = 3 << 19;
constexpr size_t WS_UV1   = WS_MODP + 5 * 65536;
constexpr size_t WS_ZP    = WS_UV1 + 16 * 12 * 2048;
constexpr size_t WS_UV2   = WS_ZP + (1 << 17);
constexpr size_t WS_KV1   = 10 << 20;
constexpr size_t WS_KV2   = WS_KV1 + (2 << 20);
constexpr size_t WS_O     = 14 << 20;
constexpr size_t WS_X1    = 18 << 20;
constexpr size_t WS_H1    = 22 << 20;
constexpr size_t WS_STAMP = 40 << 20;
constexpr int SPIN_CAP = 1 << 14;
typedef unsigned long long u64;

#define STAMP(i) do { } while (0)

DI void glds16(const void* sbase, unsigned voff, unsigned ldsaddr) {
  asm volatile("s_mov_b32 m0, %2\n\ts_nop 0\n\tglobal_load_lds_dwordx4 %0, %1" :: "v"(voff), "s"(sbase), "s"(ldsaddr) : "memory");
}
DI void glds16nt(const void* sbase, unsigned voff, unsigned ldsaddr) {
  asm volatile("s_mov_b32 m0, %2\n\ts_nop 0\n\tglobal_load_lds_dwordx4 %0, %1 nt" :: "v"(voff), "s"(sbase), "s"(ldsaddr) : "memory");
}
DI void glds16x4(const void* sbase, unsigned voff, unsigned ldsaddr) {
  asm volatile("s_mov_b32 m0, %2\n\ts_nop 0\n\tglobal_load_lds_dwordx4 %0, %1 sc1\n\tglobal_load_lds_dwordx4 %0, %1 offset:1024 sc1\n\t"
               "global_load_lds_dwordx4 %0, %1 offset:2048 sc1\n\tglobal_load_lds_dwordx4 %0, %1 offset:3072 sc1" :: "v"(voff), "s"(sbase), "s"(ldsaddr) : "memory");
}
DI void touch(const void* sbase, unsigned voff, unsigned ldsdummy) {
  asm volatile("s_mov_b32 m0, %2\n\ts_nop 0\n\tglobal_load_lds_dword %0, %1" :: "v"(voff), "s"(sbase), "s"(ldsdummy) : "memory");
}
DI void touch_any(const void* p, unsigned ldsdummy) {
  asm volatile("s_mov_b32 m0, %1\n\ts_nop 0\n\tglobal_load_lds_dword %0, off" :: "v"(p), "s"(ldsdummy) : "memory");
}
DI void touch_nt(const void* sbase, unsigned voff, unsigned ldsdummy) {
  asm volatile("s_mov_b32 m0, %2\n\ts_nop 0\n\tglobal_load_lds_dword %0, %1 nt" :: "v"(voff), "s"(sbase), "s"(ldsdummy) : "memory");
}
DI void gld(u32x4& r, const void* sb, unsigned vo) { asm volatile("global_load_dwordx4 %0, %1, %2" : "=v"(r) : "v"(vo), "s"(sb) : "memory"); }
DI void gld_nt(u32x4& r, const void* sb, unsigned vo) { asm volatile("global_load_dwordx4 %0, %1, %2 nt" : "=v"(r) : "v"(vo), "s"(sb) : "memory"); }
DI void gld_dev(u32x4& r, const void* sb, unsigned vo) { asm volatile("global_load_dwordx4 %0, %1, %2 sc1" : "=v"(r) : "v"(vo), "s"(sb) : "memory"); }
DI void gld8(f32x2& r, const void* sb, unsigned vo) { asm volatile("global_load_dwordx2 %0, %1, %2 sc1" : "=v"(r) : "v"(vo), "s"(sb) : "memory"); }
DI void gld4_dev(unsigned& r, const void* sb, unsigned vo) { asm volatile("global_load_dword %0, %1, %2 sc1" : "=v"(r) : "v"(vo), "s"(sb) : "memory"); }
DI void gld4(unsigned& r, const void* sb, unsigned vo) { asm volatile("global_load_dword %0, %1, %2 nt" : "=v"(r) : "v"(vo), "s"(sb) : "memory"); }
template <class T> DI void pin(T& r) { asm volatile("" : "+v"(r)); }
template <class T, int N> DI void pin(T (&r)[N]) { UNR for (int i = 0; i < N; i++) pin(r[i]); }
template <class T> DI T opq(T v) { asm volatile("" : "+s"(v)); return v; }
DI f32x4 asf(u32x4 v) { return __builtin_bit_cast(f32x4, v); }
template <int N> DI void waitvm() { asm volatile("s_waitcnt vmcnt(%0)" :: "i"(N) : "memory"); }
template <int B, int LO = 0, int HI = 12> DI void waitx(int e) {
  if constexpr (LO == HI) waitvm<B + LO>();
  else if constexpr (LO == 0) { if (e == 0) waitvm<B>(); else waitx<B, 1, HI>(e); }
  else { constexpr int MID = (LO + HI + 1) / 2; if (e >= MID) waitx<B, MID, HI>(e); else waitx<B, LO, MID - 1>(e); }
}
DI void waitlgkm0() { __builtin_amdgcn_s_waitcnt(0xC07F); }
DI void lwait() { asm volatile("s_waitcnt lgkmcnt(0)" ::: "memory"); }
DI void bar() { asm volatile("s_barrier" ::: "memory"); }
DI void lbar() { asm volatile("s_waitcnt lgkmcnt(0)\n\ts_barrier" ::: "memory"); }
DI unsigned cvtpk(float a, float b) { f32x2 v = {a, b}; return __builtin_bit_cast(unsigned, __builtin_convertvector(v, bf16x2)); }
DI float rbf(float a) { return __uint_as_float(cvtpk(a, a) << 16); }
DI f32x4 rbf4(f32x4 s) { return f32x4{rbf(s[0]), rbf(s[1]), rbf(s[2]), rbf(s[3])}; }
DI float blo(unsigned u) { return __uint_as_float(u << 16); }
DI float bhi(unsigned u) { return __uint_as_float(u & 0xffff0000u); }
DI f32x4 unp(unsigned a, unsigned b) { return f32x4{blo(a), bhi(a), blo(b), bhi(b)}; }
DI u32x4 pack8u(const f32x4& a, const f32x4& b) { return u32x4{cvtpk(a[0], a[1]), cvtpk(a[2], a[3]), cvtpk(b[0], b[1]), cvtpk(b[2], b[3])}; }
DI bf16x8 pack8(const f32x4& a, const f32x4& b) { return __builtin_bit_cast(bf16x8, pack8u(a, b)); }
DI f32x4 mfma(bf16x8 a, bf16x8 b, f32x4 c) { return __builtin_amdgcn_mfma_f32_16x16x32_bf16(a, b, c, 0, 0, 0); }
DI s16x4 trd(unsigned a) { return __builtin_amdgcn_ds_read_tr16_b64_v4i16((LAS s16x4*)(size_t)a); }
DI bf16x8 cat(s16x4 lo, s16x4 hi) { return __builtin_bit_cast(bf16x8, __builtin_shufflevector(lo, hi, 0, 1, 2, 3, 4, 5, 6, 7)); }
template <class T> DI T ldsr(unsigned a) { return *(const LAS T*)(size_t)a; }
template <class T> DI void ldsw(unsigned a, T v) { *(LAS T*)(size_t)a = v; }
template <int C> DI float dpp(float x) { return __builtin_bit_cast(float, __builtin_amdgcn_update_dpp(0, __builtin_bit_cast(int, x), C, 0xf, 0xf, true)); }
DI float vmax(float a, float b) { float r; asm("v_max_f32_e32 %0, %1, %2" : "=v"(r) : "v"(a), "v"(b)); return r; }
DI float sw32_add(float a, float b) { auto r = __builtin_amdgcn_permlane32_swap(__float_as_uint(a), __float_as_uint(b), false, false); return __uint_as_float(r[0]) + __uint_as_float(r[1]); }
DI float sw16_add(float a, float b) { auto r = __builtin_amdgcn_permlane16_swap(__float_as_uint(a), __float_as_uint(b), false, false); return __uint_as_float(r[0]) + __uint_as_float(r[1]); }
DI float xg_sum(float a) { a = sw32_add(a, a); return sw16_add(a, a); }
DI float xg_max(float a) {
  auto r = __builtin_amdgcn_permlane32_swap(__float_as_uint(a), __float_as_uint(a), false, false); a = vmax(__uint_as_float(r[0]), __uint_as_float(r[1]));
  auto q = __builtin_amdgcn_permlane16_swap(__float_as_uint(a), __float_as_uint(a), false, false); return vmax(__uint_as_float(q[0]), __uint_as_float(q[1]));
}
DI float row16_sum(float a) { a += dpp<0x128>(a); a += dpp<0x124>(a); a += dpp<0x122>(a); a += dpp<0x121>(a); return a; }
DI f32x4 row16_sum4(f32x4 a) { return f32x4{row16_sum(a[0]), row16_sum(a[1]), row16_sum(a[2]), row16_sum(a[3])}; }
DI f32x4 spl(float a) { return f32x4{a, a, a, a}; }
DI f32x4 fma4(f32x4 a, f32x4 b, f32x4 c) { return __builtin_elementwise_fma(a, b, c); }
struct RowSt { float rs, nm; DI void set(unsigned a, float k) { rs = ldsr<float>(a) * k; nm = ldsr<float>(a + 512) * k; asm("" : "+v"(rs)); asm("" : "+v"(nm)); } };
DI f32x4 lnf(f32x4 acc, const RowSt& st, f32x4 u, f32x4 v) { return fma4(acc, spl(st.rs), fma4(u, spl(st.nm), v)); }
DI float fadd(float a, float b) { float r; asm("v_add_f32_e32 %0, %1, %2" : "=v"(r) : "v"(a), "v"(b)); return r; }
DI float hsum4(f32x4 a) { const f32x2 t = f32x2{a[0], a[1]} + f32x2{a[2], a[3]}; float r; asm("v_add_f32_e32 %0, %1, %2" : "=v"(r) : "v"(t[0]), "v"(t[1])); return r; }
struct rsrc_t { __amdgpu_buffer_rsrc_t r; unsigned base; };
DI rsrc_t mkrsrc(const char* ws, size_t off) { return rsrc_t{__builtin_amdgcn_make_buffer_rsrc((void*)ws, 0, 64 << 20, 0x00020000), (unsigned)off}; }
DI rsrc_t outrsrc(const void* out, size_t off) { return rsrc_t{__builtin_amdgcn_make_buffer_rsrc((void*)out, 0, 2048 * D_ * 2, 0x00020000), (unsigned)off}; }
DI unsigned xchoff(int tm, int tn, int w) { return WS_KV1 + (w >> 1) * (2 << 20) + (tn * 16 + tm) * 8192 + (w & 1) * 4096; }
DI u32x4 ld_dev(const rsrc_t& r, unsigned voff, unsigned soff) { return __builtin_amdgcn_raw_buffer_load_b128(r.r, voff, r.base + soff, 16); }
DI void st_dev(u32x4 v, const rsrc_t& r, unsigned voff, unsigned soff) { __builtin_amdgcn_raw_buffer_store_b128(v, r.r, voff + (r.base + soff), 0, 16); }
DI void st_dev8(u32x2 v, const rsrc_t& r, unsigned voff, unsigned soff) { __builtin_amdgcn_raw_buffer_store_b64(v, r.r, voff + (r.base + soff), 0, 16); }
DI unsigned umax4(u32x4 v) { return max(max(v[0], v[1]), max(v[2], v[3])); }
template <int N, class F> DI void poll(u32x4 (&v)[N], F ld) {
  _Pragma("nounroll") for (int spin = 0; spin < SPIN_CAP; spin++) { unsigned m = 0;
    UNR for (int i = 0; i < N; i++) v[i] = ld(i);
    UNR for (int i = 0; i < N; i++) m = max(m, umax4(v[i]));
    if (!__any(m == 0xffffffffu)) break;
    asm volatile("s_sleep 2" ::: "memory");
  }
}
#define EV 0
#define PT 7
#define PG 0
#define K0V 0
DI float fmax3(float a, float b, float c) { return __builtin_elementwise_maximum(__builtin_elementwise_maximum(a, b), c); }
struct Blk { int tm, tn, rot, j, xq; };
DI Blk blk1() { const int xcd = blockIdx.x & 7, j = blockIdx.x >> 3; Blk b; b.tm = j >> 1; b.tn = xcd * 2 + (j & 1); b.rot = b.tm; b.j = j; b.xq = xcd; return b; }
DI Blk blk() { const int xcd = blockIdx.x & 7, j = blockIdx.x >> 3; Blk b; b.tm = (xcd >> 2) * 8 + (j >> 2); b.tn = (xcd & 3) * 4 + (j & 3); b.rot = (b.tm & 7) * 2 + (b.tm >> 3); b.j = j; b.xq = xcd & 3; return b; }
DI unsigned ptile(int c, int lane) { const int row = 4 * (lane >> 4) + ((lane & 15) >> 2); return row * 128 + ((c ^ ((row >> 1) & 3)) * 32) + (lane & 3) * 8; }
DI unsigned htile(int c, int lane) { const int row = 4 * (lane >> 4) + ((lane & 15) >> 2); return row * 64 + ((c ^ ((row >> 2) & 1)) * 32) + (lane & 3) * 8; }
DI bf16x8 wfrag(unsigned a, unsigned ts) { return cat(trd(a), trd(a + ts)); }

DI unsigned silu2(unsigned cw) { const float c0 = blo(cw), c1 = bhi(cw); return cvtpk(c0 * __builtin_amdgcn_rcpf(1.f + __expf(-c0)), c1 * __builtin_amdgcn_rcpf(1.f + __expf(-c1))); }
DI unsigned fragpos(int k) { return (k >> 5) * 64 + ((k >> 2) & 3) * 16 + ((k & 3) + 4 * ((k >> 4) & 1)) * 2; }
DI f32x4 gemv64(unsigned a, bf16x8 s0, bf16x8 s1, int lane) { f32x4 d[4];
  UNR for (int cc = 0; cc < 4; cc++) d[cc] = mfma(wfrag(a + 4096 + ptile(cc, lane), 2048), s1, mfma(wfrag(a + ptile(cc, lane), 2048), s0, f32x4{0, 0, 0, 0}));
  const bool b0 = lane & 1, b1 = lane & 2; f32x4 o;
  UNR for (int k = 0; k < 4; k++) { const float lo = b0 ? d[1][k] : d[0][k], hi = b0 ? d[3][k] : d[2][k]; o[k] = b1 ? hi : lo; }
  return o;
}

template <int CFG> struct Cfg;
template <> struct Cfg<0> { static constexpr int NT = 6, NP = 3, HALF = 0, NB = 2, NS = 3, N = 3 * D_, KT = 16, B0 = 49152, BS0 = 0; };
template <> struct Cfg<1> { static constexpr int NT = 7, NP = 3, HALF = 1, NB = 7, NS = 3, N = F_, KT = 16, B0 = 65536, BS0 = 2; };
template <> struct Cfg<2> { static constexpr int NT = 2, NP = 1, HALF = 0, NB = 2, NS = 4, N = D_, KT = 16, B0 = 73728, BS0 = 0; };
template <> struct Cfg<3> { static constexpr int NT = 4, NP = 2, HALF = 0, NB = 4, NS = 4, N = D_, KT = 28, B0 = 65536, BS0 = 0; };
template <int CFG> struct Lay { using C = Cfg<CFG>; static constexpr int SBB = C::NP * 8192 + C::HALF * 4096, PB = 2 * C::NP + C::HALF, PER = 4 + PB; };
template <int CFG> DI unsigned btile(int tl, int lane) { using C = Cfg<CFG>; return C::B0 + (tl < 4 * C::NP ? (tl >> 2) * 8192 + ptile(tl & 3, lane) : C::NP * 8192 + htile(tl & 1, lane)); }
constexpr int L_RES = 106496, L_WPHI = 155648, L_UVT = L_WPHI + 4096, L_STT = L_UVT + 2048, L_ET = L_STT + 1024, L_GT = L_ET + 256, L_DUM = L_GT + 512, L_TOT = 163840, L_REST = 122880, L_OT = 131072;
constexpr int L_XT = 131072, L_SAU = 147456, L_SIL = L_WPHI, L_RED = L_SIL + 2048, L_SROW = L_RED + 2048, L_SIL2 = L_GT;
constexpr int L_TR = 0, L_VF = 32768, L_KX = 49152;

template <int CFG> DI void compute_loop(f32x4 (&acc)[4][Cfg<CFG>::NT], unsigned lds0, int wm, int wn, int lane, unsigned& ts) {
  using C = Cfg<CFG>; using L = Lay<CFG>; constexpr int MT = 4, NT = C::NT, NB = C::NB, KT = C::KT;
  const unsigned aoff = lds0 + wm * 8192 + lane * 16; unsigned boff[NB];
  UNR for (int i = 0; i < NB; i++) boff[i] = lds0 + btile<CFG>(CFG == 1 ? (i < 6 ? wn * 6 + i : 12 + wn) : wn * NB + i, lane);
  bf16x8 fa[2][MT]; s16x4 fb[2][NT][2]; unsigned va, vb[NB];
  constexpr int NR = MT + 2 * NT, NM = MT * NT, NU = 1 + NB, FREE = NM - NR, UPG = FREE > 0 ? (NU + FREE - 1) / FREE : NU;
  auto upd = [&](int i, unsigned sa, unsigned sb) { if (i == 0) { va = aoff + sa; pin(va); } else { vb[i - 1] = boff[i - 1] + sb; pin(vb[i - 1]); } };
  auto rd1 = [&](int buf, int ks, int i) {
    if (i == 0 || i > 2 * NT) { const int m = i ? i - 2 * NT : 0; fa[buf][m] = ldsr<bf16x8>(va + m * 2048 + ks * 1024); }
    else { const int n = (i - 1) >> 1, t = (i - 1) & 1;
      fb[buf][n][t] = trd(CFG == 0 ? vb[n & 1] + (n >> 1) * 8192 + ks * 4096 + t * 2048 : (CFG == 1 && n == 6) ? vb[NB - 1] + ks * 2048 + t * 1024 : vb[n] + ks * 4096 + t * 2048); }
  };
  auto step = [&](int buf, int ks, bool doread, bool first, bool doupd, unsigned sa, unsigned sb) {
    UNR for (int i = 0; i < NM; i++) {
      const int m = i / NT, n = i % NT; const bf16x8 b = cat(fb[buf][n][0], fb[buf][n][1]);
      const f32x4 cin = first ? f32x4{0, 0, 0, 0} : acc[m][n];
      acc[m][n] = (CFG == 0 && n >= 4) ? mfma(fa[buf][m], b, cin) : mfma(b, fa[buf][m], cin);
      SB;
      if (doread && i < NR) { rd1(buf ^ 1, ks, i); SB; }
      if (doupd && (FREE > 0 ? i >= NR : i == NM - 1)) { const int u0 = FREE > 0 ? (i - NR) * UPG : 0;
        UNR for (int u = u0; u < u0 + UPG; u++) if (u < NU) upd(u, sa, sb);
        SB; }
    }
  };
  UNR for (int i = 0; i < NU; i++) upd(i, 0, C::BS0 * L::SBB);
  SB; lbar();
  STAMP(CFG == 0 ? 12 : CFG == 2 ? 22 : CFG == 1 ? 28 : 36);
  UNR for (int i = 0; i < NR; i++) rd1(0, 0, i);
  SB;
  unsigned sa = 16384, sb = ((C::BS0 + 1) % C::NS) * L::SBB;
  step(0, 1, true, true, true, sa, sb);
  for (int t = 0; t < KT - 1; t++) {
    waitlgkm0(); bar(); SB;
    sa += 16384; if (sa == C::NS * 16384) sa = 0;
    sb += L::SBB; if (sb == C::NS * L::SBB) sb = 0;
    step(1, 0, true, false, false, 0, 0);
    if (FREE >= 4) { waitlgkm0(); SB; }
    step(0, 1, true, false, true, sa, sb);
  }
  step(1, 0, false, false, false, 0, 0);
}

template <int CFG> struct Loader {
  using C = Cfg<CFG>; using L = Lay<CFG>; static constexpr int N = C::N, KT = C::KT, PER = L::PER, PB = L::PB, NS = C::NS;
  const char *ab, *bb, *hb; unsigned av, bv, hv, la, lb, lh; int ka, kb;
  DI void init(const void* A, int akt, const u16* W, int k0, int col0, int hcol, int rot, int tm, int lw, int lane, unsigned lds0) {
    const int r = lane >> 3, hr = lane >> 2;
    ab = (const char*)A + ((size_t)tm * akt + k0) * 16384 + lw * 4096; av = lane * 16;
    bb = (const char*)W + ((size_t)(k0 * 64 + lw * 16) * N + col0) * 2; bv = r * N * 2 + (((lane & 7) ^ (((r >> 1) & 3) << 1)) * 16);
    hb = (const char*)W + ((size_t)(lw * 16) * N + hcol) * 2; hv = hr * N * 2 + (((lane & 3) ^ (((hr >> 2) & 1) << 1)) * 16);
    if (CFG == 3) { const int t = lw & 1; bb -= (size_t)t * 16 * N * 2; hv = bv + (4 * t + 4 * (r >> 2)) * N * 2; bv += t * 16 * N * 2; }
    la = lds0 + lw * 4096; lb = lds0 + C::B0 + lw * 2048; lh = lds0 + C::B0 + C::NP * 8192 + lw * 1024; ka = kb = rot;
  }
  DI void issueA(int slot) { glds16x4(ab + (size_t)ka * 16384, av, la + slot * 16384); ka++; if (ka == KT) ka = 0; }
  template <bool NT> DI void issueB(int slot) { const char* b = bb + (size_t)kb * 64 * N * 2; slot += C::BS0; if (slot >= NS) slot -= NS; const unsigned so = slot * L::SBB;
    const bool nat = CFG == 3 && ((0x1020408u >> kb) & 1);
    const unsigned v0 = nat ? hv : bv, v1 = v0 + (nat ? 16 : 8) * N * 2;
    UNR for (int i = 0; i < 2 * C::NP; i++) { const char* p = b + (i >> 1) * (CFG == 0 ? 2048 : 128) + (CFG == 3 ? 0 : (i & 1) * (8 * N * 2)); const unsigned l = lb + so + (i >> 1) * 8192 + (i & 1) * 1024, vo = CFG == 3 && (i & 1) ? v1 : v0;
      if (NT) glds16nt(p, vo, l); else glds16(p, vo, l); }
    if (C::HALF) { if (NT) glds16nt(hb + (size_t)kb * 64 * N * 2, hv, lh + so); else glds16(hb + (size_t)kb * 64 * N * 2, hv, lh + so); }
    kb++; if (kb == KT) kb = 0;
  }
  DI void issue(int slot) { issueA(slot); issueB<false>(slot); }
  template <int E1, class HF, class HK, class H1> DI void run(HF hf, HK hk, H1 h1) {
    UNR for (int s = 0; s < NS; s++) issueA(s);
    if (NS == 3) {
      waitvm<8>(); bar(); int e0 = hf(0); waitx<4>(e0); lbar();
      UNR for (int t = 1; t <= 5; t++) { issue((t - 1) % 3); int e1 = hf(t); if (t >= 3) e1 += hk(t); waitx<PER>(e0 + e1); lbar(); e0 = e1; }
      int slot = 2;
      _Pragma("nounroll") for (int t = 6; t <= KT - 3; t++) { issue(slot); slot++; if (slot == NS) slot = 0; const int e1 = hk(t); waitx<PER>(e0 + e1); lbar(); e0 = e1; }
    } else {
      waitvm<12>(); bar(); waitvm<8>(); bar();
      issue(0); waitvm<4 + PER>(); bar();
      int slot = 1;
      for (int t = 2; t <= KT - 4; t++) { issue(slot); slot = (slot + 1) & 3; waitvm<2 * PER>(); bar(); }
      waitvm<PER>(); bar();
    }
    h1(); waitvm<E1>(); bar();
  }
};
DI u64* flagp(char* ws, int tile, int line, int word) { return (u64*)(ws + WS_FLAG) + (tile * 6 + line) * 16 + word; }
DI u64 wait_flags(u64* f, u64 tag, u64 need) {
  u64 got = 0;
  _Pragma("nounroll") for (int spin = 0; spin < SPIN_CAP; spin++) {
    got = __ballot(__hip_atomic_load(f, __ATOMIC_RELAXED, __HIP_MEMORY_SCOPE_AGENT) == tag);
    if ((got & need) == need) break;
    asm volatile("s_sleep 1" ::: "memory");
  }
  return got;
}
template <class H> DI bool panel_sync(char* ws, int line, const Blk& P, const Blk& C, int lw, int lane, u64 tag, H pre) {
  bar();
  if (lw == 0) { const int i = lane & 15, grp = lane >> 4;
    if (line == 0) { if (grp < 3) __hip_atomic_store(grp == 0 ? flagp(ws, P.tm * 16 + i, 0, P.tn) : grp == 1 ? flagp(ws, i * 16 + P.tn, 1, P.tm) : flagp(ws, P.tn * 16 + i, 2, P.tm), tag, __ATOMIC_RELAXED, __HIP_MEMORY_SCOPE_AGENT); }
    else if (grp == 0) __hip_atomic_store(flagp(ws, P.tm * 16 + i, line, P.tn), tag, __ATOMIC_RELAXED, __HIP_MEMORY_SCOPE_AGENT); }
  pre();
  bool all = true;
  if (lw == 0) all = wait_flags(flagp(ws, C.tm * 16 + C.tn, line, line == 0 ? min(lane, 47) : lane & 15), tag, 0xffff) == ~0ull;
  bar();
  return all;
}
DI int modcol(int lane) { return 4 * (lane & 15) + 2 * (lane >> 5) + ((lane >> 4) & 1); }
DI float modvec(const char* ws, unsigned part, int lane, u32x4 (&p)[4], bool have) {
  if (!have || __any(max(max(umax4(p[0]), umax4(p[1])), max(umax4(p[2]), umax4(p[3]))) == 0xffffffffu)) { const rsrc_t r = mkrsrc(ws, part); poll(p, [&](int i) { return ld_dev(r, lane * 16, i * 1024); }); }
  const f32x4 a = (asf(p[0]) + asf(p[1])) + (asf(p[2]) + asf(p[3]));
  return rbf(sw16_add(sw32_add(a[0], a[2]), sw32_add(a[1], a[3])));
}
DI float modvec(const char* ws, unsigned part, int lane) { u32x4 p[4]; return modvec(ws, part, lane, p, false); }
DI void put_et(unsigned lds0, int vec, int k, float v) { ldsw<u16>(lds0 + L_ET + vec * 128 + fragpos(k), (u16)(__float_as_uint(v) >> 16)); }

template <int CFG, int NTL> struct UV {
  bf16x8 f[NTL][2]; unsigned ad[NTL], ts[NTL];
  DI void prep(int lw, int lane, unsigned base) { UNR for (int i = 0; i < NTL; i++) { const int tl = min(lw * NTL + i, 2 * Cfg<CFG>::NT - 1); ad[i] = base + btile<CFG>(tl, lane); ts[i] = tl < 4 * Cfg<CFG>::NP ? 2048 : 1024; pin(ad[i]); } }
  DI void read() { UNR for (int i = 0; i < NTL; i++) { f[i][0] = wfrag(ad[i], ts[i]); f[i][1] = wfrag(ad[i] + 2 * ts[i], ts[i]); } }
  DI void publish(const rsrc_t& r, int tm, int lw, int lane, unsigned lds0) {
    const bf16x8 e0 = ldsr<bf16x8>(lds0 + L_ET + (lane & 1) * 128 + (lane >> 4) * 16), e1 = ldsr<bf16x8>(lds0 + L_ET + (lane & 1) * 128 + 64 + (lane >> 4) * 16);
    f32x4 d[NTL];
    UNR for (int i = 0; i < NTL; i++) d[i] = mfma(f[i][1], e1, mfma(f[i][0], e0, f32x4{0, 0, 0, 0}));
    if ((lane & 14) == 0) UNR for (int i = 0; i < NTL; i++) st_dev(__builtin_bit_cast(u32x4, d[i]), r, (lane & 1) * 64 + (lane >> 4) * 16, (lw * NTL + i) * 2048 + tm * 128);
  }
  u32x4 v[2 * NTL];
  DI void fetch(const char* base, int lw, int lane) { UNR for (int i = 0; i < 2 * NTL; i++) gld_dev(v[i], base + lw * NTL * 2048 + i * 1024, lane * 16); }
  DI void finish(const rsrc_t& r, int lw, int lane, unsigned tab) {
    pin(v); unsigned mx = 0;
    UNR for (int i = 0; i < 2 * NTL; i++) mx = max(mx, umax4(v[i]));
    if (__any(mx == 0xffffffffu)) poll(v, [&](int i) { return ld_dev(r, lane * 16, lw * NTL * 2048 + i * 1024); });
    UNR for (int i = 0; i < NTL; i++) { const f32x4 a = asf(v[2 * i]) + asf(v[2 * i + 1]);
      float t = sw16_add(sw32_add(a[0], a[2]), sw32_add(a[1], a[3])); t += dpp<0x128>(t);
      ldsw<float>(tab + (lw * NTL + i) * 128 + (lane & 7) * 16 + (2 * (lane >> 5) + ((lane >> 4) & 1)) * 4, t); }
  }
};
template <int NPART> struct Stats {
  f32x2 v[NPART / 2];
  DI void fetch(const char* st, int tm, int lw, int lane) { UNR for (int i = 0; i < NPART / 2; i++) gld8(v[i], st + (size_t)(tm * NPART + i) * 1024 + lw * 256, (lane >> 5) * (NPART * 512) + (lane & 31) * 8); }
  DI void finish(int lw, int lane, unsigned tab) { pin(v); f32x2 s = v[0]; float q = 0;
    UNR for (int i = 1; i < NPART / 2; i++) s += v[i];
    const float mean = sw32_add(s[0], s[0]) * (1.f / NPART);
    UNR for (int i = 0; i < NPART / 2; i++) { const float d = v[i][0] - mean; q += d * d; }
    q = s[1] + (float)(D_ / NPART) * q;
    const float rstd = __builtin_amdgcn_rsqf(sw32_add(q, q) * (1.f / D_) + 1e-5f);
    ldsw<float>(tab + (lane >> 5) * 512 + (lw * 32 + (lane & 31)) * 4, lane < 32 ? rstd : -mean * rstd); }
};

template <bool MAX> DI float fold4(float a, float b, float c, float d) {
  auto op = [](unsigned x, unsigned y) { return MAX ? __builtin_elementwise_maximum(__uint_as_float(x), __uint_as_float(y)) : __uint_as_float(x) + __uint_as_float(y); };
  const auto r0 = __builtin_amdgcn_permlane32_swap(__float_as_uint(a), __float_as_uint(b), false, false), r1 = __builtin_amdgcn_permlane32_swap(__float_as_uint(c), __float_as_uint(d), false, false);
  const auto r2 = __builtin_amdgcn_permlane16_swap(__float_as_uint(op(r0[0], r0[1])), __float_as_uint(op(r1[0], r1[1])), false, false);
  return op(r2[0], r2[1]);
}
DI void spread4(float t, float& a, float& b, float& c, float& d) {
  const auto r2 = __builtin_amdgcn_permlane16_swap(__float_as_uint(t), __float_as_uint(t), false, false);
  const auto r0 = __builtin_amdgcn_permlane32_swap(r2[0], r2[0], false, false), r1 = __builtin_amdgcn_permlane32_swap(r2[1], r2[1], false, false);
  a = __uint_as_float(r0[0]); b = __uint_as_float(r0[1]); c = __uint_as_float(r1[0]); d = __uint_as_float(r1[1]);
}
DI void feat2(const bf16x8 (&wf)[2], bf16x8 x0, bf16x8 x1, bf16x8& fp0, bf16x8& fn0, bf16x8& fp1, bf16x8& fn1) {
  const f32x4 zero = {0, 0, 0, 0}; f32x4 p[2][2] = {{mfma(wf[0], x0, zero), mfma(wf[1], x0, zero)}, {mfma(wf[0], x1, zero), mfma(wf[1], x1, zero)}};
  float mx[2], nx[2], sp[2], sn[2]; f32x4 a[2][2], b[2][2];
  UNR for (int i = 0; i < 2; i++) { const f32x4 u = p[i][0], v = p[i][1];
    mx[i] = fmax3(fmax3(fmax3(u[0], u[1], u[2]), u[3], v[0]), fmax3(v[1], v[2], v[3]), v[3]); nx[i] = fmax3(fmax3(fmax3(-u[0], -u[1], -u[2]), -u[3], -v[0]), fmax3(-v[1], -v[2], -v[3]), -v[3]); }
  spread4(fold4<true>(mx[0], mx[1], nx[0], nx[1]), mx[0], mx[1], nx[0], nx[1]);
  UNR for (int i = 0; i < 2; i++) UNR for (int h = 0; h < 2; h++) UNR for (int j = 0; j < 4; j += 2) { const f32x2 t = {p[i][h][j], p[i][h][j + 1]};
    f32x2 d = t - f32x2{mx[i], mx[i]}, e = -t - f32x2{nx[i], nx[i]}; asm("" : "+v"(d), "+v"(e));
    a[i][h][j] = __builtin_amdgcn_exp2f(d[0]); a[i][h][j + 1] = __builtin_amdgcn_exp2f(d[1]); b[i][h][j] = __builtin_amdgcn_exp2f(e[0]); b[i][h][j + 1] = __builtin_amdgcn_exp2f(e[1]); }
  UNR for (int i = 0; i < 2; i++) { sp[i] = hsum4(a[i][0] + a[i][1]); sn[i] = hsum4(b[i][0] + b[i][1]); }
  spread4(__builtin_amdgcn_rcpf(fold4<false>(sp[0], sp[1], sn[0], sn[1])), sp[0], sp[1], sn[0], sn[1]);
  fp0 = pack8(a[0][0] * sp[0], a[0][1] * sp[0]); fn0 = pack8(b[0][0] * sn[0], b[0][1] * sn[0]); fp1 = pack8(a[1][0] * sp[1], a[1][1] * sp[1]); fn1 = pack8(b[1][0] * sn[1], b[1][1] * sn[1]);
}

DI void touch_code(int lane, unsigned ldsdummy) {
  const char* pc; unsigned len, back;
  asm volatile("s_getpc_b64 %0\n.Lhere%=:\n\ts_mov_b32 %2, .Lhere%=-.Lcode_begin\n\ts_mov_b32 %1, .Lcode_end-.Lcode_begin" : "=s"(pc), "=s"(len), "=s"(back));
  pc -= back;
  const unsigned o = ((blockIdx.x >> 3) * 16 + (lane & 15)) * 128;
  touch(pc, min(o, len), ldsdummy);
}
struct Args { const u16 *x, *wqkv, *wphi, *wo, *w1, *b1, *w2, *b2, *c, *wm; u16* out; char* ws; };

DI u64 phase0(const Args& A, int w, unsigned lds0, unsigned& ts) {
  char* ws = A.ws; const Blk B = blk1(); const f32x4 zero = {0, 0, 0, 0};
  const int lane = threadIdx.x & 63, r8 = lane >> 3, g = lane >> 4, r = lane & 15;
  unsigned cw, cw2; gld4(cw, A.c, w * 256 + ((B.tm * 16 + lane * 4) & 255)); gld4(cw2, A.c, B.tm * 128 + (lane & 31) * 4);
  const char* wb = (const char*)A.wm + B.tn * 128; const unsigned wv = r8 * MODN * 2 + (((lane & 7) ^ (((r8 >> 1) & 3) << 1)) * 16);
  auto piece = [&](int i) { glds16(wb + (size_t)(w * 16 + ((B.tm + i) & 15)) * 16 * MODN + 2 * D_, wv, lds0 + (w * 16 + i) * 1024); };
  glds16nt(wb + (size_t)(w * 16 + B.tm) * 16 * MODN + 2 * D_, wv, lds0 + w * 16384);
  const unsigned rv0 = r8 * 2048 + (((lane & 7) ^ (r8 >> 1)) * 16);
  UNR for (int i = 0; i < 2; i++) glds16nt((const char*)A.x + ((size_t)(B.tm * 128 + w * 16 + i * 8) * D_ + B.tn * 64) * 2, i ? rv0 ^ 64 : rv0, lds0 + L_XT + (w * 2 + i) * 1024);
  bar();
  piece(1); piece(2);
  if (w == 7 && (PT & 1)) { const Blk C = blk(); if (lane < 6) touch_any(flagp(ws, lane < 3 ? B.tm * 16 + B.tn : C.tm * 16 + C.tn, lane, 0), lds0 + L_DUM); }
  if (w < 3) {
    const int l = w * 8 + r8;
    if (l < 22) st_dev(u32x4{~0u, ~0u, ~0u, ~0u}, mkrsrc(ws, WS_MODP), (l < 10 ? (((l >> 1) * 16 + B.tn) * 16 + B.tm) * 256 + (l & 1) * 128 : 5 * 65536 + (B.tn * 12 + l - 10) * 2048 + B.tm * 128) + (lane & 7) * 16, 0); }
  waitvm<5>(); pin(cw); pin(cw2);
  ldsw<unsigned>(lds0 + L_SIL + w * 256 + fragpos(2 * lane), silu2(cw));
  if (w == 7) ldsw<unsigned>(lds0 + L_SIL2 + fragpos(2 * (lane & 31)), silu2(cw2));
  unsigned pa[4]; UNR for (int cc = 0; cc < 4; cc++) { pa[cc] = lds0 + w * 16384 + ptile(cc, lane); pin(pa[cc]); }
  STAMP(1);
  f32x4 a[4] = {zero, zero, zero, zero};
  auto kstep = [&](int ks) { const bf16x8 sf = ldsr<bf16x8>(lds0 + L_SIL + (w * 4 + ks) * 64 + g * 16);
    UNR for (int cc = 0; cc < 4; cc++) a[cc] = mfma(wfrag(pa[cc] + ks * 4096, 2048), sf, a[cc]); };
  UNR for (int i = 3; i < 10; i++) piece(i);
  waitvm<6>();
  u32x2 xt[4];
  UNR for (int cc = 0; cc < 4; cc++) xt[cc] = ldsr<u32x2>(lds0 + L_XT + (w * 16 + r) * 128 + (((cc * 2 + (g >> 1)) ^ (r >> 1)) * 16) + (g & 1) * 8);
  kstep(0);
  UNR for (int i = 10; i < 16; i++) piece(i);
  STAMP(2);
  bar();
  glds16nt(wb + (size_t)(B.tm * 8 + w) * 16 * MODN, wv, lds0 + L_SAU + w * 1024);
  { const int e = min(w * 24 + lane, 191), row = e / 6, l = e - row * 6;
    if (lane < 24) touch_nt((const char*)A.wqkv + (size_t)B.j * 32 * 6144 + B.xq * 256, row * 6144 + (l >> 1) * 2048 + (l & 1) * 128, lds0 + L_DUM); }
  f32x2 ep; gld8(ep, ws + WS_EPOCH, 0);
  waitvm<11>(); kstep(1); waitvm<7>(); kstep(2); waitvm<3>(); kstep(3);
  if (r == 0) UNR for (int cc = 0; cc < 4; cc++) ldsw<f32x4>(lds0 + L_RED + w * 256 + cc * 64 + g * 16, a[cc]);
  STAMP(3);
  lbar();
  f32x4 xv[4]; UNR for (int cc = 0; cc < 4; cc++) xv[cc] = unp(xt[cc][0], xt[cc][1]);
  float s = 0;
  UNR for (int i = 0; i < 8; i++) s += ldsr<float>(lds0 + L_RED + i * 256 + lane * 4);
  s = rbf(1.f + rbf(s)); ldsw<float>(lds0 + L_SROW + w * 256 + lane * 4, s);
  asm volatile("" ::: "memory");
  const rsrc_t ry = outrsrc(A.out, (B.tm * 16 + B.tn) * 16384);
  UNR for (int ks = 0; ks < 2; ks++)
    st_dev(pack8u(xv[2 * ks] * ldsr<f32x4>(lds0 + L_SROW + w * 256 + ks * 128 + g * 16), xv[2 * ks + 1] * ldsr<f32x4>(lds0 + L_SROW + w * 256 + ks * 128 + 64 + g * 16)), ry, lane * 16, w * 2048 + ks * 1024);
  STAMP(4);
  if (w == 6 && B.tm == B.tn) __hip_atomic_store((float*)(ws + WS_S1) + B.tn * 64 + lane, s, __ATOMIC_RELAXED, __HIP_MEMORY_SCOPE_AGENT);
  { const float mean = xg_sum(hsum4((xv[0] + xv[1]) + (xv[2] + xv[3]))) * (1.f / 64); f32x4 q = zero;
    UNR for (int cc = 0; cc < 4; cc++) { const f32x4 d = xv[cc] - mean; q = fma4(d, d, q); }
    const float m2 = xg_sum(hsum4(q));
    if (g == 0) st_dev8(__builtin_bit_cast(u32x2, f32x2{mean, m2}), mkrsrc(ws, WS_ST1 + (size_t)(B.tm * 16 + B.tn) * 1024), r * 8, w * 128); }
  waitvm<0>(); pin(ep);
  STAMP(5);
  return __builtin_bit_cast(u64, ep) + 0x9E3779B97F4A7C15ull;
}

DI void loader_role(const Args& A, int lw, unsigned lds0, u64 tag, unsigned& ts) {
  char* ws = A.ws; const Blk B2 = blk(), B1 = blk1(); const f32x4 zero = {0, 0, 0, 0}; const u32x4 mark = {~0u, ~0u, ~0u, ~0u};
  int lane = threadIdx.x & 63; pin(lane);
  Loader<2> G2;
  {
    const Blk& B = B1; const int g = lane >> 4, r = lane & 15, q = r >> 2, p = lane & 3, r8 = lane >> 3;
    Loader<0> G; G.init(A.out, 16, A.wqkv, 0, B.tn * 64, 0, B.rot, B.tm, lw, lane, lds0);
    const rsrc_t ruv = mkrsrc(ws, WS_UV1 + B.tn * 12 * 2048);
    UV<0, 3> U; Stats<16> S; U.prep(lw, lane, lds0);
    const bool all0 = panel_sync(ws, 0, B, B, lw, lane, tag, [&]() { UNR for (int s = 0; s < 3; s++) G.issueB<false>(s); });
    STAMP(11);
    const int t1 = 3 + (B.tm * 5 >> 3), t3 = 3 + (((B.tm + 8) & 15) * 5 >> 3);
    const char* sap = ws + WS_MODP + (size_t)(64 + B.rot) * 4096; u32x4 sp[4]; unsigned s1;
    G.run<15>([&](int t) { int e = 0;
        if (t == 0) { U.read(); if (!all0) wait_flags(flagp(ws, B.tm * 16 + B.tn, 1, lane & 31), tag, ~0ull); }
        if (t == 1) { UNR for (int i = 0; i < 4; i++) gld_dev(sp[i], sap + i * 1024, lw == 1 ? lane * 16 : 0); gld4_dev(s1, ws + WS_S1 + B.rot * 256, lane * 4);
          UNR for (int c = 0; c < 2; c++) st_dev(mark, mkrsrc(ws, WS_KV1 + (size_t)(B.tn * 4 + lw) * 32768), lane * 16, B.tm * 2048 + c * 1024);
          e = 7; }
        if (t == 2) { UNR for (int c = 0; c < 2; c++) st_dev(mark, mkrsrc(ws, WS_KV2 + (size_t)(B.tn * 16 + B.tm) * 8192), lane * 16, lw * 2048 + c * 1024);
          if (r8 < 5) st_dev(mark, mkrsrc(ws, WS_ZP), (r8 == 0 ? (B.tn * 4 + lw) * 2048 : (1 << 17) + (B.tn * 16 + lw * 4 + r8 - 1) * 2048) + (lane & 7) * 16, B.tm * 128);
          e = 3; }
        if (t == 3 && PG) { const unsigned i = (B.j * 64 + lane) << PG;
          const char* p = lw == 0 ? ws + WS_KV1 + min(i, 27u << 20) : lw == 1 ? (const char*)A.w1 + min(i, D_ * F_ * 2u - 4) : lw == 2 ? (const char*)A.w2 + min(i, D_ * F_ * 2u - 4) : i < (2u << 20) ? (const char*)A.wo + i : ws + min(i - (2u << 20), 3u << 20);
          touch_any(p, lds0 + L_DUM); e = 1; }
        if (t == 4) { pin(sp); pin(s1); if (lw == 0) put_et(lds0, 0, lane, __uint_as_float(s1)); if (lw == 1) put_et(lds0, 1, modcol(lane), modvec(ws, WS_MODP + (64 + B.rot) * 4096, lane, sp, true)); }
        if (t == 5) { U.publish(ruv, B.tm, lw, lane, lds0); e = 3; }
        return e; },
      [&](int t) { int e = 0;
        if (t == t1 && !(EV & 1)) {
          const char* wb = opq((const char*)A.wm) + (size_t)B.tm * 128 * MODN + (lw + 2) * 2 * D_ + B.tn * 128; const unsigned wv = r8 * MODN * 2 + (((lane & 7) ^ (((r8 >> 1) & 3) << 1)) * 16);
          const unsigned l0 = opq(lds0 + L_REST) + lw * 8192;
          UNR for (int i = 0; i < 8; i++) glds16nt(wb + i * 16 * MODN, wv, l0 + i * 1024);
          e += 8; }
        if (t == t3 && !(EV & 2)) { touch(opq((const char*)A.w1) + (size_t)(blockIdx.x * 4 + lw) * 7168, min(lane, 55) * 128, lds0 + L_DUM); touch(opq((const char*)A.wo) + (size_t)(blockIdx.x * 4 + lw) * 2048, (lane & 15) * 128, lds0 + L_DUM); e += 2; }
        if ((PT & 4) && t == 11 && lw == 0) { touch_code(lane, lds0 + L_DUM); e += 1; }
        return e; },
      [&]() { U.fetch(opq(ws) + ruv.base, lw, lane); S.fetch(opq(ws) + WS_ST1, B.tm, lw, lane); glds16nt((const char*)A.wphi + B.tn * 4096 + lw * 1024, lane * 16, lds0 + L_WPHI + lw * 1024); });
    G2.init(ws + WS_O, 16, A.wo, 0, B2.tn * 64, 0, B2.rot, B2.tm, lw, lane, lds0);
    { const unsigned rv0 = r8 * 2048 + (((lane & 7) ^ (r8 >> 1)) * 16);
      UNR for (int i = 0; i < 4; i++) glds16nt((const char*)A.x + ((size_t)(B2.tm * 128 + (lw * 4 + i) * 8) * D_ + B2.tn * 64) * 2, (i & 1) ? rv0 ^ 64 : rv0, lds0 + L_RES + (lw * 4 + i) * 1024); }
    G2.issueB<false>(0); G2.issueB<false>(1);
    __builtin_amdgcn_s_setprio(3);
    waitvm<8>(); U.finish(ruv, lw, lane, lds0 + L_UVT); S.finish(lw, lane, lds0 + L_STT);
    __builtin_amdgcn_s_setprio((EV & 8) ? 3 : 0);
    lbar();
    bf16x8 wf[2];
    UNR for (int c = 0; c < 2; c++) wf[c] = wfrag(lds0 + L_WPHI + (lw & 1) * 2048 + (4 * g + q) * 64 + c * 32 + p * 8, 1024);
    bar();
    { const unsigned trw = lds0 + L_TR + lw * 8192 + r * 64 + ((g ^ ((r >> 1) & 3)) * 16); bf16x8 a, b, c, d;
      feat2(wf, ldsr<bf16x8>(lds0 + L_KX + lw * 2048 + lane * 16), ldsr<bf16x8>(lds0 + L_KX + lw * 2048 + 1024 + lane * 16), a, b, c, d);
      ldsw<bf16x8>(trw + 2048, a); ldsw<bf16x8>(trw + 2048 + 4096, b); ldsw<bf16x8>(trw + 3072, c); ldsw<bf16x8>(trw + 3072 + 4096, d); }
    lbar();
    __builtin_amdgcn_s_setprio(3);
    const int hd = lw >> 1, sg = lw & 1;
    const u32x4 one4 = {0x3f803f80u, 0x3f803f80u, 0x3f803f80u, 0x3f803f80u}; const bf16x8 ones = __builtin_bit_cast(bf16x8, one4);
    const unsigned tra = lds0 + L_TR + hd * 8192 + sg * 4096 + (4 * g + q) * 64 + ((p ^ (((4 * g + q) >> 1) & 3)) * 16), vfa = lds0 + L_VF + hd * 4096 + lane * 16;
    f32x4 kv[2][2] = {{zero, zero}, {zero, zero}}, z[2] = {zero, zero};
    UNR for (int k = 0; k < 4; k++) { const bf16x8 v0 = ldsr<bf16x8>(vfa + (k >> 1) * 8192 + (k & 1) * 2048), v1 = ldsr<bf16x8>(vfa + (k >> 1) * 8192 + (k & 1) * 2048 + 1024);
      UNR for (int ft = 0; ft < 2; ft++) { const bf16x8 af = wfrag(tra + (k >> 1) * 16384 + (k & 1) * 2048 + ft * 8, 1024);
        kv[ft][0] = mfma(af, v0, kv[ft][0]); kv[ft][1] = mfma(af, v1, kv[ft][1]); z[ft] = mfma(af, ones, z[ft]); } }
    const rsrc_t rk = mkrsrc(ws, WS_KV1 + (size_t)(B.tn * 4 + lw) * 32768), rz = mkrsrc(ws, WS_ZP + (size_t)(B.tn * 4 + lw) * 2048), r2 = mkrsrc(ws, WS_KV2 + (size_t)B.tn * 131072);
    UNR for (int c = 0; c < 2; c++) st_dev(pack8u(kv[0][c], kv[1][c]), rk, lane * 16, B.tm * 2048 + c * 1024);
    { const bool o = lane & 1; const f32x4 zs = {o ? z[1][0] : z[0][0], o ? z[1][1] : z[0][1], o ? z[1][2] : z[0][2], o ? z[1][3] : z[0][3]};
      if ((lane & 14) == 0) st_dev(__builtin_bit_cast(u32x4, zs), rz, (lane & 1) * 64 + g * 16, B.tm * 128); }
    STAMP(16);
    { const f32x4 d = gemv64(lds0 + L_REST + lw * 8192, ldsr<bf16x8>(lds0 + L_SIL2 + g * 16), ldsr<bf16x8>(lds0 + L_SIL2 + 64 + g * 16), lane);
      if (r < 4) st_dev(__builtin_bit_cast(u32x4, d), mkrsrc(ws, WS_MODP + (size_t)((lw * 16 + B.tn) * 16 + B.tm) * 256), r * 64 + g * 16, 0); }
    u32x4 h[4]; poll(h, [&](int i) { return i < 2 ? ld_dev(rk, (lane >> 3) * 2048 + (lane & 7) * 16, B.tm * 128 + i * 16384) : ld_dev(rz, r * 128 + g * 16, (i - 2) * 64); });
    { float e[4], o[4];
      UNR for (int k = 0; k < 4; k++) { e[k] = fadd(blo(h[0][k]), blo(h[1][k])); o[k] = fadd(bhi(h[0][k]), bhi(h[1][k])); }
      float te = fold4<false>(e[0], e[1], e[2], e[3]), to = fold4<false>(o[0], o[1], o[2], o[3]); te += dpp<0x128>(te); to += dpp<0x128>(to);
      spread4(__uint_as_float(cvtpk(te, to)), e[0], e[1], e[2], e[3]);
      const u32x4 sum = {__float_as_uint(e[0]), __float_as_uint(e[1]), __float_as_uint(e[2]), __float_as_uint(e[3])};
      UNR for (int i = 0; i < 2; i++) st_dev(sum, r2, (lane >> 3) * 8192 + (lane & 7) * 16, i * 65536 + lw * 2048 + B.tm * 128); }
    STAMP(17);
    ldsw<bf16x8>(lds0 + L_KX + lw * 1024 + lane * 16, pack8(row16_sum4(asf(h[2])), row16_sum4(asf(h[3]))));
    lbar();
    __builtin_amdgcn_s_setprio((EV & 8) ? 3 : 0);
  }
  const Blk& B = B2; const int odd = B.tn & 1, q4 = (B.tm >> 3) + 2 * ((B.tm >> 2) & 1);
  Loader<1> G3;
  {
    G2.issueB<false>(2); G2.issueB<false>(3);
    panel_sync(ws, 3, B1, B, lw, lane, tag, [&]() {});
    if (blockIdx.x == 0 && lw == 0 && lane == 0) __hip_atomic_store((u64*)(ws + WS_EPOCH), tag, __ATOMIC_RELAXED, __HIP_MEMORY_SCOPE_AGENT);
    STAMP(21);
    G3.init(A.out, 16, A.w1, 0, B.tn * 224 + odd * 32, B.tn * 224 + (odd ? 0 : 192), q4 * 4, B.tm, lw, lane, lds0);
    G2.run<7>([&](int) { return 0; }, [&](int) { return 0; }, [&]() { G3.issueB<false>(0); });
  }
  Loader<3> G4;
  {
    const rsrc_t ruv = mkrsrc(ws, WS_UV2 + B.tn * 16 * 2048);
    UV<1, 4> U; Stats<32> S; const int tu = B.tm & 3, t3 = 5 + (B.j & 7);
    U.prep(lw, lane, lds0 + (tu == 1 ? 0 : tu == 2 ? 1 : 2) * Lay<1>::SBB);
    panel_sync(ws, 4, B, B, lw, lane, tag, [&]() { G3.issueB<false>(1); G3.issueB<false>(2); });
    STAMP(27);
    auto h1pre = [&](int t) {
      if (EV & 16) { const int fp = B.tn * 7 + (t >> 1); st_dev(mark, mkrsrc(ws, WS_H1 + (B.tm * 56 + (fp >> 1)) * 16384), lane * 16, (fp & 1) * 1024 + (lw * 2 + (t & 1)) * 2048); return 1; } return 0; };
    G3.run<8>([&](int t) { int e = 0;
        if (t == 0 || (t < 4 && t == tu)) U.read();
        if (t == 4) { U.publish(ruv, B.tm, lw, lane, lds0); e = 4; }
        if (t < 3) e += h1pre(t);
        return e; },
      [&](int t) { int e = h1pre(t);
        if (t == t3 && !(EV & 4)) { touch(opq((const char*)A.w2) + (size_t)(blockIdx.x * 4 + lw) * 7168, min(lane, 55) * 128, lds0 + L_DUM); e += 1; }
        if ((PT & 4) && t == 11 && lw == 0) { touch_code(lane, lds0 + L_DUM); e += 1; }
        return e; },
      [&]() { U.fetch(opq(ws) + ruv.base, lw, lane); });
    S.fetch(opq(ws) + WS_ST2, B.tm, lw, lane);
    G4.init(ws + WS_H1, 56, A.w2, (B.tn >> 3) * 28, (B.tn & 7) * 128, 0, (B.rot * 28) >> 4, B.tm, lw, lane, lds0);
    G4.issueB<false>(0); G4.issueB<false>(1);
    __builtin_amdgcn_s_setprio(3);
    waitvm<24>(); U.finish(ruv, lw, lane, lds0 + L_UVT); waitvm<8>(); S.finish(lw, lane, lds0 + L_STT);
    __builtin_amdgcn_s_setprio((EV & 8) ? 3 : 0);
    lbar();
  }
  {
    bar();
    G4.issueB<false>(2); G4.issueB<false>(3);
    panel_sync(ws, 5, B, B, lw, lane, tag, [&]() {});
    STAMP(35);
    G4.run<0>([&](int) { return 0; }, [&](int) { return 0; }, [&]() {});
    asm volatile(".Lcode_end:");
  }
}

DI void compute_role(const Args& A, int w, unsigned lds0, unsigned& ts) {
  char* ws = A.ws; const Blk B = blk(); const f32x4 zero = {0, 0, 0, 0}; const u32x4 mark = {~0u, ~0u, ~0u, ~0u};
  const int wm = w >> 1, wn = w & 1; int lane = threadIdx.x & 63; pin(lane);
  {
    const Blk B = blk1(); const int g = lane >> 4, r = lane & 15, q = r >> 2, p = lane & 3;
    bar();
    if (w == 0) { const f32x4 d = gemv64(lds0 + L_SAU, ldsr<bf16x8>(lds0 + L_SIL2 + g * 16), ldsr<bf16x8>(lds0 + L_SIL2 + 64 + g * 16), lane);
      if (r < 4) st_dev(__builtin_bit_cast(u32x4, d), mkrsrc(ws, WS_MODP + (size_t)((64 + B.tn) * 16 + B.tm) * 256), r * 64 + g * 16, 0); }
    bar();
    f32x4 acc[4][6];
    compute_loop<0>(acc, lds0, wm, wn, lane, ts);
    STAMP(13);
    bar();
    STAMP(14);
    constexpr float L2E = 1.4426950408889634f;
    bf16x8 wf[2]; RowSt st[4];
    UNR for (int m = 0; m < 4; m++) st[m].set(lds0 + L_STT + (wm * 64 + m * 16 + r) * 4, L2E);
    const unsigned uva = lds0 + L_UVT + wn * 256;
    bf16x8 kf[4];
    { const f32x4 u0 = ldsr<f32x4>(uva + 512 + g * 16), v0 = ldsr<f32x4>(uva + 576 + g * 16) * L2E, u1 = ldsr<f32x4>(uva + 640 + g * 16), v1 = ldsr<f32x4>(uva + 704 + g * 16) * L2E;
      UNR for (int c = 0; c < 2; c++) wf[c] = wfrag(lds0 + L_WPHI + wn * 2048 + (4 * g + q) * 64 + c * 32 + p * 8, 1024);
      UNR for (int mm = 0; mm < 4; mm++) { const int m = mm ^ 2; kf[m] = pack8(lnf(acc[m][2], st[m], u0, v0), lnf(acc[m][3], st[m], u1, v1));
        if (m >= 2) ldsw<bf16x8>(lds0 + L_KX + w * 2048 + (m - 2) * 1024 + lane * 16, kf[m]);
        if (m == 3) { lbar(); asm volatile("" : "+v"(acc[0][2]), "+v"(acc[0][3]), "+v"(acc[1][2]), "+v"(acc[1][3])); } } }
    { const unsigned trw = lds0 + L_TR + w * 8192 + r * 64 + ((g ^ ((r >> 1) & 3)) * 16); bf16x8 a, b, c, d; feat2(wf, kf[0], kf[1], a, b, c, d);
      ldsw<bf16x8>(trw, a); ldsw<bf16x8>(trw + 4096, b); ldsw<bf16x8>(trw + 1024, c); ldsw<bf16x8>(trw + 1024 + 4096, d); }
    { float u[2], v[2];
      UNR for (int c = 0; c < 2; c++) { u[c] = ldsr<float>(uva + 1024 + c * 128 + r * 4); v[c] = ldsr<float>(uva + 1088 + c * 128 + r * 4); asm("" : "+v"(u[c])); asm("" : "+v"(v[c])); }
      UNR for (int m = 0; m < 4; m++) { const unsigned a = lds0 + L_STT + (wm * 64 + m * 16 + 4 * g) * 4; const f32x4 rs = ldsr<f32x4>(a), nm = ldsr<f32x4>(a + 512);
        UNR for (int c = 0; c < 2; c++) acc[m][4 + c] = fma4(acc[m][4 + c], rs, fma4(nm, spl(u[c]), spl(v[c]))); }
      UNR for (int uu = 0; uu < 2; uu++) UNR for (int c = 0; c < 2; c++) ldsw<bf16x8>(lds0 + L_VF + w * 4096 + uu * 2048 + c * 1024 + lane * 16, pack8(acc[2 * uu][4 + c], acc[2 * uu + 1][4 + c])); }
    lbar();
    STAMP(15);
    asm volatile("" : "+v"(acc[0][0]), "+v"(acc[0][1]), "+v"(acc[1][0]), "+v"(acc[1][1]), "+v"(acc[2][0]), "+v"(acc[2][1]), "+v"(acc[3][0]), "+v"(acc[3][1]));
    bf16x8 qp[4], qn[4];
    { const f32x4 u0 = ldsr<f32x4>(uva + g * 16), v0 = ldsr<f32x4>(uva + 64 + g * 16) * L2E, u1 = ldsr<f32x4>(uva + 128 + g * 16), v1 = ldsr<f32x4>(uva + 192 + g * 16) * L2E;
      UNR for (int m = 0; m < 4; m += 2) feat2(wf, pack8(lnf(acc[m][0], st[m], u0, v0), lnf(acc[m][1], st[m], u1, v1)),
                                               pack8(lnf(acc[m + 1][0], st[m + 1], u0, v0), lnf(acc[m + 1][1], st[m + 1], u1, v1)), qp[m], qn[m], qp[m + 1], qn[m + 1]); }
    asm volatile("" : "+v"(qp[0]), "+v"(qp[1]), "+v"(qp[2]), "+v"(qp[3]), "+v"(qn[0]), "+v"(qn[1]), "+v"(qn[2]), "+v"(qn[3]) :: "memory");
    STAMP(16);
    bar();
    STAMP(17);
    const rsrc_t r2 = mkrsrc(ws, WS_KV2 + (size_t)(B.tn * 16 + B.tm) * 8192 + wn * 4096);
    u32x4 kk[4]; UNR for (int i = 0; i < 4; i++) kk[i] = ld_dev(r2, lane * 16, i * 1024);
    const bf16x8 z0 = ldsr<bf16x8>(lds0 + L_KX + wn * 2048 + lane * 16), z1 = ldsr<bf16x8>(lds0 + L_KX + wn * 2048 + 1024 + lane * 16);
    float rd[4];
    UNR for (int m = 0; m < 4; m++) rd[m] = __builtin_amdgcn_rcpf(mfma(z1, qn[m], mfma(z0, qp[m], zero))[0] + 1e-6f);
    asm volatile("" : "+v"(rd[0]), "+v"(rd[1]), "+v"(rd[2]), "+v"(rd[3]));
    if (__any(max(max(umax4(kk[0]), umax4(kk[1])), max(umax4(kk[2]), umax4(kk[3]))) == 0xffffffffu)) poll(kk, [&](int i) { return ld_dev(r2, lane * 16, i * 1024); });
    const rsrc_t ro = mkrsrc(ws, WS_O + (size_t)(B.tm * 16 + B.tn) * 16384);
    UNR for (int m = 0; m < 4; m++) {
      const f32x4 n0 = mfma(__builtin_bit_cast(bf16x8, kk[2]), qn[m], mfma(__builtin_bit_cast(bf16x8, kk[0]), qp[m], zero)), n1 = mfma(__builtin_bit_cast(bf16x8, kk[3]), qn[m], mfma(__builtin_bit_cast(bf16x8, kk[1]), qp[m], zero));
      st_dev(pack8u(n0 * rd[m], n1 * rd[m]), ro, lane * 16, (wm * 8 + wn) * 1024 + m * 2048);
    }
    STAMP(18);
    waitvm<0>(); bar();
    STAMP(19);
  }
  pin(lane);
  u32x4 o[4];
  {
    const int g = lane >> 4, r = lane & 15;
    { const float v = modvec(ws, WS_MODP + (size_t)((w == 0 ? 0 : w == 3 ? 1 : 2) * 16 + (w < 2 ? B.tn : ((B.tm >> 3) + 2 * ((B.tm >> 2) & 1)) * 4 + (B.tm & 3))) * 4096, lane), s = (w == 1 || w == 2) ? rbf(1.f + v) : v;
      if (w < 2) ldsw<float>(lds0 + L_GT + w * 256 + modcol(lane) * 4, s); else put_et(lds0, w - 2, modcol(lane), s); }
    f32x4 acc[4][2];
    bar();
    compute_loop<2>(acc, lds0, wm, wn, lane, ts);
    STAMP(23);
    f32x4 gate[2], sc[2]; u32x2 xr[4][2];
    UNR for (int n = 0; n < 2; n++) { gate[n] = ldsr<f32x4>(lds0 + L_GT + wn * 128 + n * 64 + g * 16); sc[n] = ldsr<f32x4>(lds0 + L_GT + 256 + wn * 128 + n * 64 + g * 16); }
    UNR for (int m = 0; m < 4; m++) UNR for (int n = 0; n < 2; n++) xr[m][n] = ldsr<u32x2>(lds0 + L_RES + (wm * 64 + m * 16 + r) * 128 + (((wn * 4 + n * 2 + (g >> 1)) ^ (r >> 1)) * 16) + (g & 1) * 8);
    const size_t tb = (size_t)(B.tm * 16 + B.tn) * 16384; const rsrc_t ry = outrsrc(A.out, tb), rst = mkrsrc(ws, WS_ST2 + (size_t)(B.tm * 32 + B.tn * 2 + wn) * 1024);
    f32x4 v[4][2]; float mean[4], m2[4];
    UNR for (int m = 0; m < 4; m++) {
      o[m] = pack8u(fma4(gate[0], acc[m][0], unp(xr[m][0][0], xr[m][0][1])), fma4(gate[1], acc[m][1], unp(xr[m][1][0], xr[m][1][1]))); v[m][0] = unp(o[m][0], o[m][1]); v[m][1] = unp(o[m][2], o[m][3]);
      st_dev(pack8u(v[m][0] * sc[0], v[m][1] * sc[1]), ry, lane * 16, (wm * 8 + wn) * 1024 + m * 2048); }
    UNR for (int m = 0; m < 4; m++) mean[m] = hsum4(v[m][0] + v[m][1]);
    const float mf = fold4<false>(mean[0], mean[1], mean[2], mean[3]) * (1.f / 32); spread4(mf, mean[0], mean[1], mean[2], mean[3]);
    UNR for (int m = 0; m < 4; m++) { const f32x4 d0 = v[m][0] - mean[m], d1 = v[m][1] - mean[m]; m2[m] = hsum4(fma4(d0, d0, d1 * d1)); }
    st_dev8(__builtin_bit_cast(u32x2, f32x2{mf, fold4<false>(m2[0], m2[1], m2[2], m2[3])}), rst, (wm * 64 + ((g >> 1) + 2 * (g & 1)) * 16 + r) * 8, 0);
    STAMP(24);
    waitvm<0>(); bar();
    STAMP(25);
  }
  pin(lane);
  {
    const int g = lane >> 4, r = lane & 15, odd = B.tn & 1;
    const int fp0 = B.tn * 7 + odd + wn * 3, fph = odd ? B.tn * 7 : B.tn * 7 + 6;
    if (w < 2) ldsw<float>(lds0 + L_GT + w * 256 + modcol(lane) * 4, modvec(ws, WS_MODP + (size_t)(48 + (B.tn & 7) * 2 + w) * 4096, lane));
    u32x2 bb[7];
    UNR for (int n = 0; n < 7; n++) bb[n] = *(const u32x2*)(A.b1 + (n < 6 ? fp0 * 32 + n * 16 : fph * 32 + wn * 16) + 4 * g);
    pin(bb);
    UNR for (int i = 0; i < 4; i++) st_dev(mark, mkrsrc(ws, xchoff(B.tm, B.tn, w)), lane * 16, i * 1024);
    f32x4 acc[4][7];
    bar();
    { const rsrc_t rx1 = mkrsrc(ws, WS_X1 + (size_t)(B.tm * 16 + B.tn) * 16384);
      UNR for (int m = 0; m < 4; m++) st_dev(o[m], rx1, lane * 16, (wm * 8 + wn) * 1024 + m * 2048); }
    compute_loop<1>(acc, lds0, wm, wn, lane, ts);
    STAMP(29);
    bar();
    STAMP(30);
    f32x4 u[7], v[7];
    UNR for (int n = 0; n < 7; n++) { const unsigned a = lds0 + L_UVT + (n < 6 ? wn * 6 + n : 12 + wn) * 128 + g * 16; u[n] = ldsr<f32x4>(a); v[n] = ldsr<f32x4>(a + 64) + unp(bb[n][0], bb[n][1]); }
    const rsrc_t rh = mkrsrc(ws, WS_H1 + (size_t)B.tm * 56 * 16384);
    RowSt st[4]; u32x2 h6[4];
    UNR for (int m = 0; m < 4; m++) st[m].set(lds0 + L_STT + (wm * 64 + m * 16 + r) * 4, 1.f);
    auto act = [&](int m, int n) { const f32x4 t = __builtin_elementwise_max(lnf(acc[m][n], st[m], u[n], v[n]), zero); return t * t; };
    UNR for (int n = 0; n < 6; n += 2) { const int fp = fp0 + (n >> 1);
      UNR for (int m = 0; m < 4; m++) st_dev(pack8u(act(m, n), act(m, n + 1)), rh, lane * 16, (fp >> 1) * 16384 + (fp & 1) * 1024 + wm * 8192 + m * 2048); }
    UNR for (int m = 0; m < 4; m++) { const f32x4 t = act(m, 6); h6[m] = u32x2{cvtpk(t[0], t[1]), cvtpk(t[2], t[3])}; }
    UNR for (int m = 0; m < 4; m += 2) { const auto a = __builtin_amdgcn_permlane16_swap(h6[m][0], h6[m + 1][0], false, false), b = __builtin_amdgcn_permlane16_swap(h6[m][1], h6[m + 1][1], false, false);
      st_dev(u32x4{a[0], b[0], a[1], b[1]}, rh, (g & 1) * 2048 + (g >> 1) * 256 + r * 16, (fph >> 1) * 16384 + (fph & 1) * 1024 + wm * 8192 + m * 2048 + wn * 512); }
    STAMP(31);
    bar(); waitvm<0>(); bar();
    STAMP(32);
  }
  pin(lane);
  {
    const int g = lane >> 4, r = lane & 15, kh = B.tn >> 3, cg = (B.tn & 7) * 2 + wn;
    u32x2 bb[4]; u32x4 xr[2][2]; const rsrc_t rx1 = mkrsrc(ws, WS_X1 + (size_t)(B.tm * 16 + cg) * 16384);
    UNR for (int n = 0; n < 4; n++) bb[n] = *(const u32x2*)(A.b2 + cg * 64 + n * 16 + 4 * g);
    pin(bb);
    f32x4 acc[4][4];
    bar();
    UNR for (int mm = 0; mm < 2; mm++) UNR for (int ks = 0; ks < 2; ks++) xr[mm][ks] = ld_dev(rx1, lane * 16, ((wm * 4 + kh * 2 + mm) * 2 + ks) * 1024);
    compute_loop<3>(acc, lds0, wm, wn, lane, ts);
    STAMP(37);
    pin(xr[0]); pin(xr[1]);
    const rsrc_t rx = mkrsrc(ws, xchoff(B.tm, B.tn, w)), rp = mkrsrc(ws, xchoff(B.tm, B.tn ^ 8, w));
    f32x4 keep[2][4], gate[4];
    if (kh) { UNR for (int i = 0; i < 4; i++) st_dev(pack8u(acc[i >> 1][(i & 1) * 2], acc[i >> 1][(i & 1) * 2 + 1]), rx, lane * 16, i * 1024);
              UNR for (int n = 0; n < 4; n++) { keep[0][n] = acc[2][n]; keep[1][n] = acc[3][n]; } }
    else    { UNR for (int i = 0; i < 4; i++) st_dev(pack8u(acc[2 + (i >> 1)][(i & 1) * 2], acc[2 + (i >> 1)][(i & 1) * 2 + 1]), rx, lane * 16, i * 1024);
              UNR for (int n = 0; n < 4; n++) { keep[0][n] = acc[0][n]; keep[1][n] = acc[1][n]; } }
    UNR for (int n = 0; n < 4; n++) gate[n] = ldsr<f32x4>(lds0 + L_GT + wn * 256 + n * 64 + g * 16);
    UNR for (int mm = 0; mm < 2; mm++) UNR for (int n = 0; n < 4; n++) keep[mm][n] = fma4(gate[n], keep[mm][n] + unp(bb[n][0], bb[n][1]), unp(xr[mm][n >> 1][(n & 1) * 2], xr[mm][n >> 1][(n & 1) * 2 + 1]));
    pin(keep[0]); pin(keep[1]);
    u32x4 pp[4]; poll(pp, [&](int i) { return ld_dev(rp, lane * 16, i * 1024); });
    STAMP(38);
    const unsigned ot = lds0 + L_OT + w * 4096;
    UNR for (int mm = 0; mm < 2; mm++) UNR for (int n = 0; n < 4; n++) { const u32x4 t = pp[mm * 2 + (n >> 1)]; const f32x4 y = fma4(gate[n], unp(t[(n & 1) * 2], t[(n & 1) * 2 + 1]), keep[mm][n]);
      ldsw<u32x2>(ot + (mm * 16 + r) * 128 + (((n * 2 + (g >> 1)) ^ (r >> 1)) * 16) + (g & 1) * 8, u32x2{cvtpk(y[0], y[1]), cvtpk(y[2], y[3])}); }
    lwait();
    const int r8 = lane >> 3; const unsigned rv0 = r8 * 2048 + (((lane & 7) ^ (r8 >> 1)) * 16);
    char* op = (char*)A.out + ((size_t)(B.tm * 128 + wm * 64 + kh * 32) * D_ + cg * 64) * 2;
    UNR for (int i = 0; i < 4; i++) *(u32x4*)(op + i * 16384 + ((i & 1) ? rv0 ^ 64 : rv0)) = ldsr<u32x4>(ot + i * 1024 + lane * 16);
    STAMP(39);
  }
}

__global__ __launch_bounds__(512, 2) void k_block(Args A) {
  __shared__ __attribute__((aligned(1024))) char smem[L_TOT];
  const int w = __builtin_amdgcn_readfirstlane(threadIdx.x >> 6);
  const unsigned lds0 = (unsigned)(size_t)(LAS char*)smem;
  asm volatile(".Lcode_begin:");
  if ((PT & 2) && w == 3) touch_code(threadIdx.x & 63, lds0 + L_DUM);
  unsigned ts = 0; STAMP(0);
  const u64 tag = phase0(A, w, lds0, ts);
  if (w >= 4) loader_role(A, w - 4, lds0, tag, ts); else compute_role(A, w, lds0, ts);
}

extern "C" void kernel_launch(void* const* d_in, const int* in_sizes, int n_in, void* d_out, int out_size, void* d_ws, size_t ws_size, hipStream_t stream) {
  Args A; A.x = (const u16*)d_in[0]; A.wqkv = (const u16*)d_in[1]; A.wphi = (const u16*)d_in[2]; A.wo = (const u16*)d_in[3]; A.w1 = (const u16*)d_in[5]; A.b1 = (const u16*)d_in[6];
  A.w2 = (const u16*)d_in[7]; A.b2 = (const u16*)d_in[8]; A.c = (const u16*)d_in[9]; A.wm = (const u16*)d_in[10]; A.out = (u16*)d_out; A.ws = (char*)d_ws;
  k_block<<<256, 512, 0, stream>>>(A);
}
```

```cpp
#define STAMPS
#define MODE 3
#include <hip/hip_runtime.h>
#include <hip/hip_bf16.h>

typedef unsigned short u16;
typedef unsigned u32x4 __attribute__((ext_vector_type(4)));
typedef unsigned u32x2 __attribute__((ext_vector_type(2)));
typedef short s16x4 __attribute__((ext_vector_type(4)));
typedef __bf16 bf16x8 __attribute__((ext_vector_type(8)));
typedef __bf16 bf16x2 __attribute__((ext_vector_type(2)));
typedef float f32x2 __attribute__((ext_vector_type(2)));
typedef float f32x4 __attribute__((ext_vector_type(4)));
#define LAS __attribute__((address_space(3)))
#define DI __device__ __forceinline__
#define SB __builtin_amdgcn_sched_barrier(0)
#define UNR _Pragma("unroll")

constexpr int D_ = 1024, F_ = 3584, MODN = 6 * D_;
constexpr size_t WS_EPOCH = 0;
constexpr size_t WS_S1    = 4096;
constexpr size_t WS_FLAG  = 1 << 16;
constexpr size_t WS_ST1   = 1 << 19;
constexpr size_t WS_ST2   = WS_ST1 + (1 << 18);
constexpr size_t WS_MODP  = 3 << 19;
constexpr size_t WS_UV1   = WS_MODP + 5 * 65536;
constexpr size_t WS_ZP    = WS_UV1 + 16 * 12 * 2048;
constexpr size_t WS_UV2   = WS_ZP + (1 << 17);
constexpr size_t WS_KV1   = 10 << 20;
constexpr size_t WS_KV2   = WS_KV1 + (2 << 20);
constexpr size_t WS_O     = 14 << 20;
constexpr size_t WS_X1    = 18 << 20;
constexpr size_t WS_H1    = 22 << 20;
constexpr size_t WS_STAMP = 40 << 20;
constexpr int SPIN_CAP = 1 << 14;
typedef unsigned long long u64;

#define STAMP(i) do { u64 t_; asm volatile("s_memrealtime %0\n\ts_waitcnt lgkmcnt(0)" : "=s"(t_) :: "memory"); ts = (int)(threadIdx.x & 63) == (i) ? (unsigned)t_ : ts; } while (0)

DI void glds16(const void* sbase, unsigned voff, unsigned ldsaddr) {
  asm volatile("s_mov_b32 m0, %2\n\ts_nop 0\n\tglobal_load_lds_dwordx4 %0, %1" :: "v"(voff), "s"(sbase), "s"(ldsaddr) : "memory");
}
DI void glds16nt(const void* sbase, unsigned voff, unsigned ldsaddr) {
  asm volatile("s_mov_b32 m0, %2\n\ts_nop 0\n\tglobal_load_lds_dwordx4 %0, %1 nt" :: "v"(voff), "s"(sbase), "s"(ldsaddr) : "memory");
}
DI void glds16x4(const void* sbase, unsigned voff, unsigned ldsaddr) {
  asm volatile("s_mov_b32 m0, %2\n\ts_nop 0\n\tglobal_load_lds_dwordx4 %0, %1 sc1\n\tglobal_load_lds_dwordx4 %0, %1 offset:1024 sc1\n\t"
               "global_load_lds_dwordx4 %0, %1 offset:2048 sc1\n\tglobal_load_lds_dwordx4 %0, %1 offset:3072 sc1" :: "v"(voff), "s"(sbase), "s"(ldsaddr) : "memory");
}
DI void touch(const void* sbase, unsigned voff, unsigned ldsdummy) {
  asm volatile("s_mov_b32 m0, %2\n\ts_nop 0\n\tglobal_load_lds_dword %0, %1" :: "v"(voff), "s"(sbase), "s"(ldsdummy) : "memory");
}
DI void touch_any(const void* p, unsigned ldsdummy) {
  asm volatile("s_mov_b32 m0, %1\n\ts_nop 0\n\tglobal_load_lds_dword %0, off" :: "v"(p), "s"(ldsdummy) : "memory");
}
DI void touch_nt(const void* sbase, unsigned voff, unsigned ldsdummy) {
  asm volatile("s_mov_b32 m0, %2\n\ts_nop 0\n\tglobal_load_lds_dword %0, %1 nt" :: "v"(voff), "s"(sbase), "s"(ldsdummy) : "memory");
}
DI void gld(u32x4& r, const void* sb, unsigned vo) { asm volatile("global_load_dwordx4 %0, %1, %2" : "=v"(r) : "v"(vo), "s"(sb) : "memory"); }
DI void gld_nt(u32x4& r, const void* sb, unsigned vo) { asm volatile("global_load_dwordx4 %0, %1, %2 nt" : "=v"(r) : "v"(vo), "s"(sb) : "memory"); }
DI void gld_dev(u32x4& r, const void* sb, unsigned vo) { asm volatile("global_load_dwordx4 %0, %1, %2 sc1" : "=v"(r) : "v"(vo), "s"(sb) : "memory"); }
DI void gld8(f32x2& r, const void* sb, unsigned vo) { asm volatile("global_load_dwordx2 %0, %1, %2 sc1" : "=v"(r) : "v"(vo), "s"(sb) : "memory"); }
DI void gld4_dev(unsigned& r, const void* sb, unsigned vo) { asm volatile("global_load_dword %0, %1, %2 sc1" : "=v"(r) : "v"(vo), "s"(sb) : "memory"); }
DI void gld4(unsigned& r, const void* sb, unsigned vo) { asm volatile("global_load_dword %0, %1, %2 nt" : "=v"(r) : "v"(vo), "s"(sb) : "memory"); }
template <class T> DI void pin(T& r) { asm volatile("" : "+v"(r)); }
template <class T, int N> DI void pin(T (&r)[N]) { UNR for (int i = 0; i < N; i++) pin(r[i]); }
template <class T> DI T opq(T v) { asm volatile("" : "+s"(v)); return v; }
DI f32x4 asf(u32x4 v) { return __builtin_bit_cast(f32x4, v); }
template <int N> DI void waitvm() { asm volatile("s_waitcnt vmcnt(%0)" :: "i"(N) : "memory"); }
template <int B, int LO = 0, int HI = 12> DI void waitx(int e) {
  if constexpr (LO == HI) waitvm<B + LO>();
  else if constexpr (LO == 0) { if (e == 0) waitvm<B>(); else waitx<B, 1, HI>(e); }
  else { constexpr int MID = (LO + HI + 1) / 2; if (e >= MID) waitx<B, MID, HI>(e); else waitx<B, LO, MID - 1>(e); }
}
DI void waitlgkm0() { __builtin_amdgcn_s_waitcnt(0xC07F); }
DI void lwait() { asm volatile("s_waitcnt lgkmcnt(0)" ::: "memory"); }
DI void bar() { asm volatile("s_barrier" ::: "memory"); }
DI void lbar() { asm volatile("s_waitcnt lgkmcnt(0)\n\ts_barrier" ::: "memory"); }
DI unsigned cvtpk(float a, float b) { f32x2 v = {a, b}; return __builtin_bit_cast(unsigned, __builtin_convertvector(v, bf16x2)); }
DI float rbf(float a) { return __uint_as_float(cvtpk(a, a) << 16); }
DI f32x4 rbf4(f32x4 s) { return f32x4{rbf(s[0]), rbf(s[1]), rbf(s[2]), rbf(s[3])}; }
DI float blo(unsigned u) { return __uint_as_float(u << 16); }
DI float bhi(unsigned u) { return __uint_as_float(u & 0xffff0000u); }
DI f32x4 unp(unsigned a, unsigned b) { return f32x4{blo(a), bhi(a), blo(b), bhi(b)}; }
DI u32x4 pack8u(const f32x4& a, const f32x4& b) { return u32x4{cvtpk(a[0], a[1]), cvtpk(a[2], a[3]), cvtpk(b[0], b[1]), cvtpk(b[2], b[3])}; }
DI bf16x8 pack8(const f32x4& a, const f32x4& b) { return __builtin_bit_cast(bf16x8, pack8u(a, b)); }
DI f32x4 mfma(bf16x8 a, bf16x8 b, f32x4 c) { return __builtin_amdgcn_mfma_f32_16x16x32_bf16(a, b, c, 0, 0, 0); }
DI s16x4 trd(unsigned a) { return __builtin_amdgcn_ds_read_tr16_b64_v4i16((LAS s16x4*)(size_t)a); }
DI bf16x8 cat(s16x4 lo, s16x4 hi) { return __builtin_bit_cast(bf16x8, __builtin_shufflevector(lo, hi, 0, 1, 2, 3, 4, 5, 6, 7)); }
template <class T> DI T ldsr(unsigned a) { return *(const LAS T*)(size_t)a; }
template <class T> DI void ldsw(unsigned a, T v) { *(LAS T*)(size_t)a = v; }
template <int C> DI float dpp(float x) { return __builtin_bit_cast(float, __builtin_amdgcn_update_dpp(0, __builtin_bit_cast(int, x), C, 0xf, 0xf, true)); }
DI float vmax(float a, float b) { float r; asm("v_max_f32_e32 %0, %1, %2" : "=v"(r) : "v"(a), "v"(b)); return r; }
DI float sw32_add(float a, float b) { auto r = __builtin_amdgcn_permlane32_swap(__float_as_uint(a), __float_as_uint(b), false, false); return __uint_as_float(r[0]) + __uint_as_float(r[1]); }
DI float sw16_add(float a, float b) { auto r = __builtin_amdgcn_permlane16_swap(__float_as_uint(a), __float_as_uint(b), false, false); return __uint_as_float(r[0]) + __uint_as_float(r[1]); }
DI float xg_sum(float a) { a = sw32_add(a, a); return sw16_add(a, a); }
DI float xg_max(float a) {
  auto r = __builtin_amdgcn_permlane32_swap(__float_as_uint(a), __float_as_uint(a), false, false); a = vmax(__uint_as_float(r[0]), __uint_as_float(r[1]));
  auto q = __builtin_amdgcn_permlane16_swap(__float_as_uint(a), __float_as_uint(a), false, false); return vmax(__uint_as_float(q[0]), __uint_as_float(q[1]));
}
DI float row16_sum(float a) { a += dpp<0x128>(a); a += dpp<0x124>(a); a += dpp<0x122>(a); a += dpp<0x121>(a); return a; }
DI f32x4 row16_sum4(f32x4 a) { return f32x4{row16_sum(a[0]), row16_sum(a[1]), row16_sum(a[2]), row16_sum(a[3])}; }
DI f32x4 spl(float a) { return f32x4{a, a, a, a}; }
DI f32x4 fma4(f32x4 a, f32x4 b, f32x4 c) { return __builtin_elementwise_fma(a, b, c); }
struct RowSt { float rs, nm; DI void set(unsigned a, float k) { rs = ldsr<float>(a) * k; nm = ldsr<float>(a + 512) * k; asm("" : "+v"(rs)); asm("" : "+v"(nm)); } };
DI f32x4 lnf(f32x4 acc, const RowSt& st, f32x4 u, f32x4 v) { return fma4(acc, spl(st.rs), fma4(u, spl(st.nm), v)); }
DI float fadd(float a, float b) { float r; asm("v_add_f32_e32 %0, %1, %2" : "=v"(r) : "v"(a), "v"(b)); return r; }
DI float hsum4(f32x4 a) { const f32x2 t = f32x2{a[0], a[1]} + f32x2{a[2], a[3]}; float r; asm("v_add_f32_e32 %0, %1, %2" : "=v"(r) : "v"(t[0]), "v"(t[1])); return r; }
struct rsrc_t { __amdgpu_buffer_rsrc_t r; unsigned base; };
DI rsrc_t mkrsrc(const char* ws, size_t off) { return rsrc_t{__builtin_amdgcn_make_buffer_rsrc((void*)ws, 0, 64 << 20, 0x00020000), (unsigned)off}; }
DI rsrc_t outrsrc(const void* out, size_t off) { return rsrc_t{__builtin_amdgcn_make_buffer_rsrc((void*)out, 0, 2048 * D_ * 2, 0x00020000), (unsigned)off}; }
DI unsigned xchoff(int tm, int tn, int w) { return WS_KV1 + (w >> 1) * (2 << 20) + (tn * 16 + tm) * 8192 + (w & 1) * 4096; }
DI u32x4 ld_dev(const rsrc_t& r, unsigned voff, unsigned soff) { return __builtin_amdgcn_raw_buffer_load_b128(r.r, voff, r.base + soff, 16); }
DI void st_dev(u32x4 v, const rsrc_t& r, unsigned voff, unsigned soff) { __builtin_amdgcn_raw_buffer_store_b128(v, r.r, voff + (r.base + soff), 0, 16); }
DI void st_dev8(u32x2 v, const rsrc_t& r, unsigned voff, unsigned soff) { __builtin_amdgcn_raw_buffer_store_b64(v, r.r, voff + (r.base + soff), 0, 16); }
DI unsigned umax4(u32x4 v) { return max(max(v[0], v[1]), max(v[2], v[3])); }
template <int N, class F> DI void poll(u32x4 (&v)[N], F ld) {
  _Pragma("nounroll") for (int spin = 0; spin < SPIN_CAP; spin++) { unsigned m = 0;
    UNR for (int i = 0; i < N; i++) v[i] = ld(i);
    UNR for (int i = 0; i < N; i++) m = max(m, umax4(v[i]));
    if (!__any(m == 0xffffffffu)) break;
    asm volatile("s_sleep 2" ::: "memory");
  }
}
#define EV 0
#define PT 7
#define PG 0
#define K0V 0
DI float fmax3(float a, float b, float c) { return __builtin_elementwise_maximum(__builtin_elementwise_maximum(a, b), c); }
struct Blk { int tm, tn, rot, j, xq; };
DI Blk blk1() { const int xcd = blockIdx.x & 7, j = blockIdx.x >> 3; Blk b; b.tm = j >> 1; b.tn = xcd * 2 + (j & 1); b.rot = b.tm; b.j = j; b.xq = xcd; return b; }
DI Blk blk() { const int xcd = blockIdx.x & 7, j = blockIdx.x >> 3; Blk b; b.tm = (xcd >> 2) * 8 + (j >> 2); b.tn = (xcd & 3) * 4 + (j & 3); b.rot = (b.tm & 7) * 2 + (b.tm >> 3); b.j = j; b.xq = xcd & 3; return b; }
DI unsigned ptile(int c, int lane) { const int row = 4 * (lane >> 4) + ((lane & 15) >> 2); return row * 128 + ((c ^ ((row >> 1) & 3)) * 32) + (lane & 3) * 8; }
DI unsigned htile(int c, int lane) { const int row = 4 * (lane >> 4) + ((lane & 15) >> 2); return row * 64 + ((c ^ ((row >> 2) & 1)) * 32) + (lane & 3) * 8; }
DI bf16x8 wfrag(unsigned a, unsigned ts) { return cat(trd(a), trd(a + ts)); }

DI unsigned silu2(unsigned cw) { const float c0 = blo(cw), c1 = bhi(cw); return cvtpk(c0 * __builtin_amdgcn_rcpf(1.f + __expf(-c0)), c1 * __builtin_amdgcn_rcpf(1.f + __expf(-c1))); }
DI unsigned fragpos(int k) { return (k >> 5) * 64 + ((k >> 2) & 3) * 16 + ((k & 3) + 4 * ((k >> 4) & 1)) * 2; }
DI f32x4 gemv64(unsigned a, bf16x8 s0, bf16x8 s1, int lane) { f32x4 d[4];
  UNR for (int cc = 0; cc < 4; cc++) d[cc] = mfma(wfrag(a + 4096 + ptile(cc, lane), 2048), s1, mfma(wfrag(a + ptile(cc, lane), 2048), s0, f32x4{0, 0, 0, 0}));
  const bool b0 = lane & 1, b1 = lane & 2; f32x4 o;
  UNR for (int k = 0; k < 4; k++) { const float lo = b0 ? d[1][k] : d[0][k], hi = b0 ? d[3][k] : d[2][k]; o[k] = b1 ? hi : lo; }
  return o;
}

template <int CFG> struct Cfg;
template <> struct Cfg<0> { static constexpr int NT = 6, NP = 3, HALF = 0, NB = 2, NS = 3, N = 3 * D_, KT = 16, B0 = 49152, BS0 = 0; };
template <> struct Cfg<1> { static constexpr int NT = 7, NP = 3, HALF = 1, NB = 7, NS = 3, N = F_, KT = 16, B0 = 65536, BS0 = 2; };
template <> struct Cfg<2> { static constexpr int NT = 2, NP = 1, HALF = 0, NB = 2, NS = 4, N = D_, KT = 16, B0 = 73728, BS0 = 0; };
template <> struct Cfg<3> { static constexpr int NT = 4, NP = 2, HALF = 0, NB = 4, NS = 4, N = D_, KT = 28, B0 = 65536, BS0 = 0; };
template <int CFG> struct Lay { using C = Cfg<CFG>; static constexpr int SBB = C::NP * 8192 + C::HALF * 4096, PB = 2 * C::NP + C::HALF, PER = 4 + PB; };
template <int CFG> DI unsigned btile(int tl, int lane) { using C = Cfg<CFG>; return C::B0 + (tl < 4 * C::NP ? (tl >> 2) * 8192 + ptile(tl & 3, lane) : C::NP * 8192 + htile(tl & 1, lane)); }
constexpr int L_RES = 106496, L_WPHI = 155648, L_UVT = L_WPHI + 4096, L_STT = L_UVT + 2048, L_ET = L_STT + 1024, L_GT = L_ET + 256, L_DUM = L_GT + 512, L_TOT = 163840, L_REST = 122880, L_OT = 131072;
constexpr int L_XT = 131072, L_SAU = 147456, L_SIL = L_WPHI, L_RED = L_SIL + 2048, L_SROW = L_RED + 2048, L_SIL2 = L_GT;
constexpr int L_TR = 0, L_VF = 32768, L_KX = 49152;

template <int CFG> DI void compute_loop(f32x4 (&acc)[4][Cfg<CFG>::NT], unsigned lds0, int wm, int wn, int lane, unsigned& ts) {
  using C = Cfg<CFG>; using L = Lay<CFG>; constexpr int MT = 4, NT = C::NT, NB = C::NB, KT = C::KT;
  const unsigned aoff = lds0 + wm * 8192 + lane * 16; unsigned boff[NB];
  UNR for (int i = 0; i < NB; i++) boff[i] = lds0 + btile<CFG>(CFG == 1 ? (i < 6 ? wn * 6 + i : 12 + wn) : wn * NB + i, lane);
  bf16x8 fa[2][MT]; s16x4 fb[2][NT][2]; unsigned va, vb[NB];
  constexpr int NR = MT + 2 * NT, NM = MT * NT, NU = 1 + NB, FREE = NM - NR, UPG = FREE > 0 ? (NU + FREE - 1) / FREE : NU;
  auto upd = [&](int i, unsigned sa, unsigned sb) { if (i == 0) { va = aoff + sa; pin(va); } else { vb[i - 1] = boff[i - 1] + sb; pin(vb[i - 1]); } };
  auto rd1 = [&](int buf, int ks, int i) {
    if (i == 0 || i > 2 * NT) { const int m = i ? i - 2 * NT : 0; fa[buf][m] = ldsr<bf16x8>(va + m * 2048 + ks * 1024); }
    else { const int n = (i - 1) >> 1, t = (i - 1) & 1;
      fb[buf][n][t] = trd(CFG == 0 ? vb[n & 1] + (n >> 1) * 8192 + ks * 4096 + t * 2048 : (CFG == 1 && n == 6) ? vb[NB - 1] + ks * 2048 + t * 1024 : vb[n] + ks * 4096 + t * 2048); }
  };
  auto step = [&](int buf, int ks, bool doread, bool first, bool doupd, unsigned sa, unsigned sb) {
    UNR for (int i = 0; i < NM; i++) {
      const int m = i / NT, n = i % NT; const bf16x8 b = cat(fb[buf][n][0], fb[buf][n][1]);
      const f32x4 cin = first ? f32x4{0, 0, 0, 0} : acc[m][n];
      acc[m][n] = (CFG == 0 && n >= 4) ? mfma(fa[buf][m], b, cin) : mfma(b, fa[buf][m], cin);
      SB;
      if (doread && i < NR) { rd1(buf ^ 1, ks, i); SB; }
      if (doupd && (FREE > 0 ? i >= NR : i == NM - 1)) { const int u0 = FREE > 0 ? (i - NR) * UPG : 0;
        UNR for (int u = u0; u < u0 + UPG; u++) if (u < NU) upd(u, sa, sb);
        SB; }
    }
  };
  UNR for (int i = 0; i < NU; i++) upd(i, 0, C::BS0 * L::SBB);
  SB; lbar();
  STAMP(CFG == 0 ? 12 : CFG == 2 ? 22 : CFG == 1 ? 28 : 36);
  UNR for (int i = 0; i < NR; i++) rd1(0, 0, i);
  SB;
  unsigned sa = 16384, sb = ((C::BS0 + 1) % C::NS) * L::SBB;
  step(0, 1, true, true, true, sa, sb);
  for (int t = 0; t < KT - 1; t++) {
    waitlgkm0(); bar(); SB;
    sa += 16384; if (sa == C::NS * 16384) sa = 0;
    sb += L::SBB; if (sb == C::NS * L::SBB) sb = 0;
    step(1, 0, true, false, false, 0, 0);
    if (FREE >= 4) { waitlgkm0(); SB; }
    step(0, 1, true, false, true, sa, sb);
  }
  step(1, 0, false, false, false, 0, 0);
}

template <int CFG> struct Loader {
  using C = Cfg<CFG>; using L = Lay<CFG>; static constexpr int N = C::N, KT = C::KT, PER = L::PER, PB = L::PB, NS = C::NS;
  const char *ab, *bb, *hb; unsigned av, bv, hv, la, lb, lh; int ka, kb;
  DI void init(const void* A, int akt, const u16* W, int k0, int col0, int hcol, int rot, int tm, int lw, int lane, unsigned lds0) {
    const int r = lane >> 3, hr = lane >> 2;
    ab = (const char*)A + ((size_t)tm * akt + k0) * 16384 + lw * 4096; av = lane * 16;
    bb = (const char*)W + ((size_t)(k0 * 64 + lw * 16) * N + col0) * 2; bv = r * N * 2 + (((lane & 7) ^ (((r >> 1) & 3) << 1)) * 16);
    hb = (const char*)W + ((size_t)(lw * 16) * N + hcol) * 2; hv = hr * N * 2 + (((lane & 3) ^ (((hr >> 2) & 1) << 1)) * 16);
    if (CFG == 3) { const int t = lw & 1; bb -= (size_t)t * 16 * N * 2; hv = bv + (4 * t + 4 * (r >> 2)) * N * 2; bv += t * 16 * N * 2; }
    la = lds0 + lw * 4096; lb = lds0 + C::B0 + lw * 2048; lh = lds0 + C::B0 + C::NP * 8192 + lw * 1024; ka = kb = rot;
  }
  DI void issueA(int slot) { glds16x4(ab + (size_t)ka * 16384, av, la + slot * 16384); ka++; if (ka == KT) ka = 0; }
  template <bool NT> DI void issueB(int slot) { const char* b = bb + (size_t)kb * 64 * N * 2; slot += C::BS0; if (slot >= NS) slot -= NS; const unsigned so = slot * L::SBB;
    const bool nat = CFG == 3 && ((0x1020408u >> kb) & 1);
    const unsigned v0 = nat ? hv : bv, v1 = v0 + (nat ? 16 : 8) * N * 2;
    UNR for (int i = 0; i < 2 * C::NP; i++) { const char* p = b + (i >> 1) * (CFG == 0 ? 2048 : 128) + (CFG == 3 ? 0 : (i & 1) * (8 * N * 2)); const unsigned l = lb + so + (i >> 1) * 8192 + (i & 1) * 1024, vo = CFG == 3 && (i & 1) ? v1 : v0;
      if (NT) glds16nt(p, vo, l); else glds16(p, vo, l); }
    if (C::HALF) { if (NT) glds16nt(hb + (size_t)kb * 64 * N * 2, hv, lh + so); else glds16(hb + (size_t)kb * 64 * N * 2, hv, lh + so); }
    kb++; if (kb == KT) kb = 0;
  }
  DI void issue(int slot) { issueA(slot); issueB<false>(slot); }
  template <int E1, class HF, class HK, class H1> DI void run(HF hf, HK hk, H1 h1) {
    UNR for (int s = 0; s < NS; s++) issueA(s);
    if (NS == 3) {
      waitvm<8>(); bar(); int e0 = hf(0); waitx<4>(e0); lbar();
      UNR for (int t = 1; t <= 5; t++) { issue((t - 1) % 3); int e1 = hf(t); if (t >= 3) e1 += hk(t); waitx<PER>(e0 + e1); lbar(); e0 = e1; }
      int slot = 2;
      _Pragma("nounroll") for (int t = 6; t <= KT - 3; t++) { issue(slot); slot++; if (slot == NS) slot = 0; const int e1 = hk(t); waitx<PER>(e0 + e1); lbar(); e0 = e1; }
    } else {
      waitvm<12>(); bar(); waitvm<8>(); bar();
      issue(0); waitvm<4 + PER>(); bar();
      int slot = 1;
      for (int t = 2; t <= KT - 4; t++) { issue(slot); slot = (slot + 1) & 3; waitvm<2 * PER>(); bar(); }
      waitvm<PER>(); bar();
    }
    h1(); waitvm<E1>(); bar();
  }
};
DI u64* flagp(char* ws, int tile, int line, int word) { return (u64*)(ws + WS_FLAG) + (tile * 6 + line) * 16 + word; }
DI u64 wait_flags(u64* f, u64 tag, u64 need) {
  u64 got = 0;
  _Pragma("nounroll") for (int spin = 0; spin < SPIN_CAP; spin++) {
    got = __ballot(__hip_atomic_load(f, __ATOMIC_RELAXED, __HIP_MEMORY_SCOPE_AGENT) == tag);
    if ((got & need) == need) break;
    asm volatile("s_sleep 1" ::: "memory");
  }
  return got;
}
template <class H> DI bool panel_sync(char* ws, int line, const Blk& P, const Blk& C, int lw, int lane, u64 tag, H pre) {
  bar();
  if (lw == 0) { const int i = lane & 15, grp = lane >> 4;
    if (line == 0) { if (grp < 3) __hip_atomic_store(grp == 0 ? flagp(ws, P.tm * 16 + i, 0, P.tn) : grp == 1 ? flagp(ws, i * 16 + P.tn, 1, P.tm) : flagp(ws, P.tn * 16 + i, 2, P.tm), tag, __ATOMIC_RELAXED, __HIP_MEMORY_SCOPE_AGENT); }
    else if (grp == 0) __hip_atomic_store(flagp(ws, P.tm * 16 + i, line, P.tn), tag, __ATOMIC_RELAXED, __HIP_MEMORY_SCOPE_AGENT); }
  pre();
  bool all = true;
  if (lw == 0) all = wait_flags(flagp(ws, C.tm * 16 + C.tn, line, line == 0 ? min(lane, 47) : lane & 15), tag, 0xffff) == ~0ull;
  bar();
  return all;
}
DI int modcol(int lane) { return 4 * (lane & 15) + 2 * (lane >> 5) + ((lane >> 4) & 1); }
DI float modvec(const char* ws, unsigned part, int lane, u32x4 (&p)[4], bool have) {
  if (!have || __any(max(max(umax4(p[0]), umax4(p[1])), max(umax4(p[2]), umax4(p[3]))) == 0xffffffffu)) { const rsrc_t r = mkrsrc(ws, part); poll(p, [&](int i) { return ld_dev(r, lane * 16, i * 1024); }); }
  const f32x4 a = (asf(p[0]) + asf(p[1])) + (asf(p[2]) + asf(p[3]));
  return rbf(sw16_add(sw32_add(a[0], a[2]), sw32_add(a[1], a[3])));
}
DI float modvec(const char* ws, unsigned part, int lane) { u32x4 p[4]; return modvec(ws, part, lane, p, false); }
DI void put_et(unsigned lds0, int vec, int k, float v) { ldsw<u16>(lds0 + L_ET + vec * 128 + fragpos(k), (u16)(__float_as_uint(v) >> 16)); }

template <int CFG, int NTL> struct UV {
  bf16x8 f[NTL][2]; unsigned ad[NTL], ts[NTL];
  DI void prep(int lw, int lane, unsigned base) { UNR for (int i = 0; i < NTL; i++) { const int tl = min(lw * NTL + i, 2 * Cfg<CFG>::NT - 1); ad[i] = base + btile<CFG>(tl, lane); ts[i] = tl < 4 * Cfg<CFG>::NP ? 2048 : 1024; pin(ad[i]); } }
  DI void read() { UNR for (int i = 0; i < NTL; i++) { f[i][0] = wfrag(ad[i], ts[i]); f[i][1] = wfrag(ad[i] + 2 * ts[i], ts[i]); } }
  DI void publish(const rsrc_t& r, int tm, int lw, int lane, unsigned lds0) {
    const bf16x8 e0 = ldsr<bf16x8>(lds0 + L_ET + (lane & 1) * 128 + (lane >> 4) * 16), e1 = ldsr<bf16x8>(lds0 + L_ET + (lane & 1) * 128 + 64 + (lane >> 4) * 16);
    f32x4 d[NTL];
    UNR for (int i = 0; i < NTL; i++) d[i] = mfma(f[i][1], e1, mfma(f[i][0], e0, f32x4{0, 0, 0, 0}));
    if ((lane & 14) == 0) UNR for (int i = 0; i < NTL; i++) st_dev(__builtin_bit_cast(u32x4, d[i]), r, (lane & 1) * 64 + (lane >> 4) * 16, (lw * NTL + i) * 2048 + tm * 128);
  }
  u32x4 v[2 * NTL];
  DI void fetch(const char* base, int lw, int lane) { UNR for (int i = 0; i < 2 * NTL; i++) gld_dev(v[i], base + lw * NTL * 2048 + i * 1024, lane * 16); }
  DI void finish(const rsrc_t& r, int lw, int lane, unsigned tab) {
    pin(v); unsigned mx = 0;
    UNR for (int i = 0; i < 2 * NTL; i++) mx = max(mx, umax4(v[i]));
    if (__any(mx == 0xffffffffu)) poll(v, [&](int i) { return ld_dev(r, lane * 16, lw * NTL * 2048 + i * 1024); });
    UNR for (int i = 0; i < NTL; i++) { const f32x4 a = asf(v[2 * i]) + asf(v[2 * i + 1]);
      float t = sw16_add(sw32_add(a[0], a[2]), sw32_add(a[1], a[3])); t += dpp<0x128>(t);
      ldsw<float>(tab + (lw * NTL + i) * 128 + (lane & 7) * 16 + (2 * (lane >> 5) + ((lane >> 4) & 1)) * 4, t); }
  }
};
template <int NPART> struct Stats {
  f32x2 v[NPART / 2];
  DI void fetch(const char* st, int tm, int lw, int lane) { UNR for (int i = 0; i < NPART / 2; i++) gld8(v[i], st + (size_t)(tm * NPART + i) * 1024 + lw * 256, (lane >> 5) * (NPART * 512) + (lane & 31) * 8); }
  DI void finish(int lw, int lane, unsigned tab) { pin(v); f32x2 s = v[0]; float q = 0;
    UNR for (int i = 1; i < NPART / 2; i++) s += v[i];
    const float mean = sw32_add(s[0], s[0]) * (1.f / NPART);
    UNR for (int i = 0; i < NPART / 2; i++) { const float d = v[i][0] - mean; q += d * d; }
    q = s[1] + (float)(D_ / NPART) * q;
    const float rstd = __builtin_amdgcn_rsqf(sw32_add(q, q) * (1.f / D_) + 1e-5f);
    ldsw<float>(tab + (lane >> 5) * 512 + (lw * 32 + (lane & 31)) * 4, lane < 32 ? rstd : -mean * rstd); }
};

template <bool MAX> DI float fold4(float a, float b, float c, float d) {
  auto op = [](unsigned x, unsigned y) { return MAX ? __builtin_elementwise_maximum(__uint_as_float(x), __uint_as_float(y)) : __uint_as_float(x) + __uint_as_float(y); };
  const auto r0 = __builtin_amdgcn_permlane32_swap(__float_as_uint(a), __float_as_uint(b), false, false), r1 = __builtin_amdgcn_permlane32_swap(__float_as_uint(c), __float_as_uint(d), false, false);
  const auto r2 = __builtin_amdgcn_permlane16_swap(__float_as_uint(op(r0[0], r0[1])), __float_as_uint(op(r1[0], r1[1])), false, false);
  return op(r2[0], r2[1]);
}
DI void spread4(float t, float& a, float& b, float& c, float& d) {
  const auto r2 = __builtin_amdgcn_permlane16_swap(__float_as_uint(t), __float_as_uint(t), false, false);
  const auto r0 = __builtin_amdgcn_permlane32_swap(r2[0], r2[0], false, false), r1 = __builtin_amdgcn_permlane32_swap(r2[1], r2[1], false, false);
  a = __uint_as_float(r0[0]); b = __uint_as_float(r0[1]); c = __uint_as_float(r1[0]); d = __uint_as_float(r1[1]);
}
DI void feat2(const bf16x8 (&wf)[2], bf16x8 x0, bf16x8 x1, bf16x8& fp0, bf16x8& fn0, bf16x8& fp1, bf16x8& fn1) {
  const f32x4 zero = {0, 0, 0, 0}; f32x4 p[2][2] = {{mfma(wf[0], x0, zero), mfma(wf[1], x0, zero)}, {mfma(wf[0], x1, zero), mfma(wf[1], x1, zero)}};
  float mx[2], nx[2], sp[2], sn[2]; f32x4 a[2][2], b[2][2];
  UNR for (int i = 0; i < 2; i++) { const f32x4 u = p[i][0], v = p[i][1];
    mx[i] = fmax3(fmax3(fmax3(u[0], u[1], u[2]), u[3], v[0]), fmax3(v[1], v[2], v[3]), v[3]); nx[i] = fmax3(fmax3(fmax3(-u[0], -u[1], -u[2]), -u[3], -v[0]), fmax3(-v[1], -v[2], -v[3]), -v[3]); }
  spread4(fold4<true>(mx[0], mx[1], nx[0], nx[1]), mx[0], mx[1], nx[0], nx[1]);
  UNR for (int i = 0; i < 2; i++) UNR for (int h = 0; h < 2; h++) UNR for (int j = 0; j < 4; j += 2) { const f32x2 t = {p[i][h][j], p[i][h][j + 1]};
    f32x2 d = t - f32x2{mx[i], mx[i]}, e = -t - f32x2{nx[i], nx[i]}; asm("" : "+v"(d), "+v"(e));
    a[i][h][j] = __builtin_amdgcn_exp2f(d[0]); a[i][h][j + 1] = __builtin_amdgcn_exp2f(d[1]); b[i][h][j] = __builtin_amdgcn_exp2f(e[0]); b[i][h][j + 1] = __builtin_amdgcn_exp2f(e[1]); }
  UNR for (int i = 0; i < 2; i++) { sp[i] = hsum4(a[i][0] + a[i][1]); sn[i] = hsum4(b[i][0] + b[i][1]); }
  spread4(__builtin_amdgcn_rcpf(fold4<false>(sp[0], sp[1], sn[0], sn[1])), sp[0], sp[1], sn[0], sn[1]);
  fp0 = pack8(a[0][0] * sp[0], a[0][1] * sp[0]); fn0 = pack8(b[0][0] * sn[0], b[0][1] * sn[0]); fp1 = pack8(a[1][0] * sp[1], a[1][1] * sp[1]); fn1 = pack8(b[1][0] * sn[1], b[1][1] * sn[1]);
}

DI void touch_code(int lane, unsigned ldsdummy) {
  const char* pc; unsigned len, back;
  asm volatile("s_getpc_b64 %0\n.Lhere%=:\n\ts_mov_b32 %2, .Lhere%=-.Lcode_begin\n\ts_mov_b32 %1, .Lcode_end-.Lcode_begin" : "=s"(pc), "=s"(len), "=s"(back));
  pc -= back;
  const unsigned o = ((blockIdx.x >> 3) * 16 + (lane & 15)) * 128;
  touch(pc, min(o, len), ldsdummy);
}
struct Args { const u16 *x, *wqkv, *wphi, *wo, *w1, *b1, *w2, *b2, *c, *wm; u16* out; char* ws; };

DI u64 phase0(const Args& A, int w, unsigned lds0, unsigned& ts) {
  char* ws = A.ws; const Blk B = blk1(); const f32x4 zero = {0, 0, 0, 0};
  const int lane = threadIdx.x & 63, r8 = lane >> 3, g = lane >> 4, r = lane & 15;
  unsigned cw, cw2; gld4(cw, A.c, w * 256 + ((B.tm * 16 + lane * 4) & 255)); gld4(cw2, A.c, B.tm * 128 + (lane & 31) * 4);
  const char* wb = (const char*)A.wm + B.tn * 128; const unsigned wv = r8 * MODN * 2 + (((lane & 7) ^ (((r8 >> 1) & 3) << 1)) * 16);
  auto piece = [&](int i) { glds16(wb + (size_t)(w * 16 + ((B.tm + i) & 15)) * 16 * MODN + 2 * D_, wv, lds0 + (w * 16 + i) * 1024); };
  glds16nt(wb + (size_t)(w * 16 + B.tm) * 16 * MODN + 2 * D_, wv, lds0 + w * 16384);
  const unsigned rv0 = r8 * 2048 + (((lane & 7) ^ (r8 >> 1)) * 16);
  UNR for (int i = 0; i < 2; i++) glds16nt((const char*)A.x + ((size_t)(B.tm * 128 + w * 16 + i * 8) * D_ + B.tn * 64) * 2, i ? rv0 ^ 64 : rv0, lds0 + L_XT + (w * 2 + i) * 1024);
  bar();
  piece(1); piece(2);
  if (w == 7 && (PT & 1)) { const Blk C = blk(); if (lane < 6) touch_any(flagp(ws, lane < 3 ? B.tm * 16 + B.tn : C.tm * 16 + C.tn, lane, 0), lds0 + L_DUM); }
  if (w < 3) {
    const int l = w * 8 + r8;
    if (l < 22) st_dev(u32x4{~0u, ~0u, ~0u, ~0u}, mkrsrc(ws, WS_MODP), (l < 10 ? (((l >> 1) * 16 + B.tn) * 16 + B.tm) * 256 + (l & 1) * 128 : 5 * 65536 + (B.tn * 12 + l - 10) * 2048 + B.tm * 128) + (lane & 7) * 16, 0); }
  waitvm<5>(); pin(cw); pin(cw2);
  ldsw<unsigned>(lds0 + L_SIL + w * 256 + fragpos(2 * lane), silu2(cw));
  if (w == 7) ldsw<unsigned>(lds0 + L_SIL2 + fragpos(2 * (lane & 31)), silu2(cw2));
  unsigned pa[4]; UNR for (int cc = 0; cc < 4; cc++) { pa[cc] = lds0 + w * 16384 + ptile(cc, lane); pin(pa[cc]); }
  STAMP(1);
  f32x4 a[4] = {zero, zero, zero, zero};
  auto kstep = [&](int ks) { const bf16x8 sf = ldsr<bf16x8>(lds0 + L_SIL + (w * 4 + ks) * 64 + g * 16);
    UNR for (int cc = 0; cc < 4; cc++) a[cc] = mfma(wfrag(pa[cc] + ks * 4096, 2048), sf, a[cc]); };
  UNR for (int i = 3; i < 10; i++) piece(i);
  waitvm<6>();
  u32x2 xt[4];
  UNR for (int cc = 0; cc < 4; cc++) xt[cc] = ldsr<u32x2>(lds0 + L_XT + (w * 16 + r) * 128 + (((cc * 2 + (g >> 1)) ^ (r >> 1)) * 16) + (g & 1) * 8);
  kstep(0);
  UNR for (int i = 10; i < 16; i++) piece(i);
  STAMP(2);
  bar();
  glds16nt(wb + (size_t)(B.tm * 8 + w) * 16 * MODN, wv, lds0 + L_SAU + w * 1024);
  { const int e = min(w * 24 + lane, 191), row = e / 6, l = e - row * 6;
    if (lane < 24) touch_nt((const char*)A.wqkv + (size_t)B.j * 32 * 6144 + B.xq * 256, row * 6144 + (l >> 1) * 2048 + (l & 1) * 128, lds0 + L_DUM); }
  f32x2 ep; gld8(ep, ws + WS_EPOCH, 0);
  waitvm<11>(); kstep(1); waitvm<7>(); kstep(2); waitvm<3>(); kstep(3);
  if (r == 0) UNR for (int cc = 0; cc < 4; cc++) ldsw<f32x4>(lds0 + L_RED + w * 256 + cc * 64 + g * 16, a[cc]);
  STAMP(3);
  lbar();
  f32x4 xv[4]; UNR for (int cc = 0; cc < 4; cc++) xv[cc] = unp(xt[cc][0], xt[cc][1]);
  float s = 0;
  UNR for (int i = 0; i < 8; i++) s += ldsr<float>(lds0 + L_RED + i * 256 + lane * 4);
  s = rbf(1.f + rbf(s)); ldsw<float>(lds0 + L_SROW + w * 256 + lane * 4, s);
  asm volatile("" ::: "memory");
  const rsrc_t ry = outrsrc(A.out, (B.tm * 16 + B.tn) * 16384);
  UNR for (int ks = 0; ks < 2; ks++)
    st_dev(pack8u(xv[2 * ks] * ldsr<f32x4>(lds0 + L_SROW + w * 256 + ks * 128 + g * 16), xv[2 * ks + 1] * ldsr<f32x4>(lds0 + L_SROW + w * 256 + ks * 128 + 64 + g * 16)), ry, lane * 16, w * 2048 + ks * 1024);
  STAMP(4);
  if (w == 6 && B.tm == B.tn) __hip_atomic_store((float*)(ws + WS_S1) + B.tn * 64 + lane, s, __ATOMIC_RELAXED, __HIP_MEMORY_SCOPE_AGENT);
  { const float mean = xg_sum(hsum4((xv[0] + xv[1]) + (xv[2] + xv[3]))) * (1.f / 64); f32x4 q = zero;
    UNR for (int cc = 0; cc < 4; cc++) { const f32x4 d = xv[cc] - mean; q = fma4(d, d, q); }
    const float m2 = xg_sum(hsum4(q));
    if (g == 0) st_dev8(__builtin_bit_cast(u32x2, f32x2{mean, m2}), mkrsrc(ws, WS_ST1 + (size_t)(B.tm * 16 + B.tn) * 1024), r * 8, w * 128); }
  waitvm<0>(); pin(ep);
  STAMP(5);
  return __builtin_bit_cast(u64, ep) + 0x9E3779B97F4A7C15ull;
}

DI void loader_role(const Args& A, int lw, unsigned lds0, u64 tag, unsigned& ts) {
  char* ws = A.ws; const Blk B2 = blk(), B1 = blk1(); const f32x4 zero = {0, 0, 0, 0}; const u32x4 mark = {~0u, ~0u, ~0u, ~0u};
  int lane = threadIdx.x & 63; pin(lane);
  Loader<2> G2;
  {
    const Blk& B = B1; const int g = lane >> 4, r = lane & 15, q = r >> 2, p = lane & 3, r8 = lane >> 3;
    Loader<0> G; G.init(A.out, 16, A.wqkv, 0, B.tn * 64, 0, B.rot, B.tm, lw, lane, lds0);
    const rsrc_t ruv = mkrsrc(ws, WS_UV1 + B.tn * 12 * 2048);
    UV<0, 3> U; Stats<16> S; U.prep(lw, lane, lds0);
    const bool all0 = panel_sync(ws, 0, B, B, lw, lane, tag, [&]() { UNR for (int s = 0; s < 3; s++) G.issueB<false>(s); });
    STAMP(11);
    const int t1 = 3 + (B.tm * 5 >> 3), t3 = 3 + (((B.tm + 8) & 15) * 5 >> 3);
    const char* sap = ws + WS_MODP + (size_t)(64 + B.rot) * 4096; u32x4 sp[4]; unsigned s1;
    G.run<15>([&](int t) { int e = 0;
        if (t == 0) { U.read(); if (!all0) wait_flags(flagp(ws, B.tm * 16 + B.tn, 1, lane & 31), tag, ~0ull); }
        if (t == 1) { UNR for (int i = 0; i < 4; i++) gld_dev(sp[i], sap + i * 1024, lw == 1 ? lane * 16 : 0); gld4_dev(s1, ws + WS_S1 + B.rot * 256, lane * 4);
          UNR for (int c = 0; c < 2; c++) st_dev(mark, mkrsrc(ws, WS_KV1 + (size_t)(B.tn * 4 + lw) * 32768), lane * 16, B.tm * 2048 + c * 1024);
          e = 7; }
        if (t == 2) { UNR for (int c = 0; c < 2; c++) st_dev(mark, mkrsrc(ws, WS_KV2 + (size_t)(B.tn * 16 + B.tm) * 8192), lane * 16, lw * 2048 + c * 1024);
          if (r8 < 5) st_dev(mark, mkrsrc(ws, WS_ZP), (r8 == 0 ? (B.tn * 4 + lw) * 2048 : (1 << 17) + (B.tn * 16 + lw * 4 + r8 - 1) * 2048) + (lane & 7) * 16, B.tm * 128);
          e = 3; }
        if (t == 3 && PG) { const unsigned i = (B.j * 64 + lane) << PG;
          const char* p = lw == 0 ? ws + WS_KV1 + min(i, 27u << 20) : lw == 1 ? (const char*)A.w1 + min(i, D_ * F_ * 2u - 4) : lw == 2 ? (const char*)A.w2 + min(i, D_ * F_ * 2u - 4) : i < (2u << 20) ? (const char*)A.wo + i : ws + min(i - (2u << 20), 3u << 20);
          touch_any(p, lds0 + L_DUM); e = 1; }
        if (t == 4) { pin(sp); pin(s1); if (lw == 0) put_et(lds0, 0, lane, __uint_as_float(s1)); if (lw == 1) put_et(lds0, 1, modcol(lane), modvec(ws, WS_MODP + (64 + B.rot) * 4096, lane, sp, true)); }
        if (t == 5) { U.publish(ruv, B.tm, lw, lane, lds0); e = 3; }
        return e; },
      [&](int t) { int e = 0;
        if (t == t1 && !(EV & 1)) {
          const char* wb = opq((const char*)A.wm) + (size_t)B.tm * 128 * MODN + (lw + 2) * 2 * D_ + B.tn * 128; const unsigned wv = r8 * MODN * 2 + (((lane & 7) ^ (((r8 >> 1) & 3) << 1)) * 16);
          const unsigned l0 = opq(lds0 + L_REST) + lw * 8192;
          UNR for (int i = 0; i < 8; i++) glds16nt(wb + i * 16 * MODN, wv, l0 + i * 1024);
          e += 8; }
        if (t == t3 && !(EV & 2)) { touch(opq((const char*)A.w1) + (size_t)(blockIdx.x * 4 + lw) * 7168, min(lane, 55) * 128, lds0 + L_DUM); touch(opq((const char*)A.wo) + (size_t)(blockIdx.x * 4 + lw) * 2048, (lane & 15) * 128, lds0 + L_DUM); e += 2; }
        if ((PT & 4) && t == 11 && lw == 0) { touch_code(lane, lds0 + L_DUM); e += 1; }
        return e; },
      [&]() { U.fetch(opq(ws) + ruv.base, lw, lane); S.fetch(opq(ws) + WS_ST1, B.tm, lw, lane); glds16nt((const char*)A.wphi + B.tn * 4096 + lw * 1024, lane * 16, lds0 + L_WPHI + lw * 1024); });
    G2.init(ws + WS_O, 16, A.wo, 0, B2.tn * 64, 0, B2.rot, B2.tm, lw, lane, lds0);
    { const unsigned rv0 = r8 * 2048 + (((lane & 7) ^ (r8 >> 1)) * 16);
      UNR for (int i = 0; i < 4; i++) glds16nt((const char*)A.x + ((size_t)(B2.tm * 128 + (lw * 4 + i) * 8) * D_ + B2.tn * 64) * 2, (i & 1) ? rv0 ^ 64 : rv0, lds0 + L_RES + (lw * 4 + i) * 1024); }
    G2.issueB<false>(0); G2.issueB<false>(1);
    __builtin_amdgcn_s_setprio(3);
    waitvm<8>(); U.finish(ruv, lw, lane, lds0 + L_UVT); S.finish(lw, lane, lds0 + L_STT);
    __builtin_amdgcn_s_setprio((EV & 8) ? 3 : 0);
    lbar();
    bf16x8 wf[2];
    UNR for (int c = 0; c < 2; c++) wf[c] = wfrag(lds0 + L_WPHI + (lw & 1) * 2048 + (4 * g + q) * 64 + c * 32 + p * 8, 1024);
    bar();
    { const unsigned trw = lds0 + L_TR + lw * 8192 + r * 64 + ((g ^ ((r >> 1) & 3)) * 16); bf16x8 a, b, c, d;
      feat2(wf, ldsr<bf16x8>(lds0 + L_KX + lw * 2048 + lane * 16), ldsr<bf16x8>(lds0 + L_KX + lw * 2048 + 1024 + lane * 16), a, b, c, d);
      ldsw<bf16x8>(trw + 2048, a); ldsw<bf16x8>(trw + 2048 + 4096, b); ldsw<bf16x8>(trw + 3072, c); ldsw<bf16x8>(trw + 3072 + 4096, d); }
    lbar();
    __builtin_amdgcn_s_setprio(3);
    const int hd = lw >> 1, sg = lw & 1;
    const u32x4 one4 = {0x3f803f80u, 0x3f803f80u, 0x3f803f80u, 0x3f803f80u}; const bf16x8 ones = __builtin_bit_cast(bf16x8, one4);
    const unsigned tra = lds0 + L_TR + hd * 8192 + sg * 4096 + (4 * g + q) * 64 + ((p ^ (((4 * g + q) >> 1) & 3)) * 16), vfa = lds0 + L_VF + hd * 4096 + lane * 16;
    f32x4 kv[2][2] = {{zero, zero}, {zero, zero}}, z[2] = {zero, zero};
    UNR for (int k = 0; k < 4; k++) { const bf16x8 v0 = ldsr<bf16x8>(vfa + (k >> 1) * 8192 + (k & 1) * 2048), v1 = ldsr<bf16x8>(vfa + (k >> 1) * 8192 + (k & 1) * 2048 + 1024);
      UNR for (int ft = 0; ft < 2; ft++) { const bf16x8 af = wfrag(tra + (k >> 1) * 16384 + (k & 1) * 2048 + ft * 8, 1024);
        kv[ft][0] = mfma(af, v0, kv[ft][0]); kv[ft][1] = mfma(af, v1, kv[ft][1]); z[ft] = mfma(af, ones, z[ft]); } }
    const rsrc_t rk = mkrsrc(ws, WS_KV1 + (size_t)(B.tn * 4 + lw) * 32768), rz = mkrsrc(ws, WS_ZP + (size_t)(B.tn * 4 + lw) * 2048), r2 = mkrsrc(ws, WS_KV2 + (size_t)B.tn * 131072);
    UNR for (int c = 0; c < 2; c++) st_dev(pack8u(kv[0][c], kv[1][c]), rk, lane * 16, B.tm * 2048 + c * 1024);
    { const bool o = lane & 1; const f32x4 zs = {o ? z[1][0] : z[0][0], o ? z[1][1] : z[0][1], o ? z[1][2] : z[0][2], o ? z[1][3] : z[0][3]};
      if ((lane & 14) == 0) st_dev(__builtin_bit_cast(u32x4, zs), rz, (lane & 1) * 64 + g * 16, B.tm * 128); }
    STAMP(16);
    { const f32x4 d = gemv64(lds0 + L_REST + lw * 8192, ldsr<bf16x8>(lds0 + L_SIL2 + g * 16), ldsr<bf16x8>(lds0 + L_SIL2 + 64 + g * 16), lane);
      if (r < 4) st_dev(__builtin_bit_cast(u32x4, d), mkrsrc(ws, WS_MODP + (size_t)((lw * 16 + B.tn) * 16 + B.tm) * 256), r * 64 + g * 16, 0); }
    u32x4 h[4]; poll(h, [&](int i) { return i < 2 ? ld_dev(rk, (lane >> 3) * 2048 + (lane & 7) * 16, B.tm * 128 + i * 16384) : ld_dev(rz, r * 128 + g * 16, (i - 2) * 64); });
    { float e[4], o[4];
      UNR for (int k = 0; k < 4; k++) { e[k] = fadd(blo(h[0][k]), blo(h[1][k])); o[k] = fadd(bhi(h[0][k]), bhi(h[1][k])); }
      float te = fold4<false>(e[0], e[1], e[2], e[3]), to = fold4<false>(o[0], o[1], o[2], o[3]); te += dpp<0x128>(te); to += dpp<0x128>(to);
      spread4(__uint_as_float(cvtpk(te, to)), e[0], e[1], e[2], e[3]);
      const u32x4 sum = {__float_as_uint(e[0]), __float_as_uint(e[1]), __float_as_uint(e[2]), __float_as_uint(e[3])};
      UNR for (int i = 0; i < 2; i++) st_dev(sum, r2, (lane >> 3) * 8192 + (lane & 7) * 16, i * 65536 + lw * 2048 + B.tm * 128); }
    STAMP(17);
    ldsw<bf16x8>(lds0 + L_KX + lw * 1024 + lane * 16, pack8(row16_sum4(asf(h[2])), row16_sum4(asf(h[3]))));
    lbar();
    __builtin_amdgcn_s_setprio((EV & 8) ? 3 : 0);
  }
  const Blk& B = B2; const int odd = B.tn & 1, q4 = (B.tm >> 3) + 2 * ((B.tm >> 2) & 1);
  Loader<1> G3;
  {
    G2.issueB<false>(2); G2.issueB<false>(3);
    panel_sync(ws, 3, B1, B, lw, lane, tag, [&]() {});
    if (blockIdx.x == 0 && lw == 0 && lane == 0) __hip_atomic_store((u64*)(ws + WS_EPOCH), tag, __ATOMIC_RELAXED, __HIP_MEMORY_SCOPE_AGENT);
    STAMP(21);
    G3.init(A.out, 16, A.w1, 0, B.tn * 224 + odd * 32, B.tn * 224 + (odd ? 0 : 192), q4 * 4, B.tm, lw, lane, lds0);
    G2.run<7>([&](int) { return 0; }, [&](int) { return 0; }, [&]() { G3.issueB<false>(0); });
  }
  Loader<3> G4;
  {
    const rsrc_t ruv = mkrsrc(ws, WS_UV2 + B.tn * 16 * 2048);
    UV<1, 4> U; Stats<32> S; const int tu = B.tm & 3, t3 = 5 + (B.j & 7);
    U.prep(lw, lane, lds0 + (tu == 1 ? 0 : tu == 2 ? 1 : 2) * Lay<1>::SBB);
    panel_sync(ws, 4, B, B, lw, lane, tag, [&]() { G3.issueB<false>(1); G3.issueB<false>(2); });
    STAMP(27);
    auto h1pre = [&](int t) {
      if (EV & 16) { const int fp = B.tn * 7 + (t >> 1); st_dev(mark, mkrsrc(ws, WS_H1 + (B.tm * 56 + (fp >> 1)) * 16384), lane * 16, (fp & 1) * 1024 + (lw * 2 + (t & 1)) * 2048); return 1; } return 0; };
    G3.run<8>([&](int t) { int e = 0;
        if (t == 0 || (t < 4 && t == tu)) U.read();
        if (t == 4) { U.publish(ruv, B.tm, lw, lane, lds0); e = 4; }
        if (t < 3) e += h1pre(t);
        return e; },
      [&](int t) { int e = h1pre(t);
        if (t == t3 && !(EV & 4)) { touch(opq((const char*)A.w2) + (size_t)(blockIdx.x * 4 + lw) * 7168, min(lane, 55) * 128, lds0 + L_DUM); e += 1; }
        if ((PT & 4) && t == 11 && lw == 0) { touch_code(lane, lds0 + L_DUM); e += 1; }
        return e; },
      [&]() { U.fetch(opq(ws) + ruv.base, lw, lane); });
    S.fetch(opq(ws) + WS_ST2, B.tm, lw, lane);
    G4.init(ws + WS_H1, 56, A.w2, (B.tn >> 3) * 28, (B.tn & 7) * 128, 0, (B.rot * 28) >> 4, B.tm, lw, lane, lds0);
    G4.issueB<false>(0); G4.issueB<false>(1);
    __builtin_amdgcn_s_setprio(3);
    waitvm<24>(); U.finish(ruv, lw, lane, lds0 + L_UVT); waitvm<8>(); S.finish(lw, lane, lds0 + L_STT);
    __builtin_amdgcn_s_setprio((EV & 8) ? 3 : 0);
    lbar();
  }
  {
    bar();
    G4.issueB<false>(2); G4.issueB<false>(3);
    panel_sync(ws, 5, B, B, lw, lane, tag, [&]() {});
    STAMP(35);
    G4.run<0>([&](int) { return 0; }, [&](int) { return 0; }, [&]() {});
    asm volatile(".Lcode_end:");
  }
}

DI void compute_role(const Args& A, int w, unsigned lds0, unsigned& ts) {
  char* ws = A.ws; const Blk B = blk(); const f32x4 zero = {0, 0, 0, 0}; const u32x4 mark = {~0u, ~0u, ~0u, ~0u};
  const int wm = w >> 1, wn = w & 1; int lane = threadIdx.x & 63; pin(lane);
  {
    const Blk B = blk1(); const int g = lane >> 4, r = lane & 15, q = r >> 2, p = lane & 3;
    bar();
    if (w == 0) { const f32x4 d = gemv64(lds0 + L_SAU, ldsr<bf16x8>(lds0 + L_SIL2 + g * 16), ldsr<bf16x8>(lds0 + L_SIL2 + 64 + g * 16), lane);
      if (r < 4) st_dev(__builtin_bit_cast(u32x4, d), mkrsrc(ws, WS_MODP + (size_t)((64 + B.tn) * 16 + B.tm) * 256), r * 64 + g * 16, 0); }
    bar();
    f32x4 acc[4][6];
    compute_loop<0>(acc, lds0, wm, wn, lane, ts);
    STAMP(13);
    bar();
    STAMP(14);
    constexpr float L2E = 1.4426950408889634f;
    bf16x8 wf[2]; RowSt st[4];
    UNR for (int m = 0; m < 4; m++) st[m].set(lds0 + L_STT + (wm * 64 + m * 16 + r) * 4, L2E);
    const unsigned uva = lds0 + L_UVT + wn * 256;
    bf16x8 kf[4];
    { const f32x4 u0 = ldsr<f32x4>(uva + 512 + g * 16), v0 = ldsr<f32x4>(uva + 576 + g * 16) * L2E, u1 = ldsr<f32x4>(uva + 640 + g * 16), v1 = ldsr<f32x4>(uva + 704 + g * 16) * L2E;
      UNR for (int c = 0; c < 2; c++) wf[c] = wfrag(lds0 + L_WPHI + wn * 2048 + (4 * g + q) * 64 + c * 32 + p * 8, 1024);
      UNR for (int mm = 0; mm < 4; mm++) { const int m = mm ^ 2; kf[m] = pack8(lnf(acc[m][2], st[m], u0, v0), lnf(acc[m][3], st[m], u1, v1));
        if (m >= 2) ldsw<bf16x8>(lds0 + L_KX + w * 2048 + (m - 2) * 1024 + lane * 16, kf[m]);
        if (m == 3) { lbar(); asm volatile("" : "+v"(acc[0][2]), "+v"(acc[0][3]), "+v"(acc[1][2]), "+v"(acc[1][3])); } } }
    { const unsigned trw = lds0 + L_TR + w * 8192 + r * 64 + ((g ^ ((r >> 1) & 3)) * 16); bf16x8 a, b, c, d; feat2(wf, kf[0], kf[1], a, b, c, d);
      ldsw<bf16x8>(trw, a); ldsw<bf16x8>(trw + 4096, b); ldsw<bf16x8>(trw + 1024, c); ldsw<bf16x8>(trw + 1024 + 4096, d); }
    { float u[2], v[2];
      UNR for (int c = 0; c < 2; c++) { u[c] = ldsr<float>(uva + 1024 + c * 128 + r * 4); v[c] = ldsr<float>(uva + 1088 + c * 128 + r * 4); asm("" : "+v"(u[c])); asm("" : "+v"(v[c])); }
      UNR for (int m = 0; m < 4; m++) { const unsigned a = lds0 + L_STT + (wm * 64 + m * 16 + 4 * g) * 4; const f32x4 rs = ldsr<f32x4>(a), nm = ldsr<f32x4>(a + 512);
        UNR for (int c = 0; c < 2; c++) acc[m][4 + c] = fma4(acc[m][4 + c], rs, fma4(nm, spl(u[c]), spl(v[c]))); }
      UNR for (int uu = 0; uu < 2; uu++) UNR for (int c = 0; c < 2; c++) ldsw<bf16x8>(lds0 + L_VF + w * 4096 + uu * 2048 + c * 1024 + lane * 16, pack8(acc[2 * uu][4 + c], acc[2 * uu + 1][4 + c])); }
    lbar();
    STAMP(15);
    asm volatile("" : "+v"(acc[0][0]), "+v"(acc[0][1]), "+v"(acc[1][0]), "+v"(acc[1][1]), "+v"(acc[2][0]), "+v"(acc[2][1]), "+v"(acc[3][0]), "+v"(acc[3][1]));
    bf16x8 qp[4], qn[4];
    { const f32x4 u0 = ldsr<f32x4>(uva + g * 16), v0 = ldsr<f32x4>(uva + 64 + g * 16) * L2E, u1 = ldsr<f32x4>(uva + 128 + g * 16), v1 = ldsr<f32x4>(uva + 192 + g * 16) * L2E;
      UNR for (int m = 0; m < 4; m += 2) feat2(wf, pack8(lnf(acc[m][0], st[m], u0, v0), lnf(acc[m][1], st[m], u1, v1)),
                                               pack8(lnf(acc[m + 1][0], st[m + 1], u0, v0), lnf(acc[m + 1][1], st[m + 1], u1, v1)), qp[m], qn[m], qp[m + 1], qn[m + 1]); }
    asm volatile("" : "+v"(qp[0]), "+v"(qp[1]), "+v"(qp[2]), "+v"(qp[3]), "+v"(qn[0]), "+v"(qn[1]), "+v"(qn[2]), "+v"(qn[3]) :: "memory");
    STAMP(16);
    bar();
    STAMP(17);
    const rsrc_t r2 = mkrsrc(ws, WS_KV2 + (size_t)(B.tn * 16 + B.tm) * 8192 + wn * 4096);
    u32x4 kk[4]; UNR for (int i = 0; i < 4; i++) kk[i] = ld_dev(r2, lane * 16, i * 1024);
    const bf16x8 z0 = ldsr<bf16x8>(lds0 + L_KX + wn * 2048 + lane * 16), z1 = ldsr<bf16x8>(lds0 + L_KX + wn * 2048 + 1024 + lane * 16);
    float rd[4];
    UNR for (int m = 0; m < 4; m++) rd[m] = __builtin_amdgcn_rcpf(mfma(z1, qn[m], mfma(z0, qp[m], zero))[0] + 1e-6f);
    asm volatile("" : "+v"(rd[0]), "+v"(rd[1]), "+v"(rd[2]), "+v"(rd[3]));
    if (__any(max(max(umax4(kk[0]), umax4(kk[1])), max(umax4(kk[2]), umax4(kk[3]))) == 0xffffffffu)) poll(kk, [&](int i) { return ld_dev(r2, lane * 16, i * 1024); });
    const rsrc_t ro = mkrsrc(ws, WS_O + (size_t)(B.tm * 16 + B.tn) * 16384);
    UNR for (int m = 0; m < 4; m++) {
      const f32x4 n0 = mfma(__builtin_bit_cast(bf16x8, kk[2]), qn[m], mfma(__builtin_bit_cast(bf16x8, kk[0]), qp[m], zero)), n1 = mfma(__builtin_bit_cast(bf16x8, kk[3]), qn[m], mfma(__builtin_bit_cast(bf16x8, kk[1]), qp[m], zero));
      st_dev(pack8u(n0 * rd[m], n1 * rd[m]), ro, lane * 16, (wm * 8 + wn) * 1024 + m * 2048);
    }
    STAMP(18);
    waitvm<0>(); bar();
    STAMP(19);
  }
  pin(lane);
  u32x4 o[4];
  {
    const int g = lane >> 4, r = lane & 15;
    { const float v = modvec(ws, WS_MODP + (size_t)((w == 0 ? 0 : w == 3 ? 1 : 2) * 16 + (w < 2 ? B.tn : ((B.tm >> 3) + 2 * ((B.tm >> 2) & 1)) * 4 + (B.tm & 3))) * 4096, lane), s = (w == 1 || w == 2) ? rbf(1.f + v) : v;
      if (w < 2) ldsw<float>(lds0 + L_GT + w * 256 + modcol(lane) * 4, s); else put_et(lds0, w - 2, modcol(lane), s); }
    f32x4 acc[4][2];
    bar();
    compute_loop<2>(acc, lds0, wm, wn, lane, ts);
    STAMP(23);
    f32x4 gate[2], sc[2]; u32x2 xr[4][2];
    UNR for (int n = 0; n < 2; n++) { gate[n] = ldsr<f32x4>(lds0 + L_GT + wn * 128 + n * 64 + g * 16); sc[n] = ldsr<f32x4>(lds0 + L_GT + 256 + wn * 128 + n * 64 + g * 16); }
    UNR for (int m = 0; m < 4; m++) UNR for (int n = 0; n < 2; n++) xr[m][n] = ldsr<u32x2>(lds0 + L_RES + (wm * 64 + m * 16 + r) * 128 + (((wn * 4 + n * 2 + (g >> 1)) ^ (r >> 1)) * 16) + (g & 1) * 8);
    const size_t tb = (size_t)(B.tm * 16 + B.tn) * 16384; const rsrc_t ry = outrsrc(A.out, tb), rst = mkrsrc(ws, WS_ST2 + (size_t)(B.tm * 32 + B.tn * 2 + wn) * 1024);
    f32x4 v[4][2]; float mean[4], m2[4];
    UNR for (int m = 0; m < 4; m++) {
      o[m] = pack8u(fma4(gate[0], acc[m][0], unp(xr[m][0][0], xr[m][0][1])), fma4(gate[1], acc[m][1], unp(xr[m][1][0], xr[m][1][1]))); v[m][0] = unp(o[m][0], o[m][1]); v[m][1] = unp(o[m][2], o[m][3]);
      st_dev(pack8u(v[m][0] * sc[0], v[m][1] * sc[1]), ry, lane * 16, (wm * 8 + wn) * 1024 + m * 2048); }
    UNR for (int m = 0; m < 4; m++) mean[m] = hsum4(v[m][0] + v[m][1]);
    const float mf = fold4<false>(mean[0], mean[1], mean[2], mean[3]) * (1.f / 32); spread4(mf, mean[0], mean[1], mean[2], mean[3]);
    UNR for (int m = 0; m < 4; m++) { const f32x4 d0 = v[m][0] - mean[m], d1 = v[m][1] - mean[m]; m2[m] = hsum4(fma4(d0, d0, d1 * d1)); }
    st_dev8(__builtin_bit_cast(u32x2, f32x2{mf, fold4<false>(m2[0], m2[1], m2[2], m2[3])}), rst, (wm * 64 + ((g >> 1) + 2 * (g & 1)) * 16 + r) * 8, 0);
    STAMP(24);
    waitvm<0>(); bar();
    STAMP(25);
  }
  pin(lane);
  {
    const int g = lane >> 4, r = lane & 15, odd = B.tn & 1;
    const int fp0 = B.tn * 7 + odd + wn * 3, fph = odd ? B.tn * 7 : B.tn * 7 + 6;
    if (w < 2) ldsw<float>(lds0 + L_GT + w * 256 + modcol(lane) * 4, modvec(ws, WS_MODP + (size_t)(48 + (B.tn & 7) * 2 + w) * 4096, lane));
    u32x2 bb[7];
    UNR for (int n = 0; n < 7; n++) bb[n] = *(const u32x2*)(A.b1 + (n < 6 ? fp0 * 32 + n * 16 : fph * 32 + wn * 16) + 4 * g);
    pin(bb);
    UNR for (int i = 0; i < 4; i++) st_dev(mark, mkrsrc(ws, xchoff(B.tm, B.tn, w)), lane * 16, i * 1024);
    f32x4 acc[4][7];
    bar();
    { const rsrc_t rx1 = mkrsrc(ws, WS_X1 + (size_t)(B.tm * 16 + B.tn) * 16384);
      UNR for (int m = 0; m < 4; m++) st_dev(o[m], rx1, lane * 16, (wm * 8 + wn) * 1024 + m * 2048); }
    compute_loop<1>(acc, lds0, wm, wn, lane, ts);
    STAMP(29);
    bar();
    STAMP(30);
    f32x4 u[7], v[7];
    UNR for (int n = 0; n < 7; n++) { const unsigned a = lds0 + L_UVT + (n < 6 ? wn * 6 + n : 12 + wn) * 128 + g * 16; u[n] = ldsr<f32x4>(a); v[n] = ldsr<f32x4>(a + 64) + unp(bb[n][0], bb[n][1]); }
    const rsrc_t rh = mkrsrc(ws, WS_H1 + (size_t)B.tm * 56 * 16384);
    RowSt st[4]; u32x2 h6[4];
    UNR for (int m = 0; m < 4; m++) st[m].set(lds0 + L_STT + (wm * 64 + m * 16 + r) * 4, 1.f);
    auto act = [&](int m, int n) { const f32x4 t = __builtin_elementwise_max(lnf(acc[m][n], st[m], u[n], v[n]), zero); return t * t; };
    UNR for (int n = 0; n < 6; n += 2) { const int fp = fp0 + (n >> 1);
      UNR for (int m = 0; m < 4; m++) st_dev(pack8u(act(m, n), act(m, n + 1)), rh, lane * 16, (fp >> 1) * 16384 + (fp & 1) * 1024 + wm * 8192 + m * 2048); }
    UNR for (int m = 0; m < 4; m++) { const f32x4 t = act(m, 6); h6[m] = u32x2{cvtpk(t[0], t[1]), cvtpk(t[2], t[3])}; }
    UNR for (int m = 0; m < 4; m += 2) { const auto a = __builtin_amdgcn_permlane16_swap(h6[m][0], h6[m + 1][0], false, false), b = __builtin_amdgcn_permlane16_swap(h6[m][1], h6[m + 1][1], false, false);
      st_dev(u32x4{a[0], b[0], a[1], b[1]}, rh, (g & 1) * 2048 + (g >> 1) * 256 + r * 16, (fph >> 1) * 16384 + (fph & 1) * 1024 + wm * 8192 + m * 2048 + wn * 512); }
    STAMP(31);
    bar(); waitvm<0>(); bar();
    STAMP(32);
  }
  pin(lane);
  {
    const int g = lane >> 4, r = lane & 15, kh = B.tn >> 3, cg = (B.tn & 7) * 2 + wn;
    u32x2 bb[4]; u32x4 xr[2][2]; const rsrc_t rx1 = mkrsrc(ws, WS_X1 + (size_t)(B.tm * 16 + cg) * 16384);
    UNR for (int n = 0; n < 4; n++) bb[n] = *(const u32x2*)(A.b2 + cg * 64 + n * 16 + 4 * g);
    pin(bb);
    f32x4 acc[4][4];
    bar();
    UNR for (int mm = 0; mm < 2; mm++) UNR for (int ks = 0; ks < 2; ks++) xr[mm][ks] = ld_dev(rx1, lane * 16, ((wm * 4 + kh * 2 + mm) * 2 + ks) * 1024);
    compute_loop<3>(acc, lds0, wm, wn, lane, ts);
    STAMP(37);
    pin(xr[0]); pin(xr[1]);
    const rsrc_t rx = mkrsrc(ws, xchoff(B.tm, B.tn, w)), rp = mkrsrc(ws, xchoff(B.tm, B.tn ^ 8, w));
    f32x4 keep[2][4], gate[4];
    if (kh) { UNR for (int i = 0; i < 4; i++) st_dev(pack8u(acc[i >> 1][(i & 1) * 2], acc[i >> 1][(i & 1) * 2 + 1]), rx, lane * 16, i * 1024);
              UNR for (int n = 0; n < 4; n++) { keep[0][n] = acc[2][n]; keep[1][n] = acc[3][n]; } }
    else    { UNR for (int i = 0; i < 4; i++) st_dev(pack8u(acc[2 + (i >> 1)][(i & 1) * 2], acc[2 + (i >> 1)][(i & 1) * 2 + 1]), rx, lane * 16, i * 1024);
              UNR for (int n = 0; n < 4; n++) { keep[0][n] = acc[0][n]; keep[1][n] = acc[1][n]; } }
    UNR for (int n = 0; n < 4; n++) gate[n] = ldsr<f32x4>(lds0 + L_GT + wn * 256 + n * 64 + g * 16);
    UNR for (int mm = 0; mm < 2; mm++) UNR for (int n = 0; n < 4; n++) keep[mm][n] = fma4(gate[n], keep[mm][n] + unp(bb[n][0], bb[n][1]), unp(xr[mm][n >> 1][(n & 1) * 2], xr[mm][n >> 1][(n & 1) * 2 + 1]));
    pin(keep[0]); pin(keep[1]);
    u32x4 pp[4]; poll(pp, [&](int i) { return ld_dev(rp, lane * 16, i * 1024); });
    STAMP(38);
    const unsigned ot = lds0 + L_OT + w * 4096;
    UNR for (int mm = 0; mm < 2; mm++) UNR for (int n = 0; n < 4; n++) { const u32x4 t = pp[mm * 2 + (n >> 1)]; const f32x4 y = fma4(gate[n], unp(t[(n & 1) * 2], t[(n & 1) * 2 + 1]), keep[mm][n]);
      ldsw<u32x2>(ot + (mm * 16 + r) * 128 + (((n * 2 + (g >> 1)) ^ (r >> 1)) * 16) + (g & 1) * 8, u32x2{cvtpk(y[0], y[1]), cvtpk(y[2], y[3])}); }
    lwait();
    const int r8 = lane >> 3; const unsigned rv0 = r8 * 2048 + (((lane & 7) ^ (r8 >> 1)) * 16);
    char* op = (char*)A.out + ((size_t)(B.tm * 128 + wm * 64 + kh * 32) * D_ + cg * 64) * 2;
    UNR for (int i = 0; i < 4; i++) *(u32x4*)(op + i * 16384 + ((i & 1) ? rv0 ^ 64 : rv0)) = ldsr<u32x4>(ot + i * 1024 + lane * 16);
    STAMP(39);
  }
}

__global__ __launch_bounds__(512, 2) void k_block(Args A) {
  __shared__ __attribute__((aligned(1024))) char smem[L_TOT];
  const int w = __builtin_amdgcn_readfirstlane(threadIdx.x >> 6);
  const unsigned lds0 = (unsigned)(size_t)(LAS char*)smem;
  asm volatile(".Lcode_begin:");
  if ((PT & 2) && w == 3) touch_code(threadIdx.x & 63, lds0 + L_DUM);
  unsigned ts = 0; STAMP(0);
  const u64 tag = phase0(A, w, lds0, ts);
  if (w >= 4) loader_role(A, w - 4, lds0, tag, ts); else compute_role(A, w, lds0, ts);
  if (blockIdx.x == 77 && w == 0) { const unsigned s0 = __builtin_amdgcn_readlane(ts, 0); volatile unsigned* sp = (volatile unsigned*)(A.ws + WS_STAMP);
    if (MODE == 3) sp[0] = s0;
    if (MODE <= 2 || MODE == 5) { const unsigned t = MODE == 5 ? s0 : __builtin_amdgcn_readfirstlane(sp[0]); const unsigned until = t + 10000 + (MODE == 2 ? 10 * (s0 - t) : 0);
      for (int i = 0; i < 100000 && (int)(until - (unsigned)__builtin_amdgcn_s_memrealtime()) > 0; i++) __builtin_amdgcn_s_sleep(1); } }
}

__global__ void k_head(char* ws) { if (threadIdx.x == 0) *(volatile unsigned*)(ws + WS_STAMP) = (unsigned)__builtin_amdgcn_s_memrealtime(); }
__global__ void k_tail(char* ws) { const unsigned until = __builtin_amdgcn_readfirstlane(*(volatile unsigned*)(ws + WS_STAMP)) + 10000;
  for (int i = 0; i < 100000 && (int)(until - (unsigned)__builtin_amdgcn_s_memrealtime()) > 0; i++) __builtin_amdgcn_s_sleep(1); }
extern "C" void kernel_launch(void* const* d_in, const int* in_sizes, int n_in, void* d_out, int out_size, void* d_ws, size_t ws_size, hipStream_t stream) {
  Args A; A.x = (const u16*)d_in[0]; A.wqkv = (const u16*)d_in[1]; A.wphi = (const u16*)d_in[2]; A.wo = (const u16*)d_in[3]; A.w1 = (const u16*)d_in[5]; A.b1 = (const u16*)d_in[6];
  A.w2 = (const u16*)d_in[7]; A.b2 = (const u16*)d_in[8]; A.c = (const u16*)d_in[9]; A.wm = (const u16*)d_in[10]; A.out = (u16*)d_out; A.ws = (char*)d_ws;
  if (MODE == 1 || MODE == 2 || MODE == 4) k_head<<<1, 64, 0, stream>>>(A.ws);
  k_block<<<256, 512, 0, stream>>>(A);
  if (MODE == 3 || MODE == 4) k_tail<<<1, 64, 0, stream>>>(A.ws);
}
```

```cpp
#define STAMPS
#define DIAG0 0
#define DIAG 22
#define P0V 1029
#define NONT 1
#include <hip/hip_runtime.h>
#include <hip/hip_bf16.h>

typedef unsigned short u16;
typedef unsigned u32x4 __attribute__((ext_vector_type(4)));
typedef unsigned u32x2 __attribute__((ext_vector_type(2)));
typedef short s16x4 __attribute__((ext_vector_type(4)));
typedef __bf16 bf16x8 __attribute__((ext_vector_type(8)));
typedef __bf16 bf16x2 __attribute__((ext_vector_type(2)));
typedef float f32x2 __attribute__((ext_vector_type(2)));
typedef float f32x4 __attribute__((ext_vector_type(4)));
#define LAS __attribute__((address_space(3)))
#define DI __device__ __forceinline__
#define SB __builtin_amdgcn_sched_barrier(0)
#define UNR _Pragma("unroll")

constexpr int D_ = 1024, F_ = 3584, MODN = 6 * D_;
constexpr size_t WS_EPOCH = 0;
constexpr size_t WS_FLAG  = 1 << 16;
constexpr size_t WS_GX    = 1 << 18;
constexpr size_t WS_ST1   = 1 << 19;
constexpr size_t WS_ST2   = WS_ST1 + (1 << 18);
constexpr size_t WS_MODP  = 3 << 19;
constexpr size_t WS_UV1   = WS_MODP + 4 * 65536;
constexpr size_t WS_ZP    = WS_UV1 + 16 * 12 * 2048;
constexpr size_t WS_UV2   = WS_ZP + (1 << 17);
constexpr size_t WS_KV1   = 10 << 20;
constexpr size_t WS_KV2   = WS_KV1 + (2 << 20);
constexpr size_t WS_O     = 14 << 20;
constexpr size_t WS_X1    = 18 << 20;
constexpr size_t WS_H1    = 22 << 20;
constexpr size_t WS_STAMP = 40 << 20;
constexpr int SPIN_CAP = 1 << 14;
typedef unsigned long long u64;

#define STAMP(i) do if ((i) == 0 || (i) == DIAG0 || (i) == DIAG) { u64 t_; asm volatile("s_memrealtime %0\n\ts_waitcnt lgkmcnt(0)" : "=s"(t_) :: "memory"); ts = (int)(threadIdx.x & 63) == (i) ? (unsigned)t_ : ts; asm volatile("" : "+v"(ts)); } while (0)

DI void glds16(const void* sbase, unsigned voff, unsigned ldsaddr) {
  asm volatile("s_mov_b32 m0, %2\n\ts_nop 0\n\tglobal_load_lds_dwordx4 %0, %1" :: "v"(voff), "s"(sbase), "s"(ldsaddr) : "memory");
}
DI void glds16nt(const void* sbase, unsigned voff, unsigned ldsaddr) {
  glds16(sbase, voff, ldsaddr); return;
  asm volatile("s_mov_b32 m0, %2\n\ts_nop 0\n\tglobal_load_lds_dwordx4 %0, %1 nt" :: "v"(voff), "s"(sbase), "s"(ldsaddr) : "memory");
}
DI void glds16x4(const void* sbase, unsigned voff, unsigned ldsaddr) {
  asm volatile("s_mov_b32 m0, %2\n\ts_nop 0\n\tglobal_load_lds_dwordx4 %0, %1 sc1\n\tglobal_load_lds_dwordx4 %0, %1 offset:1024 sc1\n\t"
               "global_load_lds_dwordx4 %0, %1 offset:2048 sc1\n\tglobal_load_lds_dwordx4 %0, %1 offset:3072 sc1" :: "v"(voff), "s"(sbase), "s"(ldsaddr) : "memory");
}
DI void glds16dev(const void* sbase, unsigned voff, unsigned ldsaddr) {
  asm volatile("s_mov_b32 m0, %2\n\ts_nop 0\n\tglobal_load_lds_dwordx4 %0, %1 sc1" :: "v"(voff), "s"(sbase), "s"(ldsaddr) : "memory");
}
DI void prio(int p) { if (p) __builtin_amdgcn_s_setprio(3); else __builtin_amdgcn_s_setprio(0); }
DI void touch(const void* sbase, unsigned voff, unsigned ldsdummy) {
  asm volatile("s_mov_b32 m0, %2\n\ts_nop 0\n\tglobal_load_lds_dword %0, %1" :: "v"(voff), "s"(sbase), "s"(ldsdummy) : "memory");
}
DI void touch_any(const void* p, unsigned ldsdummy) {
  asm volatile("s_mov_b32 m0, %1\n\ts_nop 0\n\tglobal_load_lds_dword %0, off" :: "v"(p), "s"(ldsdummy) : "memory");
}
DI void touch_nt(const void* sbase, unsigned voff, unsigned ldsdummy) {
  asm volatile("s_mov_b32 m0, %2\n\ts_nop 0\n\tglobal_load_lds_dword %0, %1 nt" :: "v"(voff), "s"(sbase), "s"(ldsdummy) : "memory");
}
DI void gld(u32x4& r, const void* sb, unsigned vo) { asm volatile("global_load_dwordx4 %0, %1, %2" : "=v"(r) : "v"(vo), "s"(sb) : "memory"); }
DI void gld_nt(u32x4& r, const void* sb, unsigned vo) { asm volatile("global_load_dwordx4 %0, %1, %2 nt" : "=v"(r) : "v"(vo), "s"(sb) : "memory"); }
DI void gld_dev(u32x4& r, const void* sb, unsigned vo) { asm volatile("global_load_dwordx4 %0, %1, %2 sc1" : "=v"(r) : "v"(vo), "s"(sb) : "memory"); }
DI void gld8(f32x2& r, const void* sb, unsigned vo) { asm volatile("global_load_dwordx2 %0, %1, %2 sc1" : "=v"(r) : "v"(vo), "s"(sb) : "memory"); }
DI void gld4_dev(unsigned& r, const void* sb, unsigned vo) { asm volatile("global_load_dword %0, %1, %2 sc1" : "=v"(r) : "v"(vo), "s"(sb) : "memory"); }
DI void gld4(unsigned& r, const void* sb, unsigned vo) { asm volatile("global_load_dword %0, %1, %2 nt" : "=v"(r) : "v"(vo), "s"(sb) : "memory"); }
template <class T> DI void pin(T& r) { asm volatile("" : "+v"(r)); }
template <class T, int N> DI void pin(T (&r)[N]) { UNR for (int i = 0; i < N; i++) pin(r[i]); }
template <class T> DI T opq(T v) { asm volatile("" : "+s"(v)); return v; }
DI f32x4 asf(u32x4 v) { return __builtin_bit_cast(f32x4, v); }
template <int N> DI void waitvm() { asm volatile("s_waitcnt vmcnt(%0)" :: "i"(N) : "memory"); }
template <int B, int LO = 0, int HI = 12> DI void waitx(int e) {
  if constexpr (LO == HI) waitvm<B + LO>();
  else if constexpr (LO == 0) { if (e == 0) waitvm<B>(); else waitx<B, 1, HI>(e); }
  else { constexpr int MID = (LO + HI + 1) / 2; if (e >= MID) waitx<B, MID, HI>(e); else waitx<B, LO, MID - 1>(e); }
}
DI void waitlgkm0() { __builtin_amdgcn_s_waitcnt(0xC07F); }
DI void lwait() { asm volatile("s_waitcnt lgkmcnt(0)" ::: "memory"); }
DI void bar() { asm volatile("s_barrier" ::: "memory"); }
DI void lbar() { asm volatile("s_waitcnt lgkmcnt(0)\n\ts_barrier" ::: "memory"); }
DI unsigned cvtpk(float a, float b) { f32x2 v = {a, b}; return __builtin_bit_cast(unsigned, __builtin_convertvector(v, bf16x2)); }
DI float rbf(float a) { return __uint_as_float(cvtpk(a, a) << 16); }
DI f32x4 rbf4(f32x4 s) { return f32x4{rbf(s[0]), rbf(s[1]), rbf(s[2]), rbf(s[3])}; }
DI float blo(unsigned u) { return __uint_as_float(u << 16); }
DI float bhi(unsigned u) { return __uint_as_float(u & 0xffff0000u); }
DI f32x4 unp(unsigned a, unsigned b) { return f32x4{blo(a), bhi(a), blo(b), bhi(b)}; }
DI u32x4 pack8u(const f32x4& a, const f32x4& b) { return u32x4{cvtpk(a[0], a[1]), cvtpk(a[2], a[3]), cvtpk(b[0], b[1]), cvtpk(b[2], b[3])}; }
DI bf16x8 pack8(const f32x4& a, const f32x4& b) { return __builtin_bit_cast(bf16x8, pack8u(a, b)); }
DI f32x4 mfma(bf16x8 a, bf16x8 b, f32x4 c) { return __builtin_amdgcn_mfma_f32_16x16x32_bf16(a, b, c, 0, 0, 0); }
DI s16x4 trd(unsigned a) { return __builtin_amdgcn_ds_read_tr16_b64_v4i16((LAS s16x4*)(size_t)a); }
DI bf16x8 cat(s16x4 lo, s16x4 hi) { return __builtin_bit_cast(bf16x8, __builtin_shufflevector(lo, hi, 0, 1, 2, 3, 4, 5, 6, 7)); }
template <class T> DI T ldsr(unsigned a) { return *(const LAS T*)(size_t)a; }
template <class T> DI void ldsw(unsigned a, T v) { *(LAS T*)(size_t)a = v; }
template <int C> DI float dpp(float x) { return __builtin_bit_cast(float, __builtin_amdgcn_update_dpp(0, __builtin_bit_cast(int, x), C, 0xf, 0xf, true)); }
DI float vmax(float a, float b) { float r; asm("v_max_f32_e32 %0, %1, %2" : "=v"(r) : "v"(a), "v"(b)); return r; }
DI float sw32_add(float a, float b) { auto r = __builtin_amdgcn_permlane32_swap(__float_as_uint(a), __float_as_uint(b), false, false); return __uint_as_float(r[0]) + __uint_as_float(r[1]); }
DI float sw16_add(float a, float b) { auto r = __builtin_amdgcn_permlane16_swap(__float_as_uint(a), __float_as_uint(b), false, false); return __uint_as_float(r[0]) + __uint_as_float(r[1]); }
DI float xg_sum(float a) { a = sw32_add(a, a); return sw16_add(a, a); }
DI float xg_max(float a) {
  auto r = __builtin_amdgcn_permlane32_swap(__float_as_uint(a), __float_as_uint(a), false, false); a = vmax(__uint_as_float(r[0]), __uint_as_float(r[1]));
  auto q = __builtin_amdgcn_permlane16_swap(__float_as_uint(a), __float_as_uint(a), false, false); return vmax(__uint_as_float(q[0]), __uint_as_float(q[1]));
}
DI float row16_sum(float a) { a += dpp<0x128>(a); a += dpp<0x124>(a); a += dpp<0x122>(a); a += dpp<0x121>(a); return a; }
DI f32x4 row16_sum4(f32x4 a) { return f32x4{row16_sum(a[0]), row16_sum(a[1]), row16_sum(a[2]), row16_sum(a[3])}; }
DI f32x4 spl(float a) { return f32x4{a, a, a, a}; }
DI f32x4 fma4(f32x4 a, f32x4 b, f32x4 c) { return __builtin_elementwise_fma(a, b, c); }
struct RowSt { float rs, nm; DI void set(unsigned a, float k) { rs = ldsr<float>(a) * k; nm = ldsr<float>(a + 512) * k; asm("" : "+v"(rs)); asm("" : "+v"(nm)); } };
DI f32x4 lnf(f32x4 acc, const RowSt& st, f32x4 u, f32x4 v) { return fma4(acc, spl(st.rs), fma4(u, spl(st.nm), v)); }
DI float fadd(float a, float b) { float r; asm("v_add_f32_e32 %0, %1, %2" : "=v"(r) : "v"(a), "v"(b)); return r; }
DI float hsum4(f32x4 a) { const f32x2 t = f32x2{a[0], a[1]} + f32x2{a[2], a[3]}; float r; asm("v_add_f32_e32 %0, %1, %2" : "=v"(r) : "v"(t[0]), "v"(t[1])); return r; }
struct rsrc_t { __amdgpu_buffer_rsrc_t r; unsigned base; };
DI rsrc_t mkrsrc(const char* ws, size_t off) { return rsrc_t{__builtin_amdgcn_make_buffer_rsrc((void*)ws, 0, 64 << 20, 0x00020000), (unsigned)off}; }
DI rsrc_t outrsrc(const void* out, size_t off) { return rsrc_t{__builtin_amdgcn_make_buffer_rsrc((void*)out, 0, 2048 * D_ * 2, 0x00020000), (unsigned)off}; }
DI unsigned xchoff(int tm, int tn, int w) { return WS_KV1 + (w >> 1) * (2 << 20) + (tn * 16 + tm) * 8192 + (w & 1) * 4096; }
DI u32x4 ld_dev(const rsrc_t& r, unsigned voff, unsigned soff) { return __builtin_amdgcn_raw_buffer_load_b128(r.r, voff, r.base + soff, 16); }
DI void st_dev(u32x4 v, const rsrc_t& r, unsigned voff, unsigned soff) { __builtin_amdgcn_raw_buffer_store_b128(v, r.r, voff + (r.base + soff), 0, 16); }
DI void st_dev8(u32x2 v, const rsrc_t& r, unsigned voff, unsigned soff) { __builtin_amdgcn_raw_buffer_store_b64(v, r.r, voff + (r.base + soff), 0, 16); }
DI unsigned umax4(u32x4 v) { return max(max(v[0], v[1]), max(v[2], v[3])); }
template <int N, class F> DI void poll(u32x4 (&v)[N], F ld) {
  _Pragma("nounroll") for (int spin = 0; spin < SPIN_CAP; spin++) { unsigned m = 0;
    UNR for (int i = 0; i < N; i++) v[i] = ld(i);
    UNR for (int i = 0; i < N; i++) m = max(m, umax4(v[i]));
    if (!__any(m == 0xffffffffu)) break;
    asm volatile("s_sleep 2" ::: "memory");
  }
}
#define EV 0
#define PT 7
#define PG 0
#define FA 1
#define DR 7
#define K0V 0
DI float fmax3(float a, float b, float c) { return __builtin_elementwise_maximum(__builtin_elementwise_maximum(a, b), c); }
struct Blk { int tm, tn, rot, j, xq; };
DI Blk blk1() { const int xcd = blockIdx.x & 7, j = blockIdx.x >> 3; Blk b; b.tm = j >> 1; b.tn = xcd * 2 + (j & 1); b.rot = b.tm; b.j = j; b.xq = xcd; return b; }
DI Blk blk0() { Blk b = blk1(); if (!(P0V & 64)) { const int t = b.tm; b.tm = b.tn; b.tn = t; } return b; }
DI Blk blk() { const int xcd = blockIdx.x & 7, j = blockIdx.x >> 3; Blk b; b.tm = (xcd >> 2) * 8 + (j >> 2); b.tn = (xcd & 3) * 4 + (j & 3); b.rot = (b.tm & 7) * 2 + (b.tm >> 3); b.j = j; b.xq = xcd & 3; return b; }
DI unsigned ptile(int c, int lane) { const int row = 4 * (lane >> 4) + ((lane & 15) >> 2); return row * 128 + ((c ^ ((row >> 1) & 3)) * 32) + (lane & 3) * 8; }
DI unsigned htile(int c, int lane) { const int row = 4 * (lane >> 4) + ((lane & 15) >> 2); return row * 64 + ((c ^ ((row >> 2) & 1)) * 32) + (lane & 3) * 8; }
DI bf16x8 wfrag(unsigned a, unsigned ts) { return cat(trd(a), trd(a + ts)); }

DI unsigned silu2(unsigned cw) { const float c0 = blo(cw), c1 = bhi(cw); return cvtpk(c0 * __builtin_amdgcn_rcpf(1.f + __expf(-c0)), c1 * __builtin_amdgcn_rcpf(1.f + __expf(-c1))); }
DI unsigned fragpos(int k) { return (k >> 5) * 64 + ((k >> 2) & 3) * 16 + ((k & 3) + 4 * ((k >> 4) & 1)) * 2; }
DI f32x4 gemv64(unsigned a, bf16x8 s0, bf16x8 s1, int lane) { f32x4 d[4];
  UNR for (int cc = 0; cc < 4; cc++) d[cc] = mfma(wfrag(a + 4096 + ptile(cc, lane), 2048), s1, mfma(wfrag(a + ptile(cc, lane), 2048), s0, f32x4{0, 0, 0, 0}));
  const bool b0 = lane & 1, b1 = lane & 2; f32x4 o;
  UNR for (int k = 0; k < 4; k++) { const float lo = b0 ? d[1][k] : d[0][k], hi = b0 ? d[3][k] : d[2][k]; o[k] = b1 ? hi : lo; }
  return o;
}

template <int CFG> struct Cfg;
template <> struct Cfg<0> { static constexpr int NT = 6, NP = 3, HALF = 0, NB = 2, NS = 3, N = 3 * D_, KT = 16, B0 = 49152, BS0 = 0; };
template <> struct Cfg<1> { static constexpr int NT = 7, NP = 3, HALF = 1, NB = 7, NS = 3, N = F_, KT = 16, B0 = 65536, BS0 = 2; };
template <> struct Cfg<2> { static constexpr int NT = 2, NP = 1, HALF = 0, NB = 2, NS = 4, N = D_, KT = 16, B0 = 73728, BS0 = 0; };
template <> struct Cfg<3> { static constexpr int NT = 4, NP = 2, HALF = 0, NB = 4, NS = 4, N = D_, KT = 28, B0 = 65536, BS0 = 0; };
template <int CFG> struct Lay { using C = Cfg<CFG>; static constexpr int SBB = C::NP * 8192 + C::HALF * 4096, PB = 2 * C::NP + C::HALF, PER = 4 + PB; };
template <int CFG> DI unsigned btile(int tl, int lane) { using C = Cfg<CFG>; return C::B0 + (tl < 4 * C::NP ? (tl >> 2) * 8192 + ptile(tl & 3, lane) : C::NP * 8192 + htile(tl & 1, lane)); }
constexpr int L_RES = 106496, L_WPHI = 155648, L_UVT = L_WPHI + 4096, L_STT = L_UVT + 2048, L_ET = L_STT + 1024, L_GT = L_ET + 256, L_DUM = L_GT + 512, L_TOT = 163840, L_REST = 122880, L_OT = 131072;
constexpr int L_UN = 0, L_XT = 131072, L_RED = L_WPHI, L_SROW = L_RED + 2048, L_SIL2 = L_GT;
constexpr int L_GB = L_WPHI;
constexpr int L_TR = 0, L_VF = 32768, L_KX = 49152;

template <int CFG> DI void compute_loop(f32x4 (&acc)[4][Cfg<CFG>::NT], unsigned lds0, int wm, int wn, int lane, unsigned& ts) {
  using C = Cfg<CFG>; using L = Lay<CFG>; constexpr int MT = 4, NT = C::NT, NB = C::NB, KT = C::KT;
  const unsigned aoff = lds0 + wm * 8192 + lane * 16; unsigned boff[NB];
  UNR for (int i = 0; i < NB; i++) boff[i] = lds0 + btile<CFG>(CFG == 1 ? (i < 6 ? wn * 6 + i : 12 + wn) : wn * NB + i, lane);
  bf16x8 fa[2][MT]; s16x4 fb[2][NT][2]; unsigned va, vb[NB];
  constexpr int NR = MT + 2 * NT, NM = MT * NT, NU = 1 + NB, FREE = NM - NR, UPG = FREE > 0 ? (NU + FREE - 1) / FREE : NU;
  auto upd = [&](int i, unsigned sa, unsigned sb) { if (i == 0) { va = aoff + sa; pin(va); } else { vb[i - 1] = boff[i - 1] + sb; pin(vb[i - 1]); } };
  auto rd1 = [&](int buf, int ks, int i) {
    if (i == 0 || i > 2 * NT) { const int m = i ? i - 2 * NT : 0; fa[buf][m] = ldsr<bf16x8>(va + m * 2048 + ks * 1024); }
    else { const int n = (i - 1) >> 1, t = (i - 1) & 1;
      fb[buf][n][t] = trd(CFG == 0 ? vb[n & 1] + (n >> 1) * 8192 + ks * 4096 + t * 2048 : (CFG == 1 && n == 6) ? vb[NB - 1] + ks * 2048 + t * 1024 : vb[n] + ks * 4096 + t * 2048); }
  };
  auto step = [&](int buf, int ks, bool doread, bool first, bool doupd, unsigned sa, unsigned sb) {
    UNR for (int i = 0; i < NM; i++) {
      const int m = i / NT, n = i % NT; const bf16x8 b = cat(fb[buf][n][0], fb[buf][n][1]);
      const f32x4 cin = first ? f32x4{0, 0, 0, 0} : acc[m][n];
      acc[m][n] = (CFG == 0 && n >= 4) ? mfma(fa[buf][m], b, cin) : mfma(b, fa[buf][m], cin);
      SB;
      if (doread && i < NR) { rd1(buf ^ 1, ks, i); SB; }
      if (doupd && (FREE > 0 ? i >= NR : i == NM - 1)) { const int u0 = FREE > 0 ? (i - NR) * UPG : 0;
        UNR for (int u = u0; u < u0 + UPG; u++) if (u < NU) upd(u, sa, sb);
        SB; }
    }
  };
  UNR for (int i = 0; i < NU; i++) upd(i, 0, C::BS0 * L::SBB);
  SB; lbar();
  STAMP(CFG == 0 ? 12 : CFG == 2 ? 22 : CFG == 1 ? 28 : 36);
  UNR for (int i = 0; i < NR; i++) rd1(0, 0, i);
  SB;
  unsigned sa = 16384, sb = ((C::BS0 + 1) % C::NS) * L::SBB;
  step(0, 1, true, true, true, sa, sb);
  for (int t = 0; t < KT - 1; t++) {
    waitlgkm0(); bar(); SB;
    sa += 16384; if (sa == C::NS * 16384) sa = 0;
    sb += L::SBB; if (sb == C::NS * L::SBB) sb = 0;
    step(1, 0, true, false, false, 0, 0);
    if (FREE >= 4) { waitlgkm0(); SB; }
    step(0, 1, true, false, true, sa, sb);
  }
  step(1, 0, false, false, false, 0, 0);
}

template <int CFG> struct Loader {
  using C = Cfg<CFG>; using L = Lay<CFG>; static constexpr int N = C::N, KT = C::KT, PER = L::PER, PB = L::PB, NS = C::NS;
  const char *ab, *bb, *hb; unsigned av, bv, hv, la, lb, lh; int ka, kb;
  DI void init(const void* A, int akt, const u16* W, int k0, int col0, int hcol, int rot, int tm, int lw, int lane, unsigned lds0) {
    const int r = lane >> 3, hr = lane >> 2;
    ab = (const char*)A + ((size_t)tm * akt + k0) * 16384 + lw * 4096; av = lane * 16;
    bb = (const char*)W + ((size_t)(k0 * 64 + lw * 16) * N + col0) * 2; bv = r * N * 2 + (((lane & 7) ^ (((r >> 1) & 3) << 1)) * 16);
    hb = (const char*)W + ((size_t)(lw * 16) * N + hcol) * 2; hv = hr * N * 2 + (((lane & 3) ^ (((hr >> 2) & 1) << 1)) * 16);
    if (CFG == 3) { const int t = lw & 1; bb -= (size_t)t * 16 * N * 2; hv = bv + (4 * t + 4 * (r >> 2)) * N * 2; bv += t * 16 * N * 2; }
    la = lds0 + lw * 4096; lb = lds0 + C::B0 + lw * 2048; lh = lds0 + C::B0 + C::NP * 8192 + lw * 1024; ka = kb = rot;
  }
  DI void issueA(int slot) { glds16x4(ab + (size_t)ka * 16384, av, la + slot * 16384); ka++; if (ka == KT) ka = 0; }
  template <bool NT> DI void issueB(int slot) { const char* b = bb + (size_t)kb * 64 * N * 2; slot += C::BS0; if (slot >= NS) slot -= NS; const unsigned so = slot * L::SBB;
    const bool nat = CFG == 3 && ((0x1020408u >> kb) & 1);
    const unsigned v0 = nat ? hv : bv, v1 = v0 + (nat ? 16 : 8) * N * 2;
    UNR for (int i = 0; i < 2 * C::NP; i++) { const char* p = b + (i >> 1) * (CFG == 0 ? 2048 : 128) + (CFG == 3 ? 0 : (i & 1) * (8 * N * 2)); const unsigned l = lb + so + (i >> 1) * 8192 + (i & 1) * 1024, vo = CFG == 3 && (i & 1) ? v1 : v0;
      if (NT) glds16nt(p, vo, l); else glds16(p, vo, l); }
    if (C::HALF) { if (NT) glds16nt(hb + (size_t)kb * 64 * N * 2, hv, lh + so); else glds16(hb + (size_t)kb * 64 * N * 2, hv, lh + so); }
    kb++; if (kb == KT) kb = 0;
  }
  DI void issue(int slot) { issueA(slot); issueB<false>(slot); }
  template <int NPRE, int E1, bool W1, class HP, class HF, class HK, class H1> DI void run(HP hp, HF hf, HK hk, H1 h1) {
    if (NS == 3) {
      issueA(0); if (NPRE < 2) issueB<false>(1); issueA(1);
      waitvm<NPRE < 2 ? PER : 4>(); bar();
      hp(0); issueB<false>(2); issueA(2); int e0 = hf(0); waitx<PER>(e0); lbar();
      UNR for (int t = 1; t <= 6; t++) { hp(t); issue((t - 1) % 3); int e1 = hf(t); if (t == 6) e1 += hk(t); waitx<PER>(e0 + e1); lbar(); e0 = e1; }
      int slot = 0;
      _Pragma("nounroll") for (int t = 7; t <= KT - 3; t++) { issue(slot); slot++; if (slot == NS) slot = 0; const int e1 = hk(t); waitx<PER>(e0 + e1); lbar(); e0 = e1; }
      h1(); waitvm<E1>(); bar();
    } else {
      UNR for (int s = 0; s < NS; s++) issueA(s);
      waitvm<12>(); bar(); waitvm<8>(); bar();
      issue(0); waitvm<4 + PER>(); bar();
      int slot = 1;
      for (int t = 2; t <= KT - 4; t++) { issue(slot); slot = (slot + 1) & 3; waitvm<2 * PER>(); bar(); }
      if (W1) { h1(); waitvm<PER + E1>(); bar(); waitvm<0>(); bar(); } else { waitvm<PER>(); bar(); h1(); waitvm<E1>(); bar(); }
    }
  }
};
DI u64* flagp(char* ws, int tile, int line, int word) { return (u64*)(ws + WS_FLAG) + (tile * 5 + line) * 16 + word; }
DI u64 wait_flags(u64* f, int lane, int n, u64 tag, u64 need) {
  u64 got = 0;
  _Pragma("nounroll") for (int spin = 0; spin < SPIN_CAP; spin++) { u64 v = tag;
    if (FA) { if (lane < n) asm volatile("global_atomic_or_x2 %0, %1, %2, off sc0 sc1\n\ts_waitcnt vmcnt(0)" : "=&v"(v) : "v"(f + lane), "v"(0ull) : "memory"); }
    else if (lane < n) v = __hip_atomic_load(f + lane, __ATOMIC_RELAXED, __HIP_MEMORY_SCOPE_AGENT);
    got = __ballot(v == tag);
    if ((got & need) == need) break;
    asm volatile("s_sleep 1" ::: "memory");
  }
  return got;
}
template <class H> DI bool panel_sync(char* ws, int line, const Blk& P, const Blk& C, int lw, int lane, u64 tag, H pre) {
  bar();
  if (lw == 0) { const int i = lane & 15, grp = lane >> 4;
    if (line == 0) { if (grp < 2) __hip_atomic_store(grp == 0 ? flagp(ws, P.tm * 16 + i, 0, P.tn) : flagp(ws, i * 16 + C.tn, 1, C.tm), tag, __ATOMIC_RELAXED, __HIP_MEMORY_SCOPE_AGENT); }
    else if (grp == 0) __hip_atomic_store(flagp(ws, P.tm * 16 + i, line, P.tn), tag, __ATOMIC_RELAXED, __HIP_MEMORY_SCOPE_AGENT); }
  pre();
  bool all = true;
  if (lw == 0) all = wait_flags(flagp(ws, C.tm * 16 + C.tn, line, 0), lane, line == 0 ? 32 : 16, tag, 0xffff) == ~0ull;
  bar();
  return all;
}
DI int modcol(int lane) { return 4 * (lane & 15) + 2 * (lane >> 5) + ((lane >> 4) & 1); }
DI float modvec(const char* ws, unsigned part, int lane, u32x4 (&p)[4], bool have) {
  if (!have || __any(max(max(umax4(p[0]), umax4(p[1])), max(umax4(p[2]), umax4(p[3]))) == 0xffffffffu)) { const rsrc_t r = mkrsrc(ws, part); poll(p, [&](int i) { return ld_dev(r, lane * 16, i * 1024); }); }
  const f32x4 a = (asf(p[0]) + asf(p[1])) + (asf(p[2]) + asf(p[3]));
  return rbf(sw16_add(sw32_add(a[0], a[2]), sw32_add(a[1], a[3])));
}
DI float modvec(const char* ws, unsigned part, int lane) { u32x4 p[4]; return modvec(ws, part, lane, p, false); }
DI void put_et(unsigned lds0, int vec, int k, float v) { ldsw<u16>(lds0 + L_ET + vec * 128 + fragpos(k), (u16)(__float_as_uint(v) >> 16)); }

template <int CFG, int NTL> struct UV {
  bf16x8 f[NTL][2]; unsigned ad[NTL], ts[NTL];
  DI void prep(int lw, int lane, unsigned base) { UNR for (int i = 0; i < NTL; i++) { const int tl = min(lw * NTL + i, 2 * Cfg<CFG>::NT - 1); ad[i] = base + btile<CFG>(tl, lane); ts[i] = tl < 4 * Cfg<CFG>::NP ? 2048 : 1024; pin(ad[i]); } }
  DI void read() { UNR for (int i = 0; i < NTL; i++) { f[i][0] = wfrag(ad[i], ts[i]); f[i][1] = wfrag(ad[i] + 2 * ts[i], ts[i]); } }
  bf16x8 e0, e1;
  DI void reade(int lane, unsigned lds0) { e0 = ldsr<bf16x8>(lds0 + L_ET + (lane & 1) * 128 + (lane >> 4) * 16); e1 = ldsr<bf16x8>(lds0 + L_ET + (lane & 1) * 128 + 64 + (lane >> 4) * 16); }
  DI void publish(const rsrc_t& r, int tm, int lw, int lane) {
    f32x4 d[NTL], o; const int i2 = (lane & 15) >> 1;
    UNR for (int i = 0; i < NTL; i++) d[i] = mfma(f[i][1], e1, mfma(f[i][0], e0, f32x4{0, 0, 0, 0}));
    UNR for (int k = 0; k < 4; k++) { o[k] = d[0][k]; UNR for (int i = 1; i < NTL; i++) o[k] = i2 == i ? d[i][k] : o[k]; }
    if (i2 < NTL) st_dev(__builtin_bit_cast(u32x4, o), r, i2 * 2048 + (lane & 1) * 64 + (lane >> 4) * 16, lw * NTL * 2048 + tm * 128);
  }
  u32x4 v[2 * NTL];
  DI void fetch(const char* base, int lw, int lane) { UNR for (int i = 0; i < 2 * NTL; i++) gld_dev(v[i], base + lw * NTL * 2048 + i * 1024, lane * 16); }
  DI void finish(const rsrc_t& r, int lw, int lane, unsigned tab) {
    pin(v); unsigned mx = 0;
    UNR for (int i = 0; i < 2 * NTL; i++) mx = max(mx, umax4(v[i]));
    if (__any(mx == 0xffffffffu)) poll(v, [&](int i) { return ld_dev(r, lane * 16, lw * NTL * 2048 + i * 1024); });
    UNR for (int i = 0; i < NTL; i++) { const f32x4 a = asf(v[2 * i]) + asf(v[2 * i + 1]);
      float t = sw16_add(sw32_add(a[0], a[2]), sw32_add(a[1], a[3])); t += dpp<0x128>(t);
      ldsw<float>(tab + (lw * NTL + i) * 128 + (lane & 7) * 16 + (2 * (lane >> 5) + ((lane >> 4) & 1)) * 4, t); }
  }
};
template <int NPART> struct Stats {
  u32x4 v[NPART / 4];
  DI void fetch(int i, const char* st, int tm, int lw, int lane) { gld_dev(v[i], st + (size_t)(tm * NPART + i) * 1024 + lw * 256, (lane >> 4) * (NPART * 256) + (lane & 15) * 16); }
  DI void finish(int lw, int lane, unsigned tab) { pin(v); f32x4 s = asf(v[0]); float q0 = 0, q1 = 0;
    UNR for (int i = 1; i < NPART / 4; i++) s += asf(v[i]);
    float t = sw32_add(s[0], s[2]); t = sw16_add(t, t) * (1.f / NPART);
    const auto mm = __builtin_amdgcn_permlane32_swap(__float_as_uint(t), __float_as_uint(t), false, false);
    UNR for (int i = 0; i < NPART / 4; i++) { const float d0 = __uint_as_float(v[i][0]) - __uint_as_float(mm[0]), d1 = __uint_as_float(v[i][2]) - __uint_as_float(mm[1]); q0 = __builtin_fmaf(d0, d0, q0); q1 = __builtin_fmaf(d1, d1, q1); }
    float q = sw32_add(__builtin_fmaf((float)(D_ / NPART), q0, s[1]), __builtin_fmaf((float)(D_ / NPART), q1, s[3])); q = sw16_add(q, q);
    const float rstd = __builtin_amdgcn_rsqf(q * (1.f / D_) + 1e-5f);
    ldsw<float>(tab + ((lane >> 4) & 1) * 512 + (lw * 32 + (lane & 15) * 2 + (lane >> 5)) * 4, (lane & 16) ? -t * rstd : rstd); }
};

template <bool MAX> DI float fold4(float a, float b, float c, float d) {
  auto op = [](unsigned x, unsigned y) { return MAX ? __builtin_elementwise_maximum(__uint_as_float(x), __uint_as_float(y)) : __uint_as_float(x) + __uint_as_float(y); };
  const auto r0 = __builtin_amdgcn_permlane32_swap(__float_as_uint(a), __float_as_uint(b), false, false), r1 = __builtin_amdgcn_permlane32_swap(__float_as_uint(c), __float_as_uint(d), false, false);
  const auto r2 = __builtin_amdgcn_permlane16_swap(__float_as_uint(op(r0[0], r0[1])), __float_as_uint(op(r1[0], r1[1])), false, false);
  return op(r2[0], r2[1]);
}
DI void spread4(float t, float& a, float& b, float& c, float& d) {
  const auto r2 = __builtin_amdgcn_permlane16_swap(__float_as_uint(t), __float_as_uint(t), false, false);
  const auto r0 = __builtin_amdgcn_permlane32_swap(r2[0], r2[0], false, false), r1 = __builtin_amdgcn_permlane32_swap(r2[1], r2[1], false, false);
  a = __uint_as_float(r0[0]); b = __uint_as_float(r0[1]); c = __uint_as_float(r1[0]); d = __uint_as_float(r1[1]);
}
DI void feat2(const bf16x8 (&wf)[2], bf16x8 x0, bf16x8 x1, bf16x8& fp0, bf16x8& fn0, bf16x8& fp1, bf16x8& fn1) {
  const f32x4 zero = {0, 0, 0, 0}; f32x4 p[2][2] = {{mfma(wf[0], x0, zero), mfma(wf[1], x0, zero)}, {mfma(wf[0], x1, zero), mfma(wf[1], x1, zero)}};
  float mx[2], nx[2], sp[2], sn[2]; f32x4 a[2][2], b[2][2];
  UNR for (int i = 0; i < 2; i++) { const f32x4 u = p[i][0], v = p[i][1];
    mx[i] = fmax3(fmax3(fmax3(u[0], u[1], u[2]), u[3], v[0]), fmax3(v[1], v[2], v[3]), v[3]); nx[i] = fmax3(fmax3(fmax3(-u[0], -u[1], -u[2]), -u[3], -v[0]), fmax3(-v[1], -v[2], -v[3]), -v[3]); }
  spread4(fold4<true>(mx[0], mx[1], nx[0], nx[1]), mx[0], mx[1], nx[0], nx[1]);
  UNR for (int i = 0; i < 2; i++) UNR for (int h = 0; h < 2; h++) UNR for (int j = 0; j < 4; j += 2) { const f32x2 t = {p[i][h][j], p[i][h][j + 1]};
    f32x2 d = t - f32x2{mx[i], mx[i]}, e = -t - f32x2{nx[i], nx[i]}; asm("" : "+v"(d), "+v"(e));
    a[i][h][j] = __builtin_amdgcn_exp2f(d[0]); a[i][h][j + 1] = __builtin_amdgcn_exp2f(d[1]); b[i][h][j] = __builtin_amdgcn_exp2f(e[0]); b[i][h][j + 1] = __builtin_amdgcn_exp2f(e[1]); }
  UNR for (int i = 0; i < 2; i++) { sp[i] = hsum4(a[i][0] + a[i][1]); sn[i] = hsum4(b[i][0] + b[i][1]); }
  spread4(__builtin_amdgcn_rcpf(fold4<false>(sp[0], sp[1], sn[0], sn[1])), sp[0], sp[1], sn[0], sn[1]);
  fp0 = pack8(a[0][0] * sp[0], a[0][1] * sp[0]); fn0 = pack8(b[0][0] * sn[0], b[0][1] * sn[0]); fp1 = pack8(a[1][0] * sp[1], a[1][1] * sp[1]); fn1 = pack8(b[1][0] * sn[1], b[1][1] * sn[1]);
}

DI void touch_code(int lane, unsigned ldsdummy) {
  const char* pc; unsigned len, back;
  asm volatile("s_getpc_b64 %0\n.Lhere%=:\n\ts_mov_b32 %2, .Lhere%=-.Lcode_begin\n\ts_mov_b32 %1, .Lcode_end-.Lcode_begin" : "=s"(pc), "=s"(len), "=s"(back));
  pc -= back;
  const unsigned o = ((blockIdx.x >> 3) * 16 + (lane & 15)) * 128;
  touch(pc, min(o, len), ldsdummy);
}
struct Args { const u16 *x, *wqkv, *wphi, *wo, *w1, *b1, *w2, *b2, *c, *wm; u16* out; char* ws; };

DI int gxcol(int lane) { return 2 * (lane & 31) + (lane >> 5); }
template <int N> DI void gx_gather(const char* ws, unsigned off, int lane, unsigned tag, float (&t)[N]) {
  const rsrc_t r = mkrsrc(ws, WS_GX + off); u32x4 v[N];
  _Pragma("nounroll") for (int spin = 0; spin < SPIN_CAP; spin++) { unsigned bad = 0;
    UNR for (int i = 0; i < N; i++) v[i] = ld_dev(r, lane * 16, i * 1024);
    UNR for (int i = 0; i < N; i++) bad |= (v[i][1] ^ tag) | (v[i][3] ^ tag);
    if (!__any(bad != 0)) break;
    asm volatile("s_sleep 2" ::: "memory");
  }
  UNR for (int i = 0; i < N; i++) t[i] = sw32_add(__uint_as_float(v[i][0]), __uint_as_float(v[i][2]));
}
DI float sum8(const float (&p)[8]) { return fadd(fadd(fadd(p[0], p[1]), fadd(p[2], p[3])), fadd(fadd(p[4], p[5]), fadd(p[6], p[7]))); }

DI u64 phase0(const Args& A, int w, unsigned lds0, unsigned& ts) {
  char* ws = A.ws; const Blk B = blk0(), B1 = blk1(); const f32x4 zero = {0, 0, 0, 0};
  const int lane = threadIdx.x & 63, r8 = lane >> 3, g = lane >> 4, r = lane & 15;
  u64 ep; f32x2 epv; unsigned cw = 0;
  if (P0V & 4) asm volatile("s_load_dwordx2 %0, %1, 0x0" : "=s"(ep) : "s"(ws + WS_EPOCH) : "memory"); else gld8(epv, ws + WS_EPOCH, 0);
  if (!(P0V & 8) || w == 7) asm volatile("global_load_dword %0, %1, %2 nt" : "+v"(cw) : "v"(B.tm * 128 + (lane & 31) * 4), "s"(A.c) : "memory");
  { const char* wb = (const char*)A.wm + (size_t)(B.tm * 8 + w) * 16 * MODN + B.tn * 128; const unsigned wv = r8 * MODN * 2 + (((lane & 7) ^ (((r8 >> 1) & 3) << 1)) * 16);
    glds16nt(wb + 2 * D_, wv, lds0 + L_UN + w * 1024); glds16nt(wb, wv, lds0 + L_UN + 8192 + w * 1024); }
  if (P0V & 16) waitvm<0>(); else bar();
  const unsigned rv0 = r8 * 2048 + (((lane & 7) ^ (r8 >> 1)) * 16);
  UNR for (int i = 0; i < 2; i++) glds16nt((const char*)A.x + ((size_t)(B.tm * 128 + w * 16 + i * 8) * D_ + B.tn * 64) * 2, i ? rv0 ^ 64 : rv0, lds0 + L_XT + (w * 2 + i) * 1024);
  if (P0V & 1) { const int e = min(w * 24 + lane, 191), row = e / 6, l = e - row * 6;
    if (lane < 24) { if (P0V & 1024) touch((const char*)A.wqkv + (size_t)B.j * 32 * 6144 + B.xq * 256, row * 6144 + (l >> 1) * 2048 + (l & 1) * 128, lds0 + L_DUM);
      else touch_nt((const char*)A.wqkv + (size_t)B.j * 32 * 6144 + B.xq * 256, row * 6144 + (l >> 1) * 2048 + (l & 1) * 128, lds0 + L_DUM); } }
  if ((P0V & 32) && w == 6 && lane < 8) touch_any(lane < 3 ? (const char*)A.out + min(lane << 21, 2048 * D_ * 2 - 4) : (const char*)A.wqkv + min((lane - 3) << 21, 3 * D_ * D_ * 2 - 4), lds0 + L_DUM);
  unsigned tq; { const int e = min(w * 24 + lane, 191), row = e / 6, l = e - row * 6; tq = row * 6144 + (l >> 1) * 2048 + (l & 1) * 128; pin(tq); }
  unsigned pa = lds0 + L_UN + (w >> 2) * 8192 + ptile(w & 3, lane); pin(pa);
  waitvm<4 + (P0V & 1)>(); pin(cw);
  if (P0V & 4) asm volatile("s_waitcnt lgkmcnt(0)" : "+s"(ep) :: "memory"); else { pin(epv); ep = __builtin_bit_cast(u64, epv); }
  const u64 tag = ep + 0x9E3779B97F4A7C15ull;
  if (w == 7) ldsw<unsigned>(lds0 + L_SIL2 + fragpos(2 * (lane & 31)), silu2(cw));
  waitvm<2 + (P0V & 1)>(); lbar();
  STAMP(1);
  { const f32x4 d = mfma(wfrag(pa + 4096, 2048), ldsr<bf16x8>(lds0 + L_SIL2 + 64 + g * 16), mfma(wfrag(pa, 2048), ldsr<bf16x8>(lds0 + L_SIL2 + g * 16), zero));
    const bool o = r & 1; const u32x4 gr = {__float_as_uint(o ? d[2] : d[0]), (unsigned)tag, __float_as_uint(o ? d[3] : d[1]), (unsigned)tag};
    if (r < 2) st_dev(gr, mkrsrc(ws, WS_GX + (((w >> 2) * 16 + B.tn) * 16 + B.tm) * 512), g * 32 + r * 16, (w & 3) * 128); }
  asm volatile("" ::: "memory");
  if (w < 3) {
    const int l = w * 8 + r8;
    if (l < 20) st_dev(u32x4{~0u, ~0u, ~0u, ~0u}, mkrsrc(ws, WS_MODP), (l < 8 ? (((l >> 1) * 16 + B.tn) * 16 + B.tm) * 256 + (l & 1) * 128 : 4 * 65536 + (B1.tn * 12 + l - 8) * 2048 + B1.tm * 128) + (lane & 7) * 16, 0); }
  if (w == 7 && (PT & 1)) { const Blk C = blk(); if (lane < 5) touch_any(flagp(ws, lane < 2 ? B1.tm * 16 + B1.tn : C.tm * 16 + C.tn, lane, 0), lds0 + L_DUM); }
  if (w < 3 || w == 7) waitvm<((PT & 1) ? 2 : 1) + (P0V & 1)>(); else waitvm<1 + (P0V & 1)>();
  f32x4 xv[4]; f32x2 stat;
  UNR for (int cc = 0; cc < 4; cc++) { const u32x2 t = ldsr<u32x2>(lds0 + L_XT + (w * 16 + r) * 128 + (((cc * 2 + (g >> 1)) ^ (r >> 1)) * 16) + (g & 1) * 8); xv[cc] = unp(t[0], t[1]); }
  { const float mean = xg_sum(hsum4((xv[0] + xv[1]) + (xv[2] + xv[3]))) * (1.f / 64); f32x4 q = zero;
    UNR for (int cc = 0; cc < 4; cc++) { const f32x4 d = xv[cc] - mean; q = fma4(d, d, q); }
    stat = f32x2{mean, xg_sum(hsum4(q))}; pin(stat); pin(xv); }
  waitvm<0>();
  STAMP(2);
  { float t[1]; gx_gather<1>(ws, B.tn * 8192 + w * 1024, lane, (unsigned)tag, t); ldsw<float>(lds0 + L_RED + w * 256 + gxcol(lane) * 4, t[0]); }
  STAMP(3);
  lbar();
  auto wtouch = [&]() { if (lane < 24) touch_nt((const char*)A.wqkv + (size_t)B.j * 32 * 6144 + B.xq * 256, tq, lds0 + L_DUM); };
  if (P0V & 128) wtouch();
  float p[8]; UNR for (int i = 0; i < 8; i++) p[i] = ldsr<float>(lds0 + L_RED + i * 256 + lane * 4);
  ldsw<float>(lds0 + L_SROW + w * 256 + lane * 4, rbf(1.f + rbf(sum8(p))));
  asm volatile("" ::: "memory");
  const rsrc_t ry = outrsrc(A.out, (B.tm * 16 + B.tn) * 16384);
  UNR for (int ks = 0; ks < 2; ks++)
    st_dev(pack8u(xv[2 * ks] * ldsr<f32x4>(lds0 + L_SROW + w * 256 + ks * 128 + g * 16), xv[2 * ks + 1] * ldsr<f32x4>(lds0 + L_SROW + w * 256 + ks * 128 + 64 + g * 16)), ry, lane * 16, w * 2048 + ks * 1024);
  if (g == 0) st_dev8(__builtin_bit_cast(u32x2, stat), mkrsrc(ws, WS_ST1 + (size_t)(B.tm * 16 + B.tn) * 1024), r * 8, w * 128);
  asm volatile("" ::: "memory");
  if (P0V & 256) wtouch();
  STAMP(4);
  return tag;
}

DI void loader_role(const Args& A, int lw, unsigned lds0, u64 tag, unsigned& ts) {
  char* ws = A.ws; const Blk B2 = blk(), B1 = blk1(), B0 = blk0(); const f32x4 zero = {0, 0, 0, 0}; const u32x4 mark = {~0u, ~0u, ~0u, ~0u};
  int lane = threadIdx.x & 63; pin(lane);
  Loader<2> G2; auto none = [](int) { return 0; };
  {
    const Blk& B = B1; const int g = lane >> 4, r = lane & 15, q = r >> 2, p = lane & 3, r8 = lane >> 3;
    Loader<0> G; G.init(A.out, 16, A.wqkv, 0, B.tn * 64, 0, B.rot, B.tm, lw, lane, lds0);
    if (P0V & 1) { G.issueB<false>(0); G.issueB<false>(1); } else if (!(P0V & 2)) G.issueB<true>(0);
    const rsrc_t ruv = mkrsrc(ws, WS_UV1 + B.tn * 12 * 2048);
    UV<0, 3> U; Stats<16> S; U.prep(lw, lane, lds0);
    waitvm<(P0V & 1) ? 12 : (P0V & 256) ? 1 : (P0V & 2) ? 0 : 6>();
    STAMP(5);
    const bool all0 = panel_sync(ws, 0, B0, B, lw, lane, tag, [&]() { if (P0V & 384) { G.issueB<false>(0); if (P0V & 512) G.issueB<false>(1); } else if (P0V & 2) G.issueB<true>(0); });
    STAMP(11);
    const int t3 = 6 + (B.tm >> 1);
    G.run<(P0V & 513) ? 2 : 1, 7, false>(
      [&](int t) { if (t == 0) { U.read(); U.reade(lane, lds0); } },
      [&](int t) { int e = 0;
        if (t == 0) { UNR for (int c = 0; c < 2; c++) st_dev(mark, mkrsrc(ws, WS_KV1 + (size_t)(B.tn * 4 + lw) * 32768), lane * 16, B.tm * 2048 + c * 1024); e = 2; }
        if (t == 1) { UNR for (int c = 0; c < 2; c++) st_dev(mark, mkrsrc(ws, WS_KV2 + (size_t)(B.tn * 16 + B.tm) * 8192), lane * 16, lw * 2048 + c * 1024); e = 2; }
        if (t == 2) { if (r8 < 5) st_dev(mark, mkrsrc(ws, WS_ZP), (r8 == 0 ? (B.tn * 4 + lw) * 2048 : (1 << 17) + (B.tn * 16 + lw * 4 + r8 - 1) * 2048) + (lane & 7) * 16, B.tm * 128); e = 1; }
        if (t == 3 || t == 4) { UNR for (int i = 0; i < 2; i++) S.fetch(2 * t - 6 + i, ws + WS_ST1, B.tm, lw, lane); e = 2; }
        if (t == 5) { prio(1); U.publish(ruv, B.tm, lw, lane); prio(0); e = 1; }
        if (t == 6 && !all0) wait_flags(flagp(ws, B.tm * 16 + B.tn, 1, 0), lane, 16, tag, ~0ull);
        return e; },
      [&](int t) { int e = 0;
        if (t == t3 && !(EV & 2)) { touch(opq((const char*)A.w1) + (size_t)(blockIdx.x * 4 + lw) * 7168, min(lane, 55) * 128, lds0 + L_DUM); touch(opq((const char*)A.wo) + (size_t)(blockIdx.x * 4 + lw) * 2048, (lane & 15) * 128, lds0 + L_DUM); e += 2; }
        if ((PT & 4) && t == 11 && lw == 0) { touch_code(lane, lds0 + L_DUM); e += 1; }
        return e; },
      [&]() { U.fetch(opq(ws) + ruv.base, lw, lane); glds16nt((const char*)A.wphi + B.tn * 4096 + lw * 1024, lane * 16, lds0 + L_WPHI + lw * 1024); prio(1); waitvm<17>(); S.finish(lw, lane, lds0 + L_STT); prio(0); });
    {
      const char* wb = (const char*)A.wm + (size_t)B0.tm * 128 * MODN + (lw + 2) * 2 * D_ + B0.tn * 128; const unsigned wv = r8 * MODN * 2 + (((lane & 7) ^ (((r8 >> 1) & 3) << 1)) * 16);
      UNR for (int i = 0; i < 8; i++) glds16nt(wb + i * 16 * MODN, wv, lds0 + L_REST + lw * 8192 + i * 1024); }
    prio(1);
    waitvm<8>(); U.finish(ruv, lw, lane, lds0 + L_UVT);
    G2.init(ws + WS_O, 16, A.wo, 0, B2.tn * 64, 0, B2.rot, B2.tm, lw, lane, lds0);
    prio(0);
    lbar();
    bf16x8 wf[2];
    UNR for (int c = 0; c < 2; c++) wf[c] = wfrag(lds0 + L_WPHI + (lw & 1) * 2048 + (4 * g + q) * 64 + c * 32 + p * 8, 1024);
    { const unsigned rv0 = r8 * 2048 + (((lane & 7) ^ (r8 >> 1)) * 16);
      UNR for (int i = 0; i < 4; i++) glds16nt((const char*)A.x + ((size_t)(B2.tm * 128 + (lw * 4 + i) * 8) * D_ + B2.tn * 64) * 2, (i & 1) ? rv0 ^ 64 : rv0, lds0 + L_RES + (lw * 4 + i) * 1024); }
    bar();
    { const unsigned trw = lds0 + L_TR + lw * 8192 + r * 64 + ((g ^ ((r >> 1) & 3)) * 16); bf16x8 a, b, c, d;
      feat2(wf, ldsr<bf16x8>(lds0 + L_KX + lw * 2048 + lane * 16), ldsr<bf16x8>(lds0 + L_KX + lw * 2048 + 1024 + lane * 16), a, b, c, d);
      ldsw<bf16x8>(trw + 2048, a); ldsw<bf16x8>(trw + 2048 + 4096, b); ldsw<bf16x8>(trw + 3072, c); ldsw<bf16x8>(trw + 3072 + 4096, d); }
    G2.issueB<false>(0); G2.issueB<false>(1);
    lbar();
    prio(1);
    const int hd = lw >> 1, sg = lw & 1;
    const u32x4 one4 = {0x3f803f80u, 0x3f803f80u, 0x3f803f80u, 0x3f803f80u}; const bf16x8 ones = __builtin_bit_cast(bf16x8, one4);
    const unsigned tra = lds0 + L_TR + hd * 8192 + sg * 4096 + (4 * g + q) * 64 + ((p ^ (((4 * g + q) >> 1) & 3)) * 16), vfa = lds0 + L_VF + hd * 4096 + lane * 16;
    f32x4 kv[2][2] = {{zero, zero}, {zero, zero}}, z[2] = {zero, zero};
    UNR for (int k = 0; k < 4; k++) { const bf16x8 v0 = ldsr<bf16x8>(vfa + (k >> 1) * 8192 + (k & 1) * 2048), v1 = ldsr<bf16x8>(vfa + (k >> 1) * 8192 + (k & 1) * 2048 + 1024);
      UNR for (int ft = 0; ft < 2; ft++) { const bf16x8 af = wfrag(tra + (k >> 1) * 16384 + (k & 1) * 2048 + ft * 8, 1024);
        kv[ft][0] = mfma(af, v0, kv[ft][0]); kv[ft][1] = mfma(af, v1, kv[ft][1]); z[ft] = mfma(af, ones, z[ft]); } }
    const rsrc_t rk = mkrsrc(ws, WS_KV1 + (size_t)(B.tn * 4 + lw) * 32768), rz = mkrsrc(ws, WS_ZP + (size_t)(B.tn * 4 + lw) * 2048), r2 = mkrsrc(ws, WS_KV2 + (size_t)B.tn * 131072);
    UNR for (int c = 0; c < 2; c++) st_dev(pack8u(kv[0][c], kv[1][c]), rk, lane * 16, B.tm * 2048 + c * 1024);
    { const bool o = lane & 1; const f32x4 zs = {o ? z[1][0] : z[0][0], o ? z[1][1] : z[0][1], o ? z[1][2] : z[0][2], o ? z[1][3] : z[0][3]};
      if ((lane & 14) == 0) st_dev(__builtin_bit_cast(u32x4, zs), rz, (lane & 1) * 64 + g * 16, B.tm * 128); }
    STAMP(16);
    if (DR & 1) waitvm<0>();
    u32x4 h[2], zz[2]; poll(h, [&](int i) { return ld_dev(rk, (lane >> 3) * 2048 + (lane & 7) * 16, B.tm * 128 + i * 16384); });
    UNR for (int i = 0; i < 2; i++) gld_dev(zz[i], ws + rz.base + i * 64, r * 128 + g * 16);
    pin(h);
    { float e[4], o[4];
      UNR for (int k = 0; k < 4; k++) { e[k] = fadd(blo(h[0][k]), blo(h[1][k])); o[k] = fadd(bhi(h[0][k]), bhi(h[1][k])); }
      float te = fold4<false>(e[0], e[1], e[2], e[3]), to = fold4<false>(o[0], o[1], o[2], o[3]); te += dpp<0x128>(te); to += dpp<0x128>(to);
      spread4(__uint_as_float(cvtpk(te, to)), e[0], e[1], e[2], e[3]);
      const u32x4 sum = {__float_as_uint(e[0]), __float_as_uint(e[1]), __float_as_uint(e[2]), __float_as_uint(e[3])};
      UNR for (int i = 0; i < 2; i++) st_dev(sum, r2, (lane >> 3) * 8192 + (lane & 7) * 16, i * 65536 + lw * 2048 + B.tm * 128); }
    STAMP(17);
    waitvm<2>(); pin(zz);
    if (__any(max(umax4(zz[0]), umax4(zz[1])) == 0xffffffffu)) poll(zz, [&](int i) { return ld_dev(rz, r * 128 + g * 16, i * 64); });
    ldsw<bf16x8>(lds0 + L_KX + lw * 1024 + lane * 16, pack8(row16_sum4(asf(zz[0])), row16_sum4(asf(zz[1]))));
    if (DR & 4) waitvm<0>();
    lbar();
    prio(0);
    { const f32x4 d = gemv64(lds0 + L_REST + lw * 8192, ldsr<bf16x8>(lds0 + L_SIL2 + g * 16), ldsr<bf16x8>(lds0 + L_SIL2 + 64 + g * 16), lane);
      if (r < 4) st_dev(__builtin_bit_cast(u32x4, d), mkrsrc(ws, WS_MODP + (size_t)((lw * 16 + B0.tn) * 16 + B0.tm) * 256), r * 64 + g * 16, 0); }
  }
  const Blk& B = B2; const int odd = B.tn & 1, q4 = (B.tm >> 3) + 2 * ((B.tm >> 2) & 1);
  Loader<1> G3;
  {
    G2.issueB<false>(2); G2.issueB<false>(3);
    panel_sync(ws, 2, B1, B, lw, lane, tag, [&]() {});
    if (blockIdx.x == 0 && lw == 0 && lane == 0) __hip_atomic_store((u64*)(ws + WS_EPOCH), tag, __ATOMIC_RELAXED, __HIP_MEMORY_SCOPE_AGENT);
    STAMP(21);
    G3.init(A.out, 16, A.w1, 0, B.tn * 224 + odd * 32, B.tn * 224 + (odd ? 0 : 192), q4 * 4, B.tm, lw, lane, lds0);
    G2.run<4, 7, false>(none, none, none, [&]() { G3.issueB<false>(0); });
  }
  Loader<3> G4;
  {
    const rsrc_t ruv = mkrsrc(ws, WS_UV2 + B.tn * 16 * 2048);
    UV<1, 4> U; Stats<32> S; const int tu = B.tm & 3, t3 = 6 + (B.j & 7);
    U.prep(lw, lane, lds0 + (tu == 1 ? 0 : tu == 2 ? 1 : 2) * Lay<1>::SBB);
    bar();
    G3.issueB<false>(1);
    panel_sync(ws, 3, B, B, lw, lane, tag, [&]() {});
    STAMP(27);
    G3.run<2, 8, false>(
      [&](int t) { if (t == 0 || (t < 4 && t == tu)) U.read(); if (t == 4) U.reade(lane, lds0); },
      [&](int t) { int e = 0;
        if (t == 4) { prio(1); U.publish(ruv, B.tm, lw, lane); prio(0); e = 1; }
        else if (t >= 1 && t <= 5) { const int i = t == 5 ? 6 : 2 * t - 2; S.fetch(i, ws + WS_ST2, B.tm, lw, lane); S.fetch(i + 1, ws + WS_ST2, B.tm, lw, lane); e = 2; }
        return e; },
      [&](int t) { int e = 0;
        if (t < 10) { st_dev(mark, mkrsrc(ws, xchoff(B.tm, B.tn, lw)), lane * 16, (t - 6) * 1024); e = 1; }
        if (t == t3 && !(EV & 4)) { touch(opq((const char*)A.w2) + (size_t)(blockIdx.x * 4 + lw) * 7168, min(lane, 55) * 128, lds0 + L_DUM); e += 1; }
        if ((PT & 4) && t == 11 && lw == 0) { touch_code(lane, lds0 + L_DUM); e += 1; }
        return e; },
      [&]() { U.fetch(opq(ws) + ruv.base, lw, lane); prio(1); waitvm<19>(); S.finish(lw, lane, lds0 + L_STT); prio(0); });
    G4.init(ws + WS_H1, 56, A.w2, (B.tn >> 3) * 28, (B.tn & 7) * 128, 0, (B.rot * 28) >> 4, B.tm, lw, lane, lds0);
    G4.issueB<false>(0); G4.issueB<false>(1);
    prio(1);
    waitvm<8>(); U.finish(ruv, lw, lane, lds0 + L_UVT);
    prio(0);
    lbar();
  }
  {
    bar();
    G4.issueB<false>(2); G4.issueB<false>(3);
    panel_sync(ws, 4, B, B, lw, lane, tag, [&]() {});
    STAMP(35);
    G4.run<4, 4, true>(none, none, none, [&]() {
      glds16x4(opq(ws) + WS_X1 + (size_t)(B.tm * 16 + (B.tn & 7) * 2 + (lw & 1)) * 16384 + ((lw >> 1) * 4 + (B.tn >> 3) * 2) * 2048, lane * 16, lds0 + L_OT + lw * 4096); });
    asm volatile(".Lcode_end:");
  }
}

DI void compute_role(const Args& A, int w, unsigned lds0, unsigned tag, unsigned& ts) {
  char* ws = A.ws; const Blk B = blk(); const f32x4 zero = {0, 0, 0, 0}; const u32x4 mark = {~0u, ~0u, ~0u, ~0u};
  const int wm = w >> 1, wn = w & 1; int lane = threadIdx.x & 63; pin(lane);
  {
    const Blk B = blk1(); const int g = lane >> 4, r = lane & 15, q = r >> 2, p = lane & 3;
    waitvm<(P0V & 256) ? 1 : 0>();
    STAMP(5);
    bar();
    if (w < 2) { float t[8]; gx_gather<8>(ws, (w * 16 + B.rot) * 8192, lane, tag, t); const float v = rbf(sum8(t)); put_et(lds0, w, gxcol(lane), w ? v : rbf(1.f + v)); }
    bar();
    f32x4 acc[4][6];
    compute_loop<0>(acc, lds0, wm, wn, lane, ts);
    STAMP(13);
    bar();
    STAMP(14);
    constexpr float L2E = 1.4426950408889634f;
    bf16x8 wf[2]; RowSt st[4];
    UNR for (int m = 0; m < 4; m++) st[m].set(lds0 + L_STT + (wm * 64 + m * 16 + r) * 4, L2E);
    const unsigned uva = lds0 + L_UVT + wn * 256;
    bf16x8 kf[4];
    { const f32x4 u0 = ldsr<f32x4>(uva + 512 + g * 16), v0 = ldsr<f32x4>(uva + 576 + g * 16) * L2E, u1 = ldsr<f32x4>(uva + 640 + g * 16), v1 = ldsr<f32x4>(uva + 704 + g * 16) * L2E;
      UNR for (int c = 0; c < 2; c++) wf[c] = wfrag(lds0 + L_WPHI + wn * 2048 + (4 * g + q) * 64 + c * 32 + p * 8, 1024);
      UNR for (int mm = 0; mm < 4; mm++) { const int m = mm ^ 2; kf[m] = pack8(lnf(acc[m][2], st[m], u0, v0), lnf(acc[m][3], st[m], u1, v1));
        if (m >= 2) ldsw<bf16x8>(lds0 + L_KX + w * 2048 + (m - 2) * 1024 + lane * 16, kf[m]);
        if (m == 3) { lbar(); asm volatile("" : "+v"(acc[0][2]), "+v"(acc[0][3]), "+v"(acc[1][2]), "+v"(acc[1][3])); } } }
    { const unsigned trw = lds0 + L_TR + w * 8192 + r * 64 + ((g ^ ((r >> 1) & 3)) * 16); bf16x8 a, b, c, d; feat2(wf, kf[0], kf[1], a, b, c, d);
      ldsw<bf16x8>(trw, a); ldsw<bf16x8>(trw + 4096, b); ldsw<bf16x8>(trw + 1024, c); ldsw<bf16x8>(trw + 1024 + 4096, d); }
    { float u[2], v[2];
      UNR for (int c = 0; c < 2; c++) { u[c] = ldsr<float>(uva + 1024 + c * 128 + r * 4); v[c] = ldsr<float>(uva + 1088 + c * 128 + r * 4); asm("" : "+v"(u[c])); asm("" : "+v"(v[c])); }
      UNR for (int m = 0; m < 4; m++) { const unsigned a = lds0 + L_STT + (wm * 64 + m * 16 + 4 * g) * 4; const f32x4 rs = ldsr<f32x4>(a), nm = ldsr<f32x4>(a + 512);
        UNR for (int c = 0; c < 2; c++) acc[m][4 + c] = fma4(acc[m][4 + c], rs, fma4(nm, spl(u[c]), spl(v[c]))); }
      UNR for (int uu = 0; uu < 2; uu++) UNR for (int c = 0; c < 2; c++) ldsw<bf16x8>(lds0 + L_VF + w * 4096 + uu * 2048 + c * 1024 + lane * 16, pack8(acc[2 * uu][4 + c], acc[2 * uu + 1][4 + c])); }
    lbar();
    STAMP(15);
    asm volatile("" : "+v"(acc[0][0]), "+v"(acc[0][1]), "+v"(acc[1][0]), "+v"(acc[1][1]), "+v"(acc[2][0]), "+v"(acc[2][1]), "+v"(acc[3][0]), "+v"(acc[3][1]));
    bf16x8 qp[4], qn[4];
    { const f32x4 u0 = ldsr<f32x4>(uva + g * 16), v0 = ldsr<f32x4>(uva + 64 + g * 16) * L2E, u1 = ldsr<f32x4>(uva + 128 + g * 16), v1 = ldsr<f32x4>(uva + 192 + g * 16) * L2E;
      UNR for (int m = 0; m < 4; m += 2) feat2(wf, pack8(lnf(acc[m][0], st[m], u0, v0), lnf(acc[m][1], st[m], u1, v1)),
                                               pack8(lnf(acc[m + 1][0], st[m + 1], u0, v0), lnf(acc[m + 1][1], st[m + 1], u1, v1)), qp[m], qn[m], qp[m + 1], qn[m + 1]); }
    asm volatile("" : "+v"(qp[0]), "+v"(qp[1]), "+v"(qp[2]), "+v"(qp[3]), "+v"(qn[0]), "+v"(qn[1]), "+v"(qn[2]), "+v"(qn[3]) :: "memory");
    STAMP(16);
    bar();
    STAMP(17);
    const rsrc_t r2 = mkrsrc(ws, WS_KV2 + (size_t)(B.tn * 16 + B.tm) * 8192 + wn * 4096);
    u32x4 kk[4]; UNR for (int i = 0; i < 4; i++) kk[i] = ld_dev(r2, lane * 16, i * 1024);
    const bf16x8 z0 = ldsr<bf16x8>(lds0 + L_KX + wn * 2048 + lane * 16), z1 = ldsr<bf16x8>(lds0 + L_KX + wn * 2048 + 1024 + lane * 16);
    float rd[4];
    UNR for (int m = 0; m < 4; m++) rd[m] = __builtin_amdgcn_rcpf(mfma(z1, qn[m], mfma(z0, qp[m], zero))[0] + 1e-6f);
    asm volatile("" : "+v"(rd[0]), "+v"(rd[1]), "+v"(rd[2]), "+v"(rd[3]));
    if (__any(max(max(umax4(kk[0]), umax4(kk[1])), max(umax4(kk[2]), umax4(kk[3]))) == 0xffffffffu)) poll(kk, [&](int i) { return ld_dev(r2, lane * 16, i * 1024); });
    const rsrc_t ro = mkrsrc(ws, WS_O + (size_t)(B.tm * 16 + B.tn) * 16384);
    UNR for (int m = 0; m < 4; m++) {
      const f32x4 n0 = mfma(__builtin_bit_cast(bf16x8, kk[2]), qn[m], mfma(__builtin_bit_cast(bf16x8, kk[0]), qp[m], zero)), n1 = mfma(__builtin_bit_cast(bf16x8, kk[3]), qn[m], mfma(__builtin_bit_cast(bf16x8, kk[1]), qp[m], zero));
      st_dev(pack8u(n0 * rd[m], n1 * rd[m]), ro, lane * 16, (wm * 8 + wn) * 1024 + m * 2048);
    }
    STAMP(18);
    waitvm<0>(); bar();
    STAMP(19);
  }
  pin(lane);
  const int fp0 = B.tn * 7 + (B.tn & 1) + wn * 3, fph = (B.tn & 1) ? B.tn * 7 : B.tn * 7 + 6;
  u32x2 bb[7];
  {
    const int g = lane >> 4, r = lane & 15;
    UNR for (int n = 0; n < 7; n++) bb[n] = *(const u32x2*)(A.b1 + (n < 6 ? fp0 * 32 + n * 16 : fph * 32 + wn * 16) + 4 * g);
    u32x4 gp[4]; UNR for (int i = 0; i < 4; i++) gp[i] = ld_dev(mkrsrc(ws, WS_MODP + (48 + (B.tn & 7) * 2 + wn) * 4096), lane * 16, i * 1024);
    { const float v = modvec(ws, WS_MODP + (size_t)((w == 0 ? 0 : w == 3 ? 1 : 2) * 16 + (w < 2 ? B.tn : ((B.tm >> 3) + 2 * ((B.tm >> 2) & 1)) * 4 + (B.tm & 3))) * 4096, lane), s = (w == 1 || w == 2) ? rbf(1.f + v) : v;
      if (w < 2) ldsw<float>(lds0 + L_GT + w * 256 + modcol(lane) * 4, s); else put_et(lds0, w - 2, modcol(lane), s); }
    pin(bb);
    f32x4 acc[4][2];
    bar();
    if (w < 2) ldsw<float>(lds0 + L_GB + w * 256 + modcol(lane) * 4, modvec(ws, WS_MODP + (size_t)(48 + (B.tn & 7) * 2 + w) * 4096, lane, gp, true));
    compute_loop<2>(acc, lds0, wm, wn, lane, ts);
    STAMP(23);
    bar();
    f32x4 gate[2], sc[2]; u32x2 xr[4][2];
    UNR for (int n = 0; n < 2; n++) { gate[n] = ldsr<f32x4>(lds0 + L_GT + wn * 128 + n * 64 + g * 16); sc[n] = ldsr<f32x4>(lds0 + L_GT + 256 + wn * 128 + n * 64 + g * 16); }
    UNR for (int m = 0; m < 4; m++) UNR for (int n = 0; n < 2; n++) xr[m][n] = ldsr<u32x2>(lds0 + L_RES + (wm * 64 + m * 16 + r) * 128 + (((wn * 4 + n * 2 + (g >> 1)) ^ (r >> 1)) * 16) + (g & 1) * 8);
    const size_t tb = (size_t)(B.tm * 16 + B.tn) * 16384; const rsrc_t ry = outrsrc(A.out, tb), rst = mkrsrc(ws, WS_ST2 + (size_t)(B.tm * 32 + B.tn * 2 + wn) * 1024);
    f32x4 v[4][2]; float mean[4], m2[4]; u32x4 o[4];
    UNR for (int m = 0; m < 4; m++) {
      o[m] = pack8u(fma4(gate[0], acc[m][0], unp(xr[m][0][0], xr[m][0][1])), fma4(gate[1], acc[m][1], unp(xr[m][1][0], xr[m][1][1]))); v[m][0] = unp(o[m][0], o[m][1]); v[m][1] = unp(o[m][2], o[m][3]);
      st_dev(pack8u(v[m][0] * sc[0], v[m][1] * sc[1]), ry, lane * 16, (wm * 8 + wn) * 1024 + m * 2048); }
    UNR for (int m = 0; m < 4; m++) mean[m] = hsum4(v[m][0] + v[m][1]);
    const float mf = fold4<false>(mean[0], mean[1], mean[2], mean[3]) * (1.f / 32); spread4(mf, mean[0], mean[1], mean[2], mean[3]);
    UNR for (int m = 0; m < 4; m++) { const f32x4 d0 = v[m][0] - mean[m], d1 = v[m][1] - mean[m]; m2[m] = hsum4(fma4(d0, d0, d1 * d1)); }
    st_dev8(__builtin_bit_cast(u32x2, f32x2{mf, fold4<false>(m2[0], m2[1], m2[2], m2[3])}), rst, (wm * 64 + ((g >> 1) + 2 * (g & 1)) * 16 + r) * 8, 0);
    STAMP(24);
    asm volatile("" ::: "memory");
    UNR for (int m = 0; m < 4; m++) st_dev(o[m], mkrsrc(ws, WS_X1 + tb), lane * 16, (wm * 8 + wn) * 1024 + m * 2048);
    waitvm<4>(); bar();
    STAMP(25);
  }
  pin(lane);
  {
    const int g = lane >> 4, r = lane & 15;
    f32x4 acc[4][7];
    bar();
    compute_loop<1>(acc, lds0, wm, wn, lane, ts);
    STAMP(29);
    bar();
    STAMP(30);
    f32x4 u[7], v[7];
    UNR for (int n = 0; n < 7; n++) { const unsigned a = lds0 + L_UVT + (n < 6 ? wn * 6 + n : 12 + wn) * 128 + g * 16; u[n] = ldsr<f32x4>(a); v[n] = ldsr<f32x4>(a + 64) + unp(bb[n][0], bb[n][1]); }
    const rsrc_t rh = mkrsrc(ws, WS_H1 + (size_t)B.tm * 56 * 16384);
    RowSt st[4]; u32x2 h6[4];
    UNR for (int m = 0; m < 4; m++) st[m].set(lds0 + L_STT + (wm * 64 + m * 16 + r) * 4, 1.f);
    auto act = [&](int m, int n) { const f32x4 t = __builtin_elementwise_max(lnf(acc[m][n], st[m], u[n], v[n]), zero); return t * t; };
    UNR for (int n = 0; n < 6; n += 2) { const int fp = fp0 + (n >> 1);
      UNR for (int m = 0; m < 4; m++) st_dev(pack8u(act(m, n), act(m, n + 1)), rh, lane * 16, (fp >> 1) * 16384 + (fp & 1) * 1024 + wm * 8192 + m * 2048); }
    UNR for (int m = 0; m < 4; m++) { const f32x4 t = act(m, 6); h6[m] = u32x2{cvtpk(t[0], t[1]), cvtpk(t[2], t[3])}; }
    UNR for (int m = 0; m < 4; m += 2) { const auto a = __builtin_amdgcn_permlane16_swap(h6[m][0], h6[m + 1][0], false, false), b = __builtin_amdgcn_permlane16_swap(h6[m][1], h6[m + 1][1], false, false);
      st_dev(u32x4{a[0], b[0], a[1], b[1]}, rh, (g & 1) * 2048 + (g >> 1) * 256 + r * 16, (fph >> 1) * 16384 + (fph & 1) * 1024 + wm * 8192 + m * 2048 + wn * 512); }
    STAMP(31);
    bar(); waitvm<0>(); bar();
    STAMP(32);
  }
  pin(lane);
  {
    const int g = lane >> 4, r = lane & 15, kh = B.tn >> 3, cg = (B.tn & 7) * 2 + wn;
    u32x2 bb[4]; u32x4 xr[2][2];
    UNR for (int n = 0; n < 4; n++) bb[n] = *(const u32x2*)(A.b2 + cg * 64 + n * 16 + 4 * g);
    pin(bb);
    f32x4 acc[4][4];
    bar();
    compute_loop<3>(acc, lds0, wm, wn, lane, ts);
    STAMP(37);
    UNR for (int mm = 0; mm < 2; mm++) UNR for (int ks = 0; ks < 2; ks++) xr[mm][ks] = ldsr<u32x4>(lds0 + L_OT + w * 4096 + (mm * 2 + ks) * 1024 + lane * 16);
    const rsrc_t rx = mkrsrc(ws, xchoff(B.tm, B.tn, w)), rp = mkrsrc(ws, xchoff(B.tm, B.tn ^ 8, w));
    f32x4 keep[2][4], gate[4];
    if (kh) { UNR for (int i = 0; i < 4; i++) st_dev(pack8u(acc[i >> 1][(i & 1) * 2], acc[i >> 1][(i & 1) * 2 + 1]), rx, lane * 16, i * 1024);
              UNR for (int n = 0; n < 4; n++) { keep[0][n] = acc[2][n]; keep[1][n] = acc[3][n]; } }
    else    { UNR for (int i = 0; i < 4; i++) st_dev(pack8u(acc[2 + (i >> 1)][(i & 1) * 2], acc[2 + (i >> 1)][(i & 1) * 2 + 1]), rx, lane * 16, i * 1024);
              UNR for (int n = 0; n < 4; n++) { keep[0][n] = acc[0][n]; keep[1][n] = acc[1][n]; } }
    UNR for (int n = 0; n < 4; n++) gate[n] = ldsr<f32x4>(lds0 + L_GB + wn * 256 + n * 64 + g * 16);
    UNR for (int mm = 0; mm < 2; mm++) UNR for (int n = 0; n < 4; n++) keep[mm][n] = fma4(gate[n], keep[mm][n] + unp(bb[n][0], bb[n][1]), unp(xr[mm][n >> 1][(n & 1) * 2], xr[mm][n >> 1][(n & 1) * 2 + 1]));
    pin(keep[0]); pin(keep[1]);
    if (DR & 2) waitvm<0>();
    u32x4 pp[4]; poll(pp, [&](int i) { return ld_dev(rp, lane * 16, i * 1024); });
    STAMP(38);
    const unsigned ot = lds0 + L_OT + w * 4096;
    UNR for (int mm = 0; mm < 2; mm++) UNR for (int n = 0; n < 4; n++) { const u32x4 t = pp[mm * 2 + (n >> 1)]; const f32x4 y = fma4(gate[n], unp(t[(n & 1) * 2], t[(n & 1) * 2 + 1]), keep[mm][n]);
      ldsw<u32x2>(ot + (mm * 16 + r) * 128 + (((n * 2 + (g >> 1)) ^ (r >> 1)) * 16) + (g & 1) * 8, u32x2{cvtpk(y[0], y[1]), cvtpk(y[2], y[3])}); }
    lwait();
    const int r8 = lane >> 3; const unsigned rv0 = r8 * 2048 + (((lane & 7) ^ (r8 >> 1)) * 16);
    char* op = (char*)A.out + ((size_t)(B.tm * 128 + wm * 64 + kh * 32) * D_ + cg * 64) * 2;
    UNR for (int i = 0; i < 4; i++) *(u32x4*)(op + i * 16384 + ((i & 1) ? rv0 ^ 64 : rv0)) = ldsr<u32x4>(ot + i * 1024 + lane * 16);
    STAMP(39);
  }
}

__global__ __launch_bounds__(512, 2) void k_block(Args A) {
  __shared__ __attribute__((aligned(1024))) char smem[L_TOT];
  const int w = __builtin_amdgcn_readfirstlane(threadIdx.x >> 6);
  const unsigned lds0 = (unsigned)(size_t)(LAS char*)smem;
  asm volatile(".Lcode_begin:");
  if ((PT & 2) && w == 3) touch_code(threadIdx.x & 63, lds0 + L_DUM);
  unsigned ts = 0; STAMP(0);
  const u64 tag = phase0(A, w, lds0, ts);
  if (w >= 4) loader_role(A, w - 4, lds0, tag, ts); else compute_role(A, w, lds0, (unsigned)tag, ts);
  if (blockIdx.x == 77 && w == 0) { const unsigned a = __builtin_amdgcn_readlane(ts, DIAG0), b = __builtin_amdgcn_readlane(ts, DIAG), s0 = __builtin_amdgcn_readlane(ts, 0);
    const unsigned until = s0 + 10000 + 10 * (b - a);
    for (int i = 0; i < 1000000 && (int)(until - (unsigned)__builtin_amdgcn_s_memrealtime()) > 0; i++) __builtin_amdgcn_s_sleep(1); }
}

extern "C" void kernel_launch(void* const* d_in, const int* in_sizes, int n_in, void* d_out, int out_size, void* d_ws, size_t ws_size, hipStream_t stream) {
  Args A; A.x = (const u16*)d_in[0]; A.wqkv = (const u16*)d_in[1]; A.wphi = (const u16*)d_in[2]; A.wo = (const u16*)d_in[3]; A.w1 = (const u16*)d_in[5]; A.b1 = (const u16*)d_in[6];
  A.w2 = (const u16*)d_in[7]; A.b2 = (const u16*)d_in[8]; A.c = (const u16*)d_in[9]; A.wm = (const u16*)d_in[10]; A.out = (u16*)d_out; A.ws = (char*)d_ws;
  k_block<<<256, 512, 0, stream>>>(A);
}
```

```cpp
#include <hip/hip_runtime.h>
#include <hip/hip_bf16.h>

typedef unsigned short u16;
typedef unsigned u32x4 __attribute__((ext_vector_type(4)));
typedef unsigned u32x2 __attribute__((ext_vector_type(2)));
typedef short s16x4 __attribute__((ext_vector_type(4)));
typedef __bf16 bf16x8 __attribute__((ext_vector_type(8)));
typedef __bf16 bf16x2 __attribute__((ext_vector_type(2)));
typedef float f32x2 __attribute__((ext_vector_type(2)));
typedef float f32x4 __attribute__((ext_vector_type(4)));
#define LAS __attribute__((address_space(3)))
#define DI __device__ __forceinline__
#define SB __builtin_amdgcn_sched_barrier(0)
#define UNR _Pragma("unroll")

constexpr int D_ = 1024, F_ = 3584, MODN = 6 * D_;
constexpr size_t WS_EPOCH = 0;
constexpr size_t WS_FLAG  = 4 << 20;
constexpr size_t WS_GX    = 1 << 18;
constexpr size_t WS_ST1   = 1 << 19;
constexpr size_t WS_MODP  = 3 << 19;
constexpr size_t WS_UV1   = WS_MODP + 4 * 65536;
constexpr size_t WS_ZP    = WS_UV1 + 16 * 12 * 2048;
constexpr size_t WS_UV2   = WS_ZP + (1 << 17);
constexpr size_t WS_ST2   = WS_UV2 + (1 << 19);
constexpr size_t WS_KV1   = 10 << 20;
constexpr size_t WS_KV2   = WS_KV1 + (2 << 20);
constexpr size_t WS_O     = 14 << 20;
constexpr size_t WS_X1    = 18 << 20;
constexpr size_t WS_H1    = 22 << 20;
constexpr size_t WS_STAMP = 40 << 20;
constexpr int SPIN_CAP = 1 << 14;
typedef unsigned long long u64;

#define STAMP(i) do { } while (0)

DI void glds16(const void* sbase, unsigned voff, unsigned ldsaddr) {
  asm volatile("s_mov_b32 m0, %2\n\ts_nop 0\n\tglobal_load_lds_dwordx4 %0, %1" :: "v"(voff), "s"(sbase), "s"(ldsaddr) : "memory");
}
DI void glds16x4(const void* sbase, unsigned voff, unsigned ldsaddr) {
  asm volatile("s_mov_b32 m0, %2\n\ts_nop 0\n\tglobal_load_lds_dwordx4 %0, %1 sc1\n\tglobal_load_lds_dwordx4 %0, %1 offset:1024 sc1\n\t"
               "global_load_lds_dwordx4 %0, %1 offset:2048 sc1\n\tglobal_load_lds_dwordx4 %0, %1 offset:3072 sc1" :: "v"(voff), "s"(sbase), "s"(ldsaddr) : "memory");
}
DI void prio(int p) { if (p) __builtin_amdgcn_s_setprio(3); else __builtin_amdgcn_s_setprio(0); }
DI void touch(const void* sbase, unsigned voff, unsigned ldsdummy) {
  asm volatile("s_mov_b32 m0, %2\n\ts_nop 0\n\tglobal_load_lds_dword %0, %1" :: "v"(voff), "s"(sbase), "s"(ldsdummy) : "memory");
}
DI void touch_any(const void* p, unsigned ldsdummy) {
  asm volatile("s_mov_b32 m0, %1\n\ts_nop 0\n\tglobal_load_lds_dword %0, off" :: "v"(p), "s"(ldsdummy) : "memory");
}
DI void gld_dev(u32x4& r, const void* sb, unsigned vo) { asm volatile("global_load_dwordx4 %0, %1, %2 sc1" : "=v"(r) : "v"(vo), "s"(sb) : "memory"); }
DI void gld4(unsigned& r, const void* sb, unsigned vo) { asm volatile("global_load_dword %0, %1, %2" : "=v"(r) : "v"(vo), "s"(sb) : "memory"); }
template <class T> DI void pin(T& r) { asm volatile("" : "+v"(r)); }
template <class T, int N> DI void pin(T (&r)[N]) { UNR for (int i = 0; i < N; i++) pin(r[i]); }
template <class T> DI T opq(T v) { asm volatile("" : "+s"(v)); return v; }
DI f32x4 asf(u32x4 v) { return __builtin_bit_cast(f32x4, v); }
template <int N> DI void waitvm() { asm volatile("s_waitcnt vmcnt(%0)" :: "i"(N) : "memory"); }
template <int B, int LO = 0, int HI = 12> DI void waitx(int e) {
  if constexpr (LO == HI) waitvm<B + LO>();
  else if constexpr (LO == 0) { if (e == 0) waitvm<B>(); else waitx<B, 1, HI>(e); }
  else { constexpr int MID = (LO + HI + 1) / 2; if (e >= MID) waitx<B, MID, HI>(e); else waitx<B, LO, MID - 1>(e); }
}
DI void waitlgkm0() { __builtin_amdgcn_s_waitcnt(0xC07F); }
DI void lwait() { asm volatile("s_waitcnt lgkmcnt(0)" ::: "memory"); }
DI void bar() { asm volatile("s_barrier" ::: "memory"); }
DI void lbar() { asm volatile("s_waitcnt lgkmcnt(0)\n\ts_barrier" ::: "memory"); }
DI unsigned cvtpk(float a, float b) { f32x2 v = {a, b}; return __builtin_bit_cast(unsigned, __builtin_convertvector(v, bf16x2)); }
DI float rbf(float a) { return __uint_as_float(cvtpk(a, a) << 16); }
DI f32x4 rbf4(f32x4 s) { return f32x4{rbf(s[0]), rbf(s[1]), rbf(s[2]), rbf(s[3])}; }
DI float blo(unsigned u) { return __uint_as_float(u << 16); }
DI float bhi(unsigned u) { return __uint_as_float(u & 0xffff0000u); }
DI f32x4 unp(unsigned a, unsigned b) { return f32x4{blo(a), bhi(a), blo(b), bhi(b)}; }
DI u32x4 pack8u(const f32x4& a, const f32x4& b) { return u32x4{cvtpk(a[0], a[1]), cvtpk(a[2], a[3]), cvtpk(b[0], b[1]), cvtpk(b[2], b[3])}; }
DI bf16x8 pack8(const f32x4& a, const f32x4& b) { return __builtin_bit_cast(bf16x8, pack8u(a, b)); }
DI f32x4 mfma(bf16x8 a, bf16x8 b, f32x4 c) { return __builtin_amdgcn_mfma_f32_16x16x32_bf16(a, b, c, 0, 0, 0); }
DI s16x4 trd(unsigned a) { return __builtin_amdgcn_ds_read_tr16_b64_v4i16((LAS s16x4*)(size_t)a); }
DI bf16x8 cat(s16x4 lo, s16x4 hi) { return __builtin_bit_cast(bf16x8, __builtin_shufflevector(lo, hi, 0, 1, 2, 3, 4, 5, 6, 7)); }
template <class T> DI T ldsr(unsigned a) { return *(const LAS T*)(size_t)a; }
template <class T> DI void ldsw(unsigned a, T v) { *(LAS T*)(size_t)a = v; }
template <int C> DI float dpp(float x) { return __builtin_bit_cast(float, __builtin_amdgcn_update_dpp(0, __builtin_bit_cast(int, x), C, 0xf, 0xf, true)); }
DI float vmax(float a, float b) { float r; asm("v_max_f32_e32 %0, %1, %2" : "=v"(r) : "v"(a), "v"(b)); return r; }
DI float sw32_add(float a, float b) { auto r = __builtin_amdgcn_permlane32_swap(__float_as_uint(a), __float_as_uint(b), false, false); return __uint_as_float(r[0]) + __uint_as_float(r[1]); }
DI float sw16_add(float a, float b) { auto r = __builtin_amdgcn_permlane16_swap(__float_as_uint(a), __float_as_uint(b), false, false); return __uint_as_float(r[0]) + __uint_as_float(r[1]); }
DI float xg_sum(float a) { a = sw32_add(a, a); return sw16_add(a, a); }
DI float xg_max(float a) {
  auto r = __builtin_amdgcn_permlane32_swap(__float_as_uint(a), __float_as_uint(a), false, false); a = vmax(__uint_as_float(r[0]), __uint_as_float(r[1]));
  auto q = __builtin_amdgcn_permlane16_swap(__float_as_uint(a), __float_as_uint(a), false, false); return vmax(__uint_as_float(q[0]), __uint_as_float(q[1]));
}
DI float row16_sum(float a) { a += dpp<0x128>(a); a += dpp<0x124>(a); a += dpp<0x122>(a); a += dpp<0x121>(a); return a; }
DI f32x4 row16_sum4(f32x4 a) { return f32x4{row16_sum(a[0]), row16_sum(a[1]), row16_sum(a[2]), row16_sum(a[3])}; }
DI f32x4 spl(float a) { return f32x4{a, a, a, a}; }
DI f32x4 fma4(f32x4 a, f32x4 b, f32x4 c) { return __builtin_elementwise_fma(a, b, c); }
struct RowSt { float rs, nm; DI void set(unsigned a, float k) { rs = ldsr<float>(a) * k; nm = ldsr<float>(a + 512) * k; asm("" : "+v"(rs)); asm("" : "+v"(nm)); } };
DI f32x4 lnf(f32x4 acc, const RowSt& st, f32x4 u, f32x4 v) { return fma4(acc, spl(st.rs), fma4(u, spl(st.nm), v)); }
DI float fadd(float a, float b) { float r; asm("v_add_f32_e32 %0, %1, %2" : "=v"(r) : "v"(a), "v"(b)); return r; }
DI float hsum4(f32x4 a) { const f32x2 t = f32x2{a[0], a[1]} + f32x2{a[2], a[3]}; float r; asm("v_add_f32_e32 %0, %1, %2" : "=v"(r) : "v"(t[0]), "v"(t[1])); return r; }
struct rsrc_t { __amdgpu_buffer_rsrc_t r; unsigned base; };
DI rsrc_t mkrsrc(const char* ws, size_t off) { return rsrc_t{__builtin_amdgcn_make_buffer_rsrc((void*)ws, 0, 64 << 20, 0x00020000), (unsigned)off}; }
DI rsrc_t outrsrc(const void* out, size_t off) { return rsrc_t{__builtin_amdgcn_make_buffer_rsrc((void*)out, 0, 2048 * D_ * 2, 0x00020000), (unsigned)off}; }
DI unsigned xchoff(int tm, int tn, int w) { return WS_KV1 + (w >> 1) * (2 << 20) + (tn * 16 + tm) * 8192 + (w & 1) * 4096; }
DI u32x4 ld_dev(const rsrc_t& r, unsigned voff, unsigned soff) { return __builtin_amdgcn_raw_buffer_load_b128(r.r, voff, r.base + soff, 16); }
DI void st_dev(u32x4 v, const rsrc_t& r, unsigned voff, unsigned soff) { __builtin_amdgcn_raw_buffer_store_b128(v, r.r, voff + (r.base + soff), 0, 16); }
DI void st_dev8(u32x2 v, const rsrc_t& r, unsigned voff, unsigned soff) { __builtin_amdgcn_raw_buffer_store_b64(v, r.r, voff + (r.base + soff), 0, 16); }
DI unsigned umax4(u32x4 v) { return max(max(v[0], v[1]), max(v[2], v[3])); }
template <int N, class F> DI void poll(u32x4 (&v)[N], F ld) {
  _Pragma("nounroll") for (int spin = 0; spin < SPIN_CAP; spin++) { unsigned m = 0;
    UNR for (int i = 0; i < N; i++) v[i] = ld(i);
    UNR for (int i = 0; i < N; i++) m = max(m, umax4(v[i]));
    if (!__any(m == 0xffffffffu)) break;
    asm volatile("s_sleep 2" ::: "memory");
  }
}
#define EV 0
#define W2T 1
#define XF 1
#define AN 1
#define OS 16
#define PW 1
DI float fmax3(float a, float b, float c) { return __builtin_elementwise_maximum(__builtin_elementwise_maximum(a, b), c); }
struct Blk { int tm, tn, rot, j, xq; };
DI Blk blk1() { const int xcd = blockIdx.x & 7, j = blockIdx.x >> 3; Blk b; b.tm = j >> 1; b.tn = xcd * 2 + (j & 1); b.rot = b.tm; b.j = j; b.xq = xcd; return b; }
DI Blk blk0() { Blk b = blk1(); const int t = b.tm; b.tm = b.tn; b.tn = t; return b; }
DI Blk blk() { const int xcd = blockIdx.x & 7, j = blockIdx.x >> 3; Blk b; b.tm = (xcd >> 2) * 8 + (j >> 2); b.tn = (xcd & 3) * 4 + (j & 3); b.rot = (b.tm & 7) * 2 + (b.tm >> 3); b.j = j; b.xq = xcd & 3; return b; }
DI unsigned ptile(int c, int lane) { const int row = 4 * (lane >> 4) + ((lane & 15) >> 2); return row * 128 + ((c ^ ((row >> 1) & 3)) * 32) + (lane & 3) * 8; }
DI unsigned htile(int c, int lane) { const int row = 4 * (lane >> 4) + ((lane & 15) >> 2); return row * 64 + ((c ^ ((row >> 2) & 1)) * 32) + (lane & 3) * 8; }
DI bf16x8 wfrag(unsigned a, unsigned ts) { return cat(trd(a), trd(a + ts)); }

DI unsigned silu2(unsigned cw) { const float c0 = blo(cw), c1 = bhi(cw); return cvtpk(c0 * __builtin_amdgcn_rcpf(1.f + __expf(-c0)), c1 * __builtin_amdgcn_rcpf(1.f + __expf(-c1))); }
DI unsigned fragpos(int k) { return (k >> 5) * 64 + ((k >> 2) & 3) * 16 + ((k & 3) + 4 * ((k >> 4) & 1)) * 2; }
DI f32x4 gemv64(unsigned a, bf16x8 s0, bf16x8 s1, int lane) { f32x4 d[4];
  UNR for (int cc = 0; cc < 4; cc++) d[cc] = mfma(wfrag(a + 4096 + ptile(cc, lane), 2048), s1, mfma(wfrag(a + ptile(cc, lane), 2048), s0, f32x4{0, 0, 0, 0}));
  const bool b0 = lane & 1, b1 = lane & 2; f32x4 o;
  UNR for (int k = 0; k < 4; k++) { const float lo = b0 ? d[1][k] : d[0][k], hi = b0 ? d[3][k] : d[2][k]; o[k] = b1 ? hi : lo; }
  return o;
}

template <int CFG> struct Cfg;
template <> struct Cfg<0> { static constexpr int NT = 6, NP = 3, HALF = 0, NB = 2, NS = 3, N = 3 * D_, KT = 16, B0 = 49152, BS0 = 0, OWN = 0; };
template <> struct Cfg<1> { static constexpr int NT = 7, NP = 3, HALF = 1, NB = 7, NS = 3, N = F_, KT = 16, B0 = 65536, BS0 = 2, OWN = 1; };
template <> struct Cfg<2> { static constexpr int NT = 2, NP = 1, HALF = 0, NB = 2, NS = 4, N = D_, KT = 16, B0 = 73728, BS0 = 0, OWN = 0; };
template <> struct Cfg<3> { static constexpr int NT = 4, NP = 2, HALF = 0, NB = 4, NS = 4, N = D_, KT = 28, B0 = 65536, BS0 = 0, OWN = 3; };
template <int CFG> struct Lay { using C = Cfg<CFG>; static constexpr int SBB = C::NP * 8192 + C::HALF * 4096, PB = 2 * C::NP + C::HALF, PER = 4 + PB; };
template <int CFG> DI unsigned btile(int tl, int lane) { using C = Cfg<CFG>; return C::B0 + (tl < 4 * C::NP ? (tl >> 2) * 8192 + ptile(tl & 3, lane) : C::NP * 8192 + htile(tl & 1, lane)); }
constexpr int L_RES = 106496, L_WPHI = 155648, L_UVT = L_WPHI + 4096, L_STT = L_UVT + 2048, L_ET = L_STT + 1024, L_GT = L_ET + 256, L_DUM = L_GT + 512, L_TOT = 163840, L_REST = 122880, L_OT = 131072;
constexpr int L_UN = 0, L_XT = 131072, L_RED = L_WPHI, L_SROW = L_RED + 2048, L_SIL2 = L_GT;
constexpr int L_GB = L_WPHI;
constexpr int L_VF = 0, L_TR = 16384, L_KX = 49152, L_RV = 90112;

template <int CFG> DI void compute_loop(f32x4 (&acc)[4][Cfg<CFG>::NT], unsigned lds0, int wm, int wn, int lane, unsigned& ts) {
  using C = Cfg<CFG>; using L = Lay<CFG>; constexpr int MT = 4, NT = C::NT, NB = C::NB, KT = C::KT;
  const unsigned aoff = lds0 + wm * 8192 + lane * 16; unsigned boff[NB];
  UNR for (int i = 0; i < NB; i++) boff[i] = lds0 + btile<CFG>(CFG == 1 ? (i < 6 ? wn * 6 + i : 12 + wn) : wn * NB + i, lane);
  bf16x8 fa[2][MT]; s16x4 fb[2][NT][2]; unsigned va, vb[NB];
  constexpr int NR = MT + 2 * NT, NM = MT * NT, NU = 1 + NB, FREE = NM - NR, UPG = FREE > 0 ? (NU + FREE - 1) / FREE : NU;
  auto upd = [&](int i, unsigned sa, unsigned sb) { if (i == 0) { va = aoff + sa; pin(va); } else { vb[i - 1] = boff[i - 1] + sb; pin(vb[i - 1]); } };
  auto rd1 = [&](int buf, int ks, int i) {
    if (i == 0 || i > 2 * NT) { const int m = i ? i - 2 * NT : 0; fa[buf][m] = ldsr<bf16x8>(va + m * 2048 + ks * 1024); }
    else { const int n = (i - 1) >> 1, t = (i - 1) & 1;
      fb[buf][n][t] = trd(CFG == 0 ? vb[n & 1] + (n >> 1) * 8192 + ks * 4096 + t * 2048 : (CFG == 1 && n == 6) ? vb[NB - 1] + ks * 2048 + t * 1024 : vb[n] + ks * 4096 + t * 2048); }
  };
  unsigned rawk = 0, rawv = 0;
  auto step = [&](int buf, int ks, bool doread, bool first, bool doupd, unsigned sa, unsigned sb) {
    UNR for (int i = 0; i < NM; i++) {
      const int m = i / NT, n = i % NT; const bf16x8 b = cat(fb[buf][n][0], fb[buf][n][1]);
      const f32x4 cin = first ? f32x4{0, 0, 0, 0} : acc[m][n];
      acc[m][n] = (CFG == 0 && n >= 4) ? mfma(fa[buf][m], b, cin) : mfma(b, fa[buf][m], cin);
      SB;
      if (doread && i < NR) { rd1(buf ^ 1, ks, i); SB; }
      if (CFG == 0 && !doread && i >= 14 && i < 22) { const int j = i - 14, hm = j >> 2, hn = 2 + (j & 3);
        ldsw<f32x4>(hn < 4 ? rawk + (hn & 1) * 4096 + hm * 1024 : rawv + (hm * 2 + (hn & 1)) * 1024, acc[hm][hn % NT]); SB; }
      if (doupd && (FREE > 0 ? i >= NR : i == NM - 1)) { const int u0 = FREE > 0 ? (i - NR) * UPG : 0;
        UNR for (int u = u0; u < u0 + UPG; u++) if (u < NU) upd(u, sa, sb);
        SB; }
    }
  };
  UNR for (int i = 0; i < NU; i++) upd(i, 0, C::BS0 * L::SBB);
  SB; lbar();
  STAMP(CFG == 0 ? 12 : CFG == 2 ? 22 : CFG == 1 ? 28 : 36);
  UNR for (int i = 0; i < NR; i++) rd1(0, 0, i);
  SB;
  unsigned sa = 16384, sb = ((C::BS0 + 1) % C::NS) * L::SBB;
  step(0, 1, true, true, true, sa, sb);
  for (int t = 0; t < KT - 1; t++) {
    waitlgkm0();
    if (C::OWN && t == C::OWN - 1) { if (CFG == 3) { waitvm<0>(); STAMP(32); bar(); } bar(); }
    bar(); SB;
    sa += 16384; if (sa == C::NS * 16384) sa = 0;
    sb += L::SBB; if (sb == C::NS * L::SBB) sb = 0;
    step(1, 0, true, false, false, 0, 0);
    if (FREE >= 4) { waitlgkm0(); SB; }
    step(0, 1, true, false, true, sa, sb);
  }
  if (CFG == 0) { rawk = lds0 + L_TR + (wm * 2 + wn) * 8192 + lane * 16; rawv = lds0 + L_RV + (wm * 2 + wn) * 4096 + lane * 16; pin(rawk); pin(rawv); SB; }
  step(1, 0, false, false, false, 0, 0);
}

template <int CFG> struct Loader {
  using C = Cfg<CFG>; using L = Lay<CFG>; static constexpr int N = C::N, KT = C::KT, PER = L::PER, PB = L::PB, NS = C::NS, OWN = C::OWN;
  const char *ab, *bb, *hb; unsigned av, bv, hv, la, lb, lh; int ka, kb, own;
  DI int skip(int k) const { if (k == KT) k = 0; if (OWN && k == own) { k += OWN; if (k == KT) k = 0; } return k; }
  DI void init(const void* A, int akt, const u16* W, int k0, int col0, int hcol, int rot, int own_, int tm, int lw, int lane, unsigned lds0) {
    const int r = lane >> 3, hr = lane >> 2;
    ab = (const char*)A + ((size_t)tm * akt + k0) * 16384 + lw * 4096; av = lane * 16;
    bb = (const char*)W + ((size_t)(k0 * 64 + lw * 16) * N + col0) * 2; bv = r * N * 2 + (((lane & 7) ^ (((r >> 1) & 3) << 1)) * 16);
    hb = (const char*)W + ((size_t)(lw * 16) * N + hcol) * 2; hv = hr * N * 2 + (((lane & 3) ^ (((hr >> 2) & 1) << 1)) * 16);
    if (CFG == 3) { const int t = lw & 1; bb -= (size_t)t * 16 * N * 2; hv = bv + (4 * t + 4 * (r >> 2)) * N * 2; bv += t * 16 * N * 2; }
    la = lds0 + lw * 4096; lb = lds0 + C::B0 + lw * 2048; lh = lds0 + C::B0 + C::NP * 8192 + lw * 1024; own = own_; ka = kb = skip(rot);
  }
  DI void issueA(int slot) { glds16x4(ab + (size_t)ka * 16384, av, la + slot * 16384); ka = skip(ka + 1); }
  DI void issueBk(int slot, int k) const { const char* b = bb + (size_t)k * 64 * N * 2; slot += C::BS0; if (slot >= NS) slot -= NS; const unsigned so = slot * L::SBB;
    const bool nat = CFG == 3 && ((0x1020408u >> k) & 1);
    const unsigned v0 = nat ? hv : bv, v1 = v0 + (nat ? 16 : 8) * N * 2;
    UNR for (int i = 0; i < 2 * C::NP; i++) glds16(b + (i >> 1) * (CFG == 0 ? 2048 : 128) + (CFG == 3 ? 0 : (i & 1) * (8 * N * 2)), CFG == 3 && (i & 1) ? v1 : v0, lb + so + (i >> 1) * 8192 + (i & 1) * 1024);
    if (C::HALF) glds16(hb + (size_t)k * 64 * N * 2, hv, lh + so);
  }
  DI void issueB(int slot) { issueBk(slot, kb); kb = skip(kb + 1); }
  DI void issue(int slot) { issueA(slot); issueB(slot); }
  DI u64 first_need(int n, int k0) const { u64 m = 0; int k = ka;
    for (int i = 0; i < n; i++) { if (CFG == 3) m |= (1ull << ((2 * (k0 + k)) / 7)) | (1ull << ((2 * (k0 + k) + 1) / 7)); else m |= 1ull << k; k = skip(k + 1); }
    return PW ? m : 0xffffull; }
  template <int E1, class HP, class HF, class HK, class H1> DI void run3(int e0, HP hp, HF hf, HK hk, H1 h1) {
    UNR for (int t = 1; t <= 6; t++) { hp(t); issue((t - 1) % 3); int e1 = hf(t); if (t == 6) e1 += hk(t); waitx<PER>(e0 + e1); lbar(); e0 = e1; }
    int slot = 0;
    _Pragma("nounroll") for (int t = 7; t <= KT - 3; t++) { issue(slot); slot++; if (slot == NS) slot = 0; const int e1 = hk(t); waitx<PER>(e0 + e1); lbar(); e0 = e1; }
    h1(); waitvm<E1>(); bar();
  }
  template <int E1, bool W1, class H0, class H1> DI void run4(H0 h0, H1 h1) {
    UNR for (int s = 0; s < 4; s++) issueA((OWN + s) & 3);
    waitvm<12>(); bar(); h0(); waitvm<8>(); bar();
    issue(OWN & 3); waitvm<4 + PER>(); bar();
    int slot = (OWN + 1) & 3;
    for (int t = 2; t <= KT - OWN - 4; t++) { issue(slot); slot = (slot + 1) & 3; waitvm<2 * PER>(); bar(); }
    if (W1) { h1(); waitvm<PER + E1>(); bar(); waitvm<0>(); bar(); } else { waitvm<PER>(); bar(); h1(); waitvm<E1>(); bar(); }
  }
};
DI u64* flagp(char* ws, int tile, int line, int word) { return (u64*)(ws + WS_FLAG) + (tile * 8 + line) * 16 + word; }
DI u64 wait_flags(u64* f, int lane, int n, u64 tag, u64 need) {
  u64 got = 0;
  _Pragma("nounroll") for (int spin = 0; spin < SPIN_CAP; spin++) { u64 v = tag;
    if (lane < n) asm volatile("global_atomic_or_x2 %0, %1, %2, off sc0 sc1\n\ts_waitcnt vmcnt(0)" : "=&v"(v) : "v"(f + lane), "v"(0ull) : "memory");
    got = __ballot(v == tag);
    if ((got & need) == need) break;
    asm volatile("s_sleep 1" ::: "memory");
  }
  return got;
}
struct Sync { u64* f; int n; u64 tag; bool all;
  template <class H> DI void run(char* ws, int line, const Blk& P, const Blk& C, int lw, int lane, u64 tag_, u64 need, H pre) {
    bar();
    tag = tag_; f = flagp(ws, C.tm * 16 + C.tn, line, 0); n = line == 0 ? 32 : 16; all = true;
    if (lw == 0) { const int i = lane & 15, grp = lane >> 4;
      if (grp < n / 16) __hip_atomic_store(grp == 0 ? flagp(ws, P.tm * 16 + i, line, P.tn) : flagp(ws, i * 16 + C.tn, 1, C.tm), tag, __ATOMIC_RELAXED, __HIP_MEMORY_SCOPE_AGENT); }
    pre();
    if (lw == 0) all = wait_flags(f, lane, n, tag, need) == ~0ull;
    lbar();
  }
  DI void rest(int lane) { if (!all) wait_flags(f, lane, n, tag, ~0ull); }
};
DI int modcol(int lane) { return 4 * (lane & 15) + 2 * (lane >> 5) + ((lane >> 4) & 1); }
DI float modvec(const char* ws, unsigned part, int lane, u32x4 (&p)[4], bool have) {
  if (!have || __any(max(max(umax4(p[0]), umax4(p[1])), max(umax4(p[2]), umax4(p[3]))) == 0xffffffffu)) { const rsrc_t r = mkrsrc(ws, part); poll(p, [&](int i) { return ld_dev(r, lane * 16, i * 1024); }); }
  const f32x4 a = (asf(p[0]) + asf(p[1])) + (asf(p[2]) + asf(p[3]));
  return rbf(sw16_add(sw32_add(a[0], a[2]), sw32_add(a[1], a[3])));
}
DI float modvec(const char* ws, unsigned part, int lane) { u32x4 p[4]; return modvec(ws, part, lane, p, false); }
DI void put_et(unsigned lds0, int vec, int k, float v) { ldsw<u16>(lds0 + L_ET + vec * 128 + fragpos(k), (u16)(__float_as_uint(v) >> 16)); }

template <int CFG, int NTL> struct UV {
  bf16x8 f[NTL][2]; unsigned ad[NTL], ts[NTL];
  DI void prep(int lw, int lane, unsigned base) { UNR for (int i = 0; i < NTL; i++) { const int tl = min(lw * NTL + i, 2 * Cfg<CFG>::NT - 1); ad[i] = base + btile<CFG>(tl, lane); ts[i] = tl < 4 * Cfg<CFG>::NP ? 2048 : 1024; pin(ad[i]); } }
  DI void read() { UNR for (int i = 0; i < NTL; i++) { f[i][0] = wfrag(ad[i], ts[i]); f[i][1] = wfrag(ad[i] + 2 * ts[i], ts[i]); } }
  bf16x8 e0, e1;
  DI void reade(int lane, unsigned lds0) { e0 = ldsr<bf16x8>(lds0 + L_ET + (lane & 1) * 128 + (lane >> 4) * 16); e1 = ldsr<bf16x8>(lds0 + L_ET + (lane & 1) * 128 + 64 + (lane >> 4) * 16); }
  DI void publish(const rsrc_t& r, int tm, int lw, int lane) {
    f32x4 d[NTL], o; const int i2 = (lane & 15) >> 1;
    UNR for (int i = 0; i < NTL; i++) d[i] = mfma(f[i][1], e1, mfma(f[i][0], e0, f32x4{0, 0, 0, 0}));
    UNR for (int k = 0; k < 4; k++) { o[k] = d[0][k]; UNR for (int i = 1; i < NTL; i++) o[k] = i2 == i ? d[i][k] : o[k]; }
    if (i2 < NTL) st_dev(__builtin_bit_cast(u32x4, o), r, i2 * 2048 + (lane & 1) * 64 + (lane >> 4) * 16, lw * NTL * 2048 + tm * 128);
  }
  u32x4 v[2 * NTL];
  DI void fetch(const char* base, int lw, int lane) { UNR for (int i = 0; i < 2 * NTL; i++) gld_dev(v[i], base + lw * NTL * 2048 + i * 1024, lane * 16); }
  DI void finish(const rsrc_t& r, int lw, int lane, unsigned tab) {
    pin(v); unsigned mx = 0;
    UNR for (int i = 0; i < 2 * NTL; i++) mx = max(mx, umax4(v[i]));
    if (__any(mx == 0xffffffffu)) poll(v, [&](int i) { return ld_dev(r, lane * 16, lw * NTL * 2048 + i * 1024); });
    UNR for (int i = 0; i < NTL; i++) { const f32x4 a = asf(v[2 * i]) + asf(v[2 * i + 1]);
      float t = sw16_add(sw32_add(a[0], a[2]), sw32_add(a[1], a[3])); t += dpp<0x128>(t);
      ldsw<float>(tab + (lw * NTL + i) * 128 + (lane & 7) * 16 + (2 * (lane >> 5) + ((lane >> 4) & 1)) * 4, t); }
  }
};
template <int NPART> struct Stats {
  u32x4 v[NPART / 4];
  DI void fetch(int i, const char* st, int tm, int lw, int lane) { gld_dev(v[i], st + (size_t)(tm * NPART + i) * 1024 + lw * 256, (lane >> 4) * (NPART * 256) + (lane & 15) * 16); }
  DI void check(const char* ws, unsigned st, int tm, int lw, int lane) { pin(v); unsigned mx = 0;
    UNR for (int i = 0; i < NPART / 4; i++) mx = max(mx, umax4(v[i]));
    if (__any(mx == 0xffffffffu)) poll(v, [&](int i) { return ld_dev(mkrsrc(ws, st + (tm * NPART + i) * 1024 + lw * 256), (lane >> 4) * (NPART * 256) + (lane & 15) * 16, 0); }); }
  DI void finish(int lw, int lane, unsigned tab) { pin(v); f32x4 s = asf(v[0]); float q0 = 0, q1 = 0;
    UNR for (int i = 1; i < NPART / 4; i++) s += asf(v[i]);
    float t = sw32_add(s[0], s[2]); t = sw16_add(t, t) * (1.f / NPART);
    const auto mm = __builtin_amdgcn_permlane32_swap(__float_as_uint(t), __float_as_uint(t), false, false);
    UNR for (int i = 0; i < NPART / 4; i++) { const float d0 = __uint_as_float(v[i][0]) - __uint_as_float(mm[0]), d1 = __uint_as_float(v[i][2]) - __uint_as_float(mm[1]); q0 = __builtin_fmaf(d0, d0, q0); q1 = __builtin_fmaf(d1, d1, q1); }
    float q = sw32_add(__builtin_fmaf((float)(D_ / NPART), q0, s[1]), __builtin_fmaf((float)(D_ / NPART), q1, s[3])); q = sw16_add(q, q);
    const float rstd = __builtin_amdgcn_rsqf(q * (1.f / D_) + 1e-5f);
    ldsw<float>(tab + ((lane >> 4) & 1) * 512 + (lw * 32 + (lane & 15) * 2 + (lane >> 5)) * 4, (lane & 16) ? -t * rstd : rstd); }
};

template <bool MAX> DI float fold4(float a, float b, float c, float d) {
  auto op = [](unsigned x, unsigned y) { return MAX ? __builtin_elementwise_maximum(__uint_as_float(x), __uint_as_float(y)) : __uint_as_float(x) + __uint_as_float(y); };
  const auto r0 = __builtin_amdgcn_permlane32_swap(__float_as_uint(a), __float_as_uint(b), false, false), r1 = __builtin_amdgcn_permlane32_swap(__float_as_uint(c), __float_as_uint(d), false, false);
  const auto r2 = __builtin_amdgcn_permlane16_swap(__float_as_uint(op(r0[0], r0[1])), __float_as_uint(op(r1[0], r1[1])), false, false);
  return op(r2[0], r2[1]);
}
DI void spread4(float t, float& a, float& b, float& c, float& d) {
  const auto r2 = __builtin_amdgcn_permlane16_swap(__float_as_uint(t), __float_as_uint(t), false, false);
  const auto r0 = __builtin_amdgcn_permlane32_swap(r2[0], r2[0], false, false), r1 = __builtin_amdgcn_permlane32_swap(r2[1], r2[1], false, false);
  a = __uint_as_float(r0[0]); b = __uint_as_float(r0[1]); c = __uint_as_float(r1[0]); d = __uint_as_float(r1[1]);
}
DI void feat2(const bf16x8 (&wf)[2], bf16x8 x0, bf16x8 x1, bf16x8& fp0, bf16x8& fn0, bf16x8& fp1, bf16x8& fn1) {
  const f32x4 zero = {0, 0, 0, 0}; f32x4 p[2][2] = {{mfma(wf[0], x0, zero), mfma(wf[1], x0, zero)}, {mfma(wf[0], x1, zero), mfma(wf[1], x1, zero)}};
  float mx[2], nx[2], sp[2], sn[2]; f32x4 a[2][2], b[2][2];
  UNR for (int i = 0; i < 2; i++) { const f32x4 u = p[i][0], v = p[i][1];
    mx[i] = fmax3(fmax3(fmax3(u[0], u[1], u[2]), u[3], v[0]), fmax3(v[1], v[2], v[3]), v[3]); nx[i] = fmax3(fmax3(fmax3(-u[0], -u[1], -u[2]), -u[3], -v[0]), fmax3(-v[1], -v[2], -v[3]), -v[3]); }
  spread4(fold4<true>(mx[0], mx[1], nx[0], nx[1]), mx[0], mx[1], nx[0], nx[1]);
  UNR for (int i = 0; i < 2; i++) UNR for (int h = 0; h < 2; h++) UNR for (int j = 0; j < 4; j += 2) { const f32x2 t = {p[i][h][j], p[i][h][j + 1]};
    f32x2 d = t - f32x2{mx[i], mx[i]}, e = -t - f32x2{nx[i], nx[i]}; asm("" : "+v"(d), "+v"(e));
    a[i][h][j] = __builtin_amdgcn_exp2f(d[0]); a[i][h][j + 1] = __builtin_amdgcn_exp2f(d[1]); b[i][h][j] = __builtin_amdgcn_exp2f(e[0]); b[i][h][j + 1] = __builtin_amdgcn_exp2f(e[1]); }
  UNR for (int i = 0; i < 2; i++) { sp[i] = hsum4(a[i][0] + a[i][1]); sn[i] = hsum4(b[i][0] + b[i][1]); }
  spread4(__builtin_amdgcn_rcpf(fold4<false>(sp[0], sp[1], sn[0], sn[1])), sp[0], sp[1], sn[0], sn[1]);
  fp0 = pack8(a[0][0] * sp[0], a[0][1] * sp[0]); fn0 = pack8(b[0][0] * sn[0], b[0][1] * sn[0]); fp1 = pack8(a[1][0] * sp[1], a[1][1] * sp[1]); fn1 = pack8(b[1][0] * sn[1], b[1][1] * sn[1]);
}

constexpr float L2E = 1.4426950408889634f;
DI void kv_prep(const f32x4 (&k)[2][2], const f32x4 (&vv)[2][2], const bf16x8 (&wf)[2], int hf, int w, int lane, unsigned lds0) {
  const int g = lane >> 4, r = lane & 15, row0 = (w >> 1) * 64 + hf * 32; const unsigned uva = lds0 + L_UVT + (w & 1) * 256;
  RowSt st[2]; float u[2], v[2]; f32x4 rs[2], nm[2];
  UNR for (int m = 0; m < 2; m++) st[m].set(lds0 + L_STT + (row0 + m * 16 + r) * 4, L2E);
  const f32x4 u0 = ldsr<f32x4>(uva + 512 + g * 16), v0 = ldsr<f32x4>(uva + 576 + g * 16) * L2E, u1 = ldsr<f32x4>(uva + 640 + g * 16), v1 = ldsr<f32x4>(uva + 704 + g * 16) * L2E;
  UNR for (int c = 0; c < 2; c++) { u[c] = ldsr<float>(uva + 1024 + c * 128 + r * 4); v[c] = ldsr<float>(uva + 1088 + c * 128 + r * 4); asm("" : "+v"(u[c])); asm("" : "+v"(v[c])); }
  UNR for (int m = 0; m < 2; m++) { const unsigned a = lds0 + L_STT + (row0 + m * 16 + 4 * g) * 4; rs[m] = ldsr<f32x4>(a); nm[m] = ldsr<f32x4>(a + 512); }
  bf16x8 a, b, c, d; feat2(wf, pack8(lnf(k[0][0], st[0], u0, v0), lnf(k[0][1], st[0], u1, v1)), pack8(lnf(k[1][0], st[1], u0, v0), lnf(k[1][1], st[1], u1, v1)), a, b, c, d);
  UNR for (int cc = 0; cc < 2; cc++) ldsw<bf16x8>(lds0 + L_VF + w * 4096 + hf * 2048 + cc * 1024 + lane * 16,
      pack8(fma4(vv[0][cc], rs[0], fma4(nm[0], spl(u[cc]), spl(v[cc]))), fma4(vv[1][cc], rs[1], fma4(nm[1], spl(u[cc]), spl(v[cc])))));
  const unsigned trw = lds0 + L_TR + w * 8192 + hf * 2048 + r * 64 + ((g ^ ((r >> 1) & 3)) * 16);
  ldsw<bf16x8>(trw, a); ldsw<bf16x8>(trw + 4096, b); ldsw<bf16x8>(trw + 1024, c); ldsw<bf16x8>(trw + 1024 + 4096, d);
}

DI void touch_code(int lane, unsigned ldsdummy) {
  const char* pc; unsigned len, back;
  asm volatile("s_getpc_b64 %0\n.Lhere%=:\n\ts_mov_b32 %2, .Lhere%=-.Lcode_begin\n\ts_mov_b32 %1, .Lcode_end-.Lcode_begin" : "=s"(pc), "=s"(len), "=s"(back));
  pc -= back;
  const unsigned o = ((blockIdx.x >> 3) * 16 + (lane & 15)) * 128;
  touch(pc, min(o, len), ldsdummy);
}
struct Args { const u16 *x, *wqkv, *wphi, *wo, *w1, *b1, *w2, *b2, *c, *wm; u16* out; char* ws; };

DI int gxcol(int lane) { return 2 * (lane & 31) + (lane >> 5); }
template <int N> DI void gx_gather(const char* ws, unsigned off, int lane, unsigned tag, float (&t)[N]) {
  const rsrc_t r = mkrsrc(ws, WS_GX + off); u32x4 v[N];
  _Pragma("nounroll") for (int spin = 0; spin < SPIN_CAP; spin++) { unsigned bad = 0;
    UNR for (int i = 0; i < N; i++) v[i] = ld_dev(r, lane * 16, i * 1024);
    UNR for (int i = 0; i < N; i++) bad |= (v[i][1] ^ tag) | (v[i][3] ^ tag);
    if (!__any(bad != 0)) break;
    asm volatile("s_sleep 2" ::: "memory");
  }
  UNR for (int i = 0; i < N; i++) t[i] = sw32_add(__uint_as_float(v[i][0]), __uint_as_float(v[i][2]));
}
DI float sum8(const float (&p)[8]) { return fadd(fadd(fadd(p[0], p[1]), fadd(p[2], p[3])), fadd(fadd(p[4], p[5]), fadd(p[6], p[7]))); }

DI u64 phase0(const Args& A, int w, unsigned lds0, unsigned& ts) {
  char* ws = A.ws; const Blk B = blk0(), B1 = blk1(); const f32x4 zero = {0, 0, 0, 0};
  const int lane = threadIdx.x & 63, r8 = lane >> 3, g = lane >> 4, r = lane & 15;
  u64 ep; asm volatile("s_load_dwordx2 %0, %1, 0x0" : "=s"(ep) : "s"(ws + WS_EPOCH) : "memory");
  unsigned cw; gld4(cw, A.c, B.tm * 128 + (lane & 31) * 4);
  { const char* wb = (const char*)A.wm + (size_t)(B.tm * 8 + w) * 16 * MODN + B.tn * 128; const unsigned wv = r8 * MODN * 2 + (((lane & 7) ^ (((r8 >> 1) & 3) << 1)) * 16);
    glds16(wb + 2 * D_, wv, lds0 + L_UN + w * 1024); glds16(wb, wv, lds0 + L_UN + 8192 + w * 1024); }
  bar();
  const unsigned rv0 = r8 * 2048 + (((lane & 7) ^ (r8 >> 1)) * 16);
  UNR for (int i = 0; i < 2; i++) glds16((const char*)A.x + ((size_t)(B.tm * 128 + w * 16 + i * 8) * D_ + B.tn * 64) * 2, i ? rv0 ^ 64 : rv0, lds0 + L_XT + (w * 2 + i) * 1024);
  { const int e = min(w * 24 + lane, 191), row = e / 6, l = e - row * 6;
    if (lane < 24) touch((const char*)A.wqkv + (size_t)B.j * 32 * 6144 + B.xq * 256, row * 6144 + (l >> 1) * 2048 + (l & 1) * 128, lds0 + L_DUM); }
  unsigned pa = lds0 + L_UN + (w >> 2) * 8192 + ptile(w & 3, lane); pin(pa);
  waitvm<5>(); pin(cw);
  asm volatile("s_waitcnt lgkmcnt(0)" : "+s"(ep) :: "memory");
  const u64 tag = ep + 0x9E3779B97F4A7C15ull;
  if (w == 7) ldsw<unsigned>(lds0 + L_SIL2 + fragpos(2 * (lane & 31)), silu2(cw));
  waitvm<3>(); lbar();
  STAMP(1);
  { const f32x4 d = mfma(wfrag(pa + 4096, 2048), ldsr<bf16x8>(lds0 + L_SIL2 + 64 + g * 16), mfma(wfrag(pa, 2048), ldsr<bf16x8>(lds0 + L_SIL2 + g * 16), zero));
    const bool o = r & 1; const u32x4 gr = {__float_as_uint(o ? d[2] : d[0]), (unsigned)tag, __float_as_uint(o ? d[3] : d[1]), (unsigned)tag};
    if (r < 2) st_dev(gr, mkrsrc(ws, WS_GX + (((w >> 2) * 16 + B.tn) * 16 + B.tm) * 512), g * 32 + r * 16, (w & 3) * 128); }
  asm volatile("" ::: "memory");
  if (w < 3) {
    const int l = w * 8 + r8;
    if (l < 20) st_dev(u32x4{~0u, ~0u, ~0u, ~0u}, mkrsrc(ws, WS_MODP), (l < 8 ? (((l >> 1) * 16 + B.tn) * 16 + B.tm) * 256 + (l & 1) * 128 : 4 * 65536 + (B1.tn * 12 + l - 8) * 2048 + B1.tm * 128) + (lane & 7) * 16, 0); }
  if (w == 7) { const Blk C = blk(); if (lane < 7) touch_any(flagp(ws, lane < 2 || lane == 6 ? B1.tm * 16 + B1.tn : C.tm * 16 + C.tn, lane, 0), lds0 + L_DUM); }
  if (w < 3 || w == 7) waitvm<3>(); else waitvm<2>();
  f32x4 xv[4]; f32x2 stat;
  UNR for (int cc = 0; cc < 4; cc++) { const u32x2 t = ldsr<u32x2>(lds0 + L_XT + (w * 16 + r) * 128 + (((cc * 2 + (g >> 1)) ^ (r >> 1)) * 16) + (g & 1) * 8); xv[cc] = unp(t[0], t[1]); }
  { const float mean = xg_sum(hsum4((xv[0] + xv[1]) + (xv[2] + xv[3]))) * (1.f / 64); f32x4 q = zero;
    UNR for (int cc = 0; cc < 4; cc++) { const f32x4 d = xv[cc] - mean; q = fma4(d, d, q); }
    stat = f32x2{mean, xg_sum(hsum4(q))}; pin(stat); pin(xv); }
  waitvm<0>();
  STAMP(2);
  { float t[1]; gx_gather<1>(ws, B.tn * 8192 + w * 1024, lane, (unsigned)tag, t); ldsw<float>(lds0 + L_RED + w * 256 + gxcol(lane) * 4, t[0]); }
  STAMP(3);
  lbar();
  float p[8]; UNR for (int i = 0; i < 8; i++) p[i] = ldsr<float>(lds0 + L_RED + i * 256 + lane * 4);
  ldsw<float>(lds0 + L_SROW + w * 256 + lane * 4, rbf(1.f + rbf(sum8(p))));
  asm volatile("" ::: "memory");
  const rsrc_t ry = outrsrc(A.out, (B.tm * 16 + B.tn) * 16384);
  UNR for (int ks = 0; ks < 2; ks++)
    st_dev(pack8u(xv[2 * ks] * ldsr<f32x4>(lds0 + L_SROW + w * 256 + ks * 128 + g * 16), xv[2 * ks + 1] * ldsr<f32x4>(lds0 + L_SROW + w * 256 + ks * 128 + 64 + g * 16)), ry, lane * 16, w * 2048 + ks * 1024);
  if (g == 0) st_dev8(__builtin_bit_cast(u32x2, stat), mkrsrc(ws, WS_ST1 + (size_t)(B.tm * 16 + B.tn) * 1024), r * 8, w * 128);
  asm volatile("" ::: "memory");
  STAMP(4);
  return tag;
}

DI void loader_role(const Args& A, int lw, unsigned lds0, u64 tag, unsigned& ts) {
  char* ws = A.ws; const Blk B2 = blk(), B1 = blk1(), B0 = blk0(); const f32x4 zero = {0, 0, 0, 0}; const u32x4 mark = {~0u, ~0u, ~0u, ~0u};
  int lane = threadIdx.x & 63; pin(lane);
  Loader<2> G2; Sync Y; auto none = [](int) { return 0; };
  {
    const Blk& B = B1; const int g = lane >> 4, r = lane & 15, q = r >> 2, p = lane & 3, r8 = lane >> 3;
    Loader<0> G; G.init(A.out, 16, A.wqkv, 0, B.tn * 64, 0, B.rot, 0, B.tm, lw, lane, lds0);
    G.issueB(0); G.issueB(1);
    const rsrc_t ruv = mkrsrc(ws, WS_UV1 + B.tn * 12 * 2048);
    UV<0, 3> U; Stats<16> S; U.prep(lw, lane, lds0);
    waitvm<12>();
    STAMP(5);
    Y.run(ws, 0, B0, B, lw, lane, tag, G.first_need(3, 0), [&]() {});
    STAMP(11);
    const int t3 = 6 + (B.tm >> 1);
    G.issueA(0); G.issueA(1); waitvm<4>(); bar();
    U.read(); U.reade(lane, lds0); G.issue(2); Y.rest(lane); waitvm<10>(); lbar();
    G.run3<7>(0, none,
      [&](int t) { int e = 0;
        if (t == 1) { UNR for (int c = 0; c < 2; c++) st_dev(mark, mkrsrc(ws, WS_KV1 + (size_t)(B.tn * 4 + lw) * 32768), lane * 16, B.tm * 2048 + c * 1024); e = 2; }
        if (t == 2) { UNR for (int c = 0; c < 2; c++) st_dev(mark, mkrsrc(ws, WS_KV2 + (size_t)(B.tn * 16 + B.tm) * 8192), lane * 16, lw * 2048 + c * 1024); e = 2; }
        if (t == 3) { if (r8 < 5) st_dev(mark, mkrsrc(ws, WS_ZP), (r8 == 0 ? (B.tn * 4 + lw) * 2048 : (1 << 17) + (B.tn * 16 + lw * 4 + r8 - 1) * 2048) + (lane & 7) * 16, B.tm * 128);
          if (lane < 32) st_dev(mark, mkrsrc(ws, WS_ST2 + blockIdx.x * 2048), lane * 16, lw * 512); e = 2; }
        if (t == 4 || t == 5) { UNR for (int i = 0; i < 2; i++) S.fetch(2 * t - 8 + i, ws + WS_ST1, B.tm, lw, lane); e = 2; }
        if (t == 6) { prio(1); U.publish(ruv, B.tm, lw, lane); prio(0); e = 1; }
        return e; },
      [&](int t) { int e = 0;
        if (t == t3 && !(EV & 2)) { touch(opq((const char*)A.w1) + (size_t)(blockIdx.x * 4 + lw) * 7168, min(lane, 55) * 128, lds0 + L_DUM); touch(opq((const char*)A.wo) + (size_t)(blockIdx.x * 4 + lw) * 2048, (lane & 15) * 128, lds0 + L_DUM); e += 2; }
        if (t == 11 && lw == 0) { touch_code(lane, lds0 + L_DUM); e += 1; }
        return e; },
      [&]() { U.fetch(opq(ws) + ruv.base, lw, lane); glds16((const char*)A.wphi + B.tn * 4096 + lw * 1024, lane * 16, lds0 + L_WPHI + lw * 1024); });
    {
      const char* wb = (const char*)A.wm + (size_t)B0.tm * 128 * MODN + (lw + 2) * 2 * D_ + B0.tn * 128; const unsigned wv = r8 * MODN * 2 + (((lane & 7) ^ (((r8 >> 1) & 3) << 1)) * 16);
      UNR for (int i = 0; i < 8; i++) glds16(wb + i * 16 * MODN, wv, lds0 + L_REST + lw * 8192 + i * 1024); }
    prio(1);
    S.finish(lw, lane, lds0 + L_STT);
    waitvm<8>(); U.finish(ruv, lw, lane, lds0 + L_UVT);
    G2.init(ws + WS_O, 16, A.wo, 0, B2.tn * 64, 0, B2.rot, 0, B2.tm, lw, lane, lds0);
    prio(0);
    { const unsigned rv0 = r8 * 2048 + (((lane & 7) ^ (r8 >> 1)) * 16);
      UNR for (int i = 0; i < 4; i++) glds16((const char*)A.x + ((size_t)(B2.tm * 128 + (lw * 4 + i) * 8) * D_ + B2.tn * 64) * 2, (i & 1) ? rv0 ^ 64 : rv0, lds0 + L_RES + (lw * 4 + i) * 1024); }
    lbar();
    { bf16x8 wf[2]; f32x4 k[2][2], vv[2][2];
      UNR for (int m = 0; m < 2; m++) UNR for (int c = 0; c < 2; c++) { k[m][c] = ldsr<f32x4>(lds0 + L_TR + lw * 8192 + c * 4096 + m * 1024 + lane * 16); vv[m][c] = ldsr<f32x4>(lds0 + L_RV + lw * 4096 + (m * 2 + c) * 1024 + lane * 16); }
      UNR for (int c = 0; c < 2; c++) wf[c] = wfrag(lds0 + L_WPHI + (lw & 1) * 2048 + (4 * g + q) * 64 + c * 32 + p * 8, 1024);
      kv_prep(k, vv, wf, 0, lw, lane, lds0); }
    if (AN && lw == 0 && lane < 16) __hip_atomic_store(flagp(ws, lane * 16 + B.tn, 6, B.tm), tag, __ATOMIC_RELAXED, __HIP_MEMORY_SCOPE_AGENT);
    lbar();
    prio(1);
    const int hd = lw >> 1, sg = lw & 1;
    const u32x4 one4 = {0x3f803f80u, 0x3f803f80u, 0x3f803f80u, 0x3f803f80u}; const bf16x8 ones = __builtin_bit_cast(bf16x8, one4);
    const unsigned tra = lds0 + L_TR + hd * 8192 + sg * 4096 + (4 * g + q) * 64 + ((p ^ (((4 * g + q) >> 1) & 3)) * 16), vfa = lds0 + L_VF + hd * 4096 + lane * 16;
    f32x4 kv[2][2] = {{zero, zero}, {zero, zero}}, z[2] = {zero, zero};
    UNR for (int k = 0; k < 4; k++) { const bf16x8 v0 = ldsr<bf16x8>(vfa + (k >> 1) * 8192 + (k & 1) * 2048), v1 = ldsr<bf16x8>(vfa + (k >> 1) * 8192 + (k & 1) * 2048 + 1024);
      UNR for (int ft = 0; ft < 2; ft++) { const bf16x8 af = wfrag(tra + (k >> 1) * 16384 + (k & 1) * 2048 + ft * 8, 1024);
        kv[ft][0] = mfma(af, v0, kv[ft][0]); kv[ft][1] = mfma(af, v1, kv[ft][1]); z[ft] = mfma(af, ones, z[ft]); } }
    const rsrc_t rk = mkrsrc(ws, WS_KV1 + (size_t)(B.tn * 4 + lw) * 32768), rz = mkrsrc(ws, WS_ZP + (size_t)(B.tn * 4 + lw) * 2048), r2 = mkrsrc(ws, WS_KV2 + (size_t)B.tn * 131072);
    UNR for (int c = 0; c < 2; c++) st_dev(pack8u(kv[0][c], kv[1][c]), rk, lane * 16, B.tm * 2048 + c * 1024);
    { const bool o = lane & 1; const f32x4 zs = {o ? z[1][0] : z[0][0], o ? z[1][1] : z[0][1], o ? z[1][2] : z[0][2], o ? z[1][3] : z[0][3]};
      if ((lane & 14) == 0) st_dev(__builtin_bit_cast(u32x4, zs), rz, (lane & 1) * 64 + g * 16, B.tm * 128); }
    STAMP(16);
    waitvm<3>();
    { const f32x4 d = gemv64(lds0 + L_REST + lw * 8192, ldsr<bf16x8>(lds0 + L_SIL2 + g * 16), ldsr<bf16x8>(lds0 + L_SIL2 + 64 + g * 16), lane);
      if (r < 4) st_dev(__builtin_bit_cast(u32x4, d), mkrsrc(ws, WS_MODP + (size_t)((lw * 16 + B0.tn) * 16 + B0.tm) * 256), r * 64 + g * 16, 0); }
    if (AN) wait_flags(flagp(ws, B.tm * 16 + B.tn, 6, 0), lane, 16, tag, ~0ull);
    waitvm<1>();
    u32x4 h[2], zz[2]; poll(h, [&](int i) { return ld_dev(rk, (lane >> 3) * 2048 + (lane & 7) * 16, B.tm * 128 + i * 16384); });
    UNR for (int i = 0; i < 2; i++) gld_dev(zz[i], ws + rz.base + i * 64, r * 128 + g * 16);
    pin(h);
    { float e[4], o[4];
      UNR for (int k = 0; k < 4; k++) { e[k] = fadd(blo(h[0][k]), blo(h[1][k])); o[k] = fadd(bhi(h[0][k]), bhi(h[1][k])); }
      float te = fold4<false>(e[0], e[1], e[2], e[3]), to = fold4<false>(o[0], o[1], o[2], o[3]); te += dpp<0x128>(te); to += dpp<0x128>(to);
      spread4(__uint_as_float(cvtpk(te, to)), e[0], e[1], e[2], e[3]);
      const u32x4 sum = {__float_as_uint(e[0]), __float_as_uint(e[1]), __float_as_uint(e[2]), __float_as_uint(e[3])};
      UNR for (int i = 0; i < 2; i++) st_dev(sum, r2, (lane >> 3) * 8192 + (lane & 7) * 16, i * 65536 + lw * 2048 + B.tm * 128); }
    STAMP(17);
    waitvm<2>(); pin(zz);
    if (__any(max(umax4(zz[0]), umax4(zz[1])) == 0xffffffffu)) poll(zz, [&](int i) { return ld_dev(rz, r * 128 + g * 16, i * 64); });
    ldsw<bf16x8>(lds0 + L_KX + lw * 1024 + lane * 16, pack8(row16_sum4(asf(zz[0])), row16_sum4(asf(zz[1]))));
    waitvm<0>();
    lbar();
    prio(0);
  }
  const Blk& B = B2; const int odd = B.tn & 1, q4 = (B.tm >> 3) + 2 * ((B.tm >> 2) & 1);
  Loader<1> G3;
  {
    UNR for (int s = 0; s < 4; s++) G2.issueB(s);
    Y.run(ws, 2, B1, B, lw, lane, tag, G2.first_need(4, 0), [&]() {});
    STAMP(21);
    G3.init(A.out, 16, A.w1, 0, B.tn * 224 + odd * 32, B.tn * 224 + (odd ? 0 : 192), q4 * 4, B.tn, B.tm, lw, lane, lds0);
    G2.run4<7, false>([&]() { Y.rest(lane); }, [&]() { G3.issueBk(0, B.tn); });
    if (blockIdx.x == 0 && lw == 0 && lane == 0) __hip_atomic_store((u64*)(ws + WS_EPOCH), tag, __ATOMIC_RELAXED, __HIP_MEMORY_SCOPE_AGENT);
  }
  Loader<3> G4; const int own4 = (7 * (B.tn & 7) + 1) >> 1;
  {
    const rsrc_t ruv = mkrsrc(ws, WS_UV2 + B.tn * 16 * 2048);
    UV<1, 4> U; Stats<32> S; const int ku = q4 * 4 + (B.tm & 3), tu = ku == B.tn ? 0 : 1 + (B.tm & 3) - (B.tn >= q4 * 4 && B.tn < ku), t3 = 6 + (B.j & 7), t4 = W2T == 1 ? 7 + ((B.j + 16 * (B.tm >> 3)) & 31) * 7 / 32 : 10 + (B.j & 3);
    U.prep(lw, lane, lds0 + ((tu + 2) % 3) * Lay<1>::SBB);
    bar();
    G3.issueB(1);
    waitvm<7>();
    Y.run(ws, 3, B, B, lw, lane, tag, G3.first_need(2, 0), [&]() { U.read(); G3.issueB(2); });
    STAMP(27);
    G3.issueA(1); G3.issueA(2); Y.rest(lane); waitvm<4>(); bar();
    G3.run3<8>(0,
      [&](int t) { if (t < 5 && t == tu) U.read(); if (t == 5) U.reade(lane, lds0); },
      [&](int t) { int e = 0;
        if (t <= 3) { UNR for (int i = 3 * t - 3; i < 3 * t && i < 8; i++) { S.fetch(i, ws + WS_ST2, B.tm, lw, lane); e++; } }
        if (t == 5) { prio(1); U.publish(ruv, B.tm, lw, lane); prio(0); e = 1; }
        if (t == 6) { prio(1); S.check(ws, WS_ST2, B.tm, lw, lane); S.finish(lw, lane, lds0 + L_STT); prio(0); }
        return e; },
      [&](int t) { int e = 0;
        if (!XF && t >= 7 && t < 11) { st_dev(mark, mkrsrc(ws, xchoff(B.tm, B.tn, lw)), lane * 16, (t - 7) * 1024); e = 1; }
        if (W2T == 0 && t == t3) { touch(opq((const char*)A.w2) + (size_t)(blockIdx.x * 4 + lw) * 7168, min(lane, 55) * 128, lds0 + L_DUM); e += 1; }
        if (W2T && t == t4) {
          const char* p = opq((const char*)A.w2) + (size_t)((B.xq >> 1) * 1792 + B.j * 56 + lw * 14) * 2048 + (B.xq & 1) * 1024; const unsigned v = min(lane >> 3, 6) * 2048 + (lane & 7) * 128;
          touch(p, v, lds0 + L_DUM); touch(p + 7 * 2048, v, lds0 + L_DUM); e += 2; }
        if (t == 11 && lw == 0) { touch_code(lane, lds0 + L_DUM); e += 1; }
        return e; },
      [&]() { U.fetch(opq(ws) + ruv.base, lw, lane); });
    { const int r0 = (B.rot * 25) >> 4; G4.init(ws + WS_H1, 56, A.w2, (B.tn >> 3) * 28, (B.tn & 7) * 128, 0, r0 < own4 ? r0 : r0 + 3, own4, B.tm, lw, lane, lds0); }
    G4.issueBk(0, own4);
    prio(1);
    waitvm<4>(); U.finish(ruv, lw, lane, lds0 + L_UVT);
    prio(0);
    G4.issueBk(1, own4 + 1);
    lbar();
    G4.issueBk(2, own4 + 2);
  }
  {
    waitvm<0>(); bar();
    G4.issueB(3); bar(); G4.issueB(0); bar(); G4.issueB(1);
    Y.run(ws, 4, B, B, lw, lane, tag, G4.first_need(4, (B.tn >> 3) * 28), [&]() { G4.issueB(2); });
    STAMP(35);
    G4.run4<4, true>([&]() { Y.rest(lane); }, [&]() {
      glds16x4(opq(ws) + WS_X1 + (size_t)(B.tm * 16 + (B.tn & 7) * 2 + (lw & 1)) * 16384 + ((lw >> 1) * 4 + (B.tn >> 3) * 2) * 2048, lane * 16, lds0 + L_OT + lw * 4096); });
    asm volatile(".Lcode_end:");
  }
}

DI void compute_role(const Args& A, int w, unsigned lds0, u64 tag, unsigned& ts) {
  char* ws = A.ws; const Blk B = blk(); const f32x4 zero = {0, 0, 0, 0}; const u32x4 mark = {~0u, ~0u, ~0u, ~0u};
  const int wm = w >> 1, wn = w & 1; int lane = threadIdx.x & 63; pin(lane);
  {
    const Blk B = blk1(); const int g = lane >> 4, r = lane & 15, q = r >> 2, p = lane & 3;
    waitvm<0>();
    STAMP(5);
    bar();
    if (w < 2) { float t[8]; gx_gather<8>(ws, (w * 16 + B.rot) * 8192, lane, (unsigned)tag, t); const float v = rbf(sum8(t)); put_et(lds0, w, gxcol(lane), w ? v : rbf(1.f + v)); }
    bar();
    f32x4 acc[4][6];
    compute_loop<0>(acc, lds0, wm, wn, lane, ts);
    STAMP(13);
    lbar();
    STAMP(14);
    bf16x8 wf[2];
    UNR for (int c = 0; c < 2; c++) wf[c] = wfrag(lds0 + L_WPHI + wn * 2048 + (4 * g + q) * 64 + c * 32 + p * 8, 1024);
    { const f32x4 k[2][2] = {{acc[2][2], acc[2][3]}, {acc[3][2], acc[3][3]}}, vv[2][2] = {{acc[2][4], acc[2][5]}, {acc[3][4], acc[3][5]}}; kv_prep(k, vv, wf, 1, w, lane, lds0); }
    lbar();
    STAMP(15);
    asm volatile("" : "+v"(acc[0][0]), "+v"(acc[0][1]), "+v"(acc[1][0]), "+v"(acc[1][1]), "+v"(acc[2][0]), "+v"(acc[2][1]), "+v"(acc[3][0]), "+v"(acc[3][1]));
    bf16x8 qp[4], qn[4]; RowSt st[4]; const unsigned uva = lds0 + L_UVT + wn * 256;
    UNR for (int m = 0; m < 4; m++) st[m].set(lds0 + L_STT + (wm * 64 + m * 16 + r) * 4, L2E);
    { const f32x4 u0 = ldsr<f32x4>(uva + g * 16), v0 = ldsr<f32x4>(uva + 64 + g * 16) * L2E, u1 = ldsr<f32x4>(uva + 128 + g * 16), v1 = ldsr<f32x4>(uva + 192 + g * 16) * L2E;
      UNR for (int m = 0; m < 4; m += 2) feat2(wf, pack8(lnf(acc[m][0], st[m], u0, v0), lnf(acc[m][1], st[m], u1, v1)),
                                               pack8(lnf(acc[m + 1][0], st[m + 1], u0, v0), lnf(acc[m + 1][1], st[m + 1], u1, v1)), qp[m], qn[m], qp[m + 1], qn[m + 1]); }
    asm volatile("" : "+v"(qp[0]), "+v"(qp[1]), "+v"(qp[2]), "+v"(qp[3]), "+v"(qn[0]), "+v"(qn[1]), "+v"(qn[2]), "+v"(qn[3]) :: "memory");
    STAMP(16);
    bar();
    STAMP(17);
    const rsrc_t r2 = mkrsrc(ws, WS_KV2 + (size_t)(B.tn * 16 + B.tm) * 8192 + wn * 4096);
    u32x4 kk[4]; UNR for (int i = 0; i < 4; i++) kk[i] = ld_dev(r2, lane * 16, i * 1024);
    const bf16x8 z0 = ldsr<bf16x8>(lds0 + L_KX + wn * 2048 + lane * 16), z1 = ldsr<bf16x8>(lds0 + L_KX + wn * 2048 + 1024 + lane * 16);
    float rd[4];
    UNR for (int m = 0; m < 4; m++) rd[m] = __builtin_amdgcn_rcpf(mfma(z1, qn[m], mfma(z0, qp[m], zero))[0] + 1e-6f);
    asm volatile("" : "+v"(rd[0]), "+v"(rd[1]), "+v"(rd[2]), "+v"(rd[3]));
    if (__any(max(max(umax4(kk[0]), umax4(kk[1])), max(umax4(kk[2]), umax4(kk[3]))) == 0xffffffffu)) poll(kk, [&](int i) { return ld_dev(r2, lane * 16, i * 1024); });
    const rsrc_t ro = mkrsrc(ws, WS_O + (size_t)(B.tm * 16 + B.tn) * 16384);
    UNR for (int m = 0; m < 4; m++) {
      const f32x4 n0 = mfma(__builtin_bit_cast(bf16x8, kk[2]), qn[m], mfma(__builtin_bit_cast(bf16x8, kk[0]), qp[m], zero)), n1 = mfma(__builtin_bit_cast(bf16x8, kk[3]), qn[m], mfma(__builtin_bit_cast(bf16x8, kk[1]), qp[m], zero));
      st_dev(pack8u(n0 * rd[m], n1 * rd[m]), ro, lane * 16, (wm * 8 + wn) * 1024 + m * 2048);
    }
    STAMP(18);
    waitvm<0>(); bar();
    STAMP(19);
  }
  pin(lane);
  const int fp0 = B.tn * 7 + (B.tn & 1) + wn * 3, fph = (B.tn & 1) ? B.tn * 7 : B.tn * 7 + 6;
  u32x2 bb[7];
  {
    const int g = lane >> 4, r = lane & 15;
    UNR for (int n = 0; n < 7; n++) bb[n] = *(const u32x2*)(A.b1 + (n < 6 ? fp0 * 32 + n * 16 : fph * 32 + wn * 16) + 4 * g);
    const unsigned b2v = A.b2[((B.tn & 7) * 2 + wn) * 64 + lane];
    u32x4 gp[4]; UNR for (int i = 0; i < 4; i++) gp[i] = ld_dev(mkrsrc(ws, WS_MODP + (48 + (B.tn & 7) * 2 + wn) * 4096), lane * 16, i * 1024);
    { const float v = modvec(ws, WS_MODP + (size_t)((w == 0 ? 0 : w == 3 ? 1 : 2) * 16 + (w < 2 ? B.tn : ((B.tm >> 3) + 2 * ((B.tm >> 2) & 1)) * 4 + (B.tm & 3))) * 4096, lane), s = (w == 1 || w == 2) ? rbf(1.f + v) : v;
      if (w < 2) ldsw<float>(lds0 + L_GT + w * 256 + modcol(lane) * 4, s); else put_et(lds0, w - 2, modcol(lane), s); }
    pin(bb);
    f32x4 acc[4][2];
    bar();
    if (w < 2) ldsw<float>(lds0 + L_GB + w * 256 + modcol(lane) * 4, modvec(ws, WS_MODP + (size_t)(48 + (B.tn & 7) * 2 + w) * 4096, lane, gp, true));
    else ldsw<float>(lds0 + L_GB + w * 256 + lane * 4, blo(b2v));
    compute_loop<2>(acc, lds0, wm, wn, lane, ts);
    STAMP(23);
    bar();
    f32x4 gate[2], sc[2]; u32x2 xr[4][2];
    UNR for (int n = 0; n < 2; n++) { gate[n] = ldsr<f32x4>(lds0 + L_GT + wn * 128 + n * 64 + g * 16); sc[n] = ldsr<f32x4>(lds0 + L_GT + 256 + wn * 128 + n * 64 + g * 16); }
    UNR for (int m = 0; m < 4; m++) UNR for (int n = 0; n < 2; n++) xr[m][n] = ldsr<u32x2>(lds0 + L_RES + (wm * 64 + m * 16 + r) * 128 + (((wn * 4 + n * 2 + (g >> 1)) ^ (r >> 1)) * 16) + (g & 1) * 8);
    const size_t tb = (size_t)(B.tm * 16 + B.tn) * 16384; const rsrc_t ry = outrsrc(A.out, tb), rst = mkrsrc(ws, WS_ST2 + (size_t)(B.tm * 32 + B.tn * 2 + wn) * 1024);
    f32x4 v[4][2]; float mean[4], m2[4]; u32x4 o[4];
    UNR for (int m = 0; m < 4; m++) {
      o[m] = pack8u(fma4(gate[0], acc[m][0], unp(xr[m][0][0], xr[m][0][1])), fma4(gate[1], acc[m][1], unp(xr[m][1][0], xr[m][1][1]))); v[m][0] = unp(o[m][0], o[m][1]); v[m][1] = unp(o[m][2], o[m][3]);
      const u32x4 y = pack8u(v[m][0] * sc[0], v[m][1] * sc[1]);
      st_dev(y, ry, lane * 16, (wm * 8 + wn) * 1024 + m * 2048); ldsw<u32x4>(lds0 + (wm * 8 + wn) * 1024 + m * 2048 + lane * 16, y); }
    asm volatile("" ::: "memory");
    UNR for (int m = 0; m < 4; m++) mean[m] = hsum4(v[m][0] + v[m][1]);
    const float mf = fold4<false>(mean[0], mean[1], mean[2], mean[3]) * (1.f / 32); spread4(mf, mean[0], mean[1], mean[2], mean[3]);
    UNR for (int m = 0; m < 4; m++) { const f32x4 d0 = v[m][0] - mean[m], d1 = v[m][1] - mean[m]; m2[m] = hsum4(fma4(d0, d0, d1 * d1)); }
    st_dev8(__builtin_bit_cast(u32x2, f32x2{mf, fold4<false>(m2[0], m2[1], m2[2], m2[3])}), rst, (wm * 64 + ((g >> 1) + 2 * (g & 1)) * 16 + r) * 8, 0);
    STAMP(24);
    asm volatile("" ::: "memory");
    UNR for (int m = 0; m < 4; m++) st_dev(o[m], mkrsrc(ws, WS_X1 + tb), lane * 16, (wm * 8 + wn) * 1024 + m * 2048);
    waitvm<5>();
    STAMP(25);
  }
  pin(lane);
  {
    const int g = lane >> 4, r = lane & 15;
    f32x4 acc[4][7];
    compute_loop<1>(acc, lds0, wm, wn, lane, ts);
    STAMP(29);
    bar();
    STAMP(30);
    f32x4 u[7], v[7];
    UNR for (int n = 0; n < 7; n++) { const unsigned a = lds0 + L_UVT + (n < 6 ? wn * 6 + n : 12 + wn) * 128 + g * 16; u[n] = ldsr<f32x4>(a); v[n] = ldsr<f32x4>(a + 64) + unp(bb[n][0], bb[n][1]); }
    const rsrc_t rh = mkrsrc(ws, WS_H1 + (size_t)B.tm * 56 * 16384);
    RowSt st[4]; u32x2 h6[4];
    UNR for (int m = 0; m < 4; m++) st[m].set(lds0 + L_STT + (wm * 64 + m * 16 + r) * 4, 1.f);
    auto act = [&](int m, int n) { const f32x4 t = __builtin_elementwise_max(lnf(acc[m][n], st[m], u[n], v[n]), zero); return t * t; };
    UNR for (int n = 0; n < 6; n += 2) { const int fp = fp0 + (n >> 1);
      const int gi = wn * 3 + (n >> 1); const unsigned la = lds0 + (gi >> 1) * 16384 + (gi & 1) * 1024 + wm * 8192 + lane * 16;
      UNR for (int m = 0; m < 4; m++) { const u32x4 hh = pack8u(act(m, n), act(m, n + 1)); st_dev(hh, rh, lane * 16, (fp >> 1) * 16384 + (fp & 1) * 1024 + wm * 8192 + m * 2048); ldsw<u32x4>(la + m * 2048, hh); } }
    UNR for (int m = 0; m < 4; m++) { const f32x4 t = act(m, 6); h6[m] = u32x2{cvtpk(t[0], t[1]), cvtpk(t[2], t[3])}; }
    UNR for (int m = 0; m < 4; m += 2) { const auto a = __builtin_amdgcn_permlane16_swap(h6[m][0], h6[m + 1][0], false, false), b = __builtin_amdgcn_permlane16_swap(h6[m][1], h6[m + 1][1], false, false);
      st_dev(u32x4{a[0], b[0], a[1], b[1]}, rh, (g & 1) * 2048 + (g >> 1) * 256 + r * 16, (fph >> 1) * 16384 + (fph & 1) * 1024 + wm * 8192 + m * 2048 + wn * 512); }
    STAMP(31);
  }
  pin(lane);
  {
    const int g = lane >> 4, r = lane & 15, kh = B.tn >> 3, cg = (B.tn & 7) * 2 + wn;
    u32x4 xr[2][2]; f32x4 acc[4][4];
    compute_loop<3>(acc, lds0, wm, wn, lane, ts);
    STAMP(37);
    UNR for (int mm = 0; mm < 2; mm++) UNR for (int ks = 0; ks < 2; ks++) xr[mm][ks] = ldsr<u32x4>(lds0 + L_OT + w * 4096 + (mm * 2 + ks) * 1024 + lane * 16);
    const rsrc_t rx = mkrsrc(ws, xchoff(B.tm, B.tn, w)), rp = mkrsrc(ws, xchoff(B.tm, B.tn ^ 8, w));
    f32x4 keep[2][4], gate[4], bias[4];
    if (kh) { UNR for (int i = 0; i < 4; i++) st_dev(pack8u(acc[i >> 1][(i & 1) * 2], acc[i >> 1][(i & 1) * 2 + 1]), rx, lane * 16, i * 1024);
              UNR for (int n = 0; n < 4; n++) { keep[0][n] = acc[2][n]; keep[1][n] = acc[3][n]; } }
    else    { UNR for (int i = 0; i < 4; i++) st_dev(pack8u(acc[2 + (i >> 1)][(i & 1) * 2], acc[2 + (i >> 1)][(i & 1) * 2 + 1]), rx, lane * 16, i * 1024);
              UNR for (int n = 0; n < 4; n++) { keep[0][n] = acc[0][n]; keep[1][n] = acc[1][n]; } }
    UNR for (int n = 0; n < 4; n++) { gate[n] = ldsr<f32x4>(lds0 + L_GB + wn * 256 + n * 64 + g * 16); bias[n] = ldsr<f32x4>(lds0 + L_GB + 512 + wn * 256 + n * 64 + g * 16); }
    UNR for (int mm = 0; mm < 2; mm++) UNR for (int n = 0; n < 4; n++) keep[mm][n] = fma4(gate[n], keep[mm][n] + bias[n], unp(xr[mm][n >> 1][(n & 1) * 2], xr[mm][n >> 1][(n & 1) * 2 + 1]));
    pin(keep[0]); pin(keep[1]);
    waitvm<0>();
    u32x4 pp[4];
    if (XF) { if (lane == 0) __hip_atomic_store(flagp(ws, B.tm * 16 + (B.tn ^ 8), 5, w), tag, __ATOMIC_RELAXED, __HIP_MEMORY_SCOPE_AGENT);
      wait_flags(flagp(ws, B.tm * 16 + B.tn, 5, w), lane, 1, tag, 1);
      UNR for (int i = 0; i < 4; i++) pp[i] = ld_dev(rp, lane * 16, i * 1024); pin(pp); }
    else poll(pp, [&](int i) { return ld_dev(rp, lane * 16, i * 1024); });
    STAMP(38);
    const unsigned ot = lds0 + L_OT + w * 4096;
    UNR for (int mm = 0; mm < 2; mm++) UNR for (int n = 0; n < 4; n++) { const u32x4 t = pp[mm * 2 + (n >> 1)]; const f32x4 y = fma4(gate[n], unp(t[(n & 1) * 2], t[(n & 1) * 2 + 1]), keep[mm][n]);
      ldsw<u32x2>(ot + (mm * 16 + r) * 128 + (((n * 2 + (g >> 1)) ^ (r >> 1)) * 16) + (g & 1) * 8, u32x2{cvtpk(y[0], y[1]), cvtpk(y[2], y[3])}); }
    lwait();
    const int r8 = lane >> 3; const unsigned rv0 = r8 * 2048 + (((lane & 7) ^ (r8 >> 1)) * 16);
    char* op = (char*)A.out + ((size_t)(B.tm * 128 + wm * 64 + kh * 32) * D_ + cg * 64) * 2;
    UNR for (int i = 0; i < 4; i++) { const u32x4 t = ldsr<u32x4>(ot + i * 1024 + lane * 16);
      if (OS == 0) *(u32x4*)(op + i * 16384 + ((i & 1) ? rv0 ^ 64 : rv0)) = t;
      else __builtin_amdgcn_raw_buffer_store_b128(t, outrsrc(A.out, 0).r, (unsigned)(op - (char*)A.out) + i * 16384 + ((i & 1) ? rv0 ^ 64 : rv0), 0, OS); }
    STAMP(39);
  }
}

__global__ __launch_bounds__(512, 2) void k_block(Args A) {
  __shared__ __attribute__((aligned(1024))) char smem[L_TOT];
  const int w = __builtin_amdgcn_readfirstlane(threadIdx.x >> 6);
  const unsigned lds0 = (unsigned)(size_t)(LAS char*)smem;
  asm volatile(".Lcode_begin:");
  if (w == 3) touch_code(threadIdx.x & 63, lds0 + L_DUM);
  unsigned ts = 0; STAMP(0);
  const u64 tag = phase0(A, w, lds0, ts);
  if (w >= 4) loader_role(A, w - 4, lds0, tag, ts); else compute_role(A, w, lds0, tag, ts);
}

extern "C" void kernel_launch(void* const* d_in, const int* in_sizes, int n_in, void* d_out, int out_size, void* d_ws, size_t ws_size, hipStream_t stream) {
  Args A; A.x = (const u16*)d_in[0]; A.wqkv = (const u16*)d_in[1]; A.wphi = (const u16*)d_in[2]; A.wo = (const u16*)d_in[3]; A.w1 = (const u16*)d_in[5]; A.b1 = (const u16*)d_in[6];
  A.w2 = (const u16*)d_in[7]; A.b2 = (const u16*)d_in[8]; A.c = (const u16*)d_in[9]; A.wm = (const u16*)d_in[10]; A.out = (u16*)d_out; A.ws = (char*)d_ws;
  k_block<<<256, 512, 0, stream>>>(A);
}
```

```cpp
#include <hip/hip_runtime.h>
#include <hip/hip_bf16.h>
#include <type_traits>

typedef unsigned short u16;
typedef unsigned u32x4 __attribute__((ext_vector_type(4)));
typedef unsigned u32x2 __attribute__((ext_vector_type(2)));
typedef short s16x4 __attribute__((ext_vector_type(4)));
typedef __bf16 bf16x8 __attribute__((ext_vector_type(8)));
typedef __bf16 bf16x2 __attribute__((ext_vector_type(2)));
typedef float f32x2 __attribute__((ext_vector_type(2)));
typedef float f32x4 __attribute__((ext_vector_type(4)));
#define LAS __attribute__((address_space(3)))
#define DI __device__ __forceinline__
#define SB __builtin_amdgcn_sched_barrier(0)
#define UNR _Pragma("unroll")

constexpr int D_ = 1024, F_ = 3584, MODN = 6 * D_;
constexpr size_t WS_EPOCH = 0;
constexpr size_t WS_FLAG  = 4 << 20;
constexpr size_t WS_GX    = 1 << 18;
constexpr size_t WS_ST1   = 1 << 19;
constexpr size_t WS_MODP  = 3 << 19;
constexpr size_t WS_UV1   = WS_MODP + 4 * 65536;
constexpr size_t WS_ZP    = WS_UV1 + 16 * 12 * 2048;
constexpr size_t WS_UV2   = WS_ZP + (1 << 17);
constexpr size_t WS_ST2   = WS_UV2 + (1 << 19);
constexpr size_t WS_KV1   = 10 << 20;
constexpr size_t WS_KV2   = WS_KV1 + (2 << 20);
constexpr size_t WS_O     = 14 << 20;
constexpr size_t WS_X1    = 18 << 20;
constexpr size_t WS_H1    = 22 << 20;
constexpr size_t WS_STAMP = 40 << 20;
constexpr int SPIN_CAP = 1 << 14;
typedef unsigned long long u64;

#define STAMP(i) do { } while (0)

DI void glds16(const void* sbase, unsigned voff, unsigned ldsaddr) {
  asm volatile("s_mov_b32 m0, %2\n\ts_nop 0\n\tglobal_load_lds_dwordx4 %0, %1" :: "v"(voff), "s"(sbase), "s"(ldsaddr) : "memory");
}
template <int IMM> DI void glds16i(const void* sbase, unsigned voff, unsigned ldsbase) {
  asm volatile("s_add_i32 m0, %2, %3\n\ts_nop 0\n\tglobal_load_lds_dwordx4 %0, %1" :: "v"(voff), "s"(sbase), "s"(ldsbase), "i"(IMM) : "memory", "scc");
}
template <int I, int N, class F> DI void sfor(F f) { if constexpr (I < N) { f(std::integral_constant<int, I>()); sfor<I + 1, N>(f); } }
DI void glds16x4(const void* sbase, unsigned voff, unsigned ldsaddr) {
  asm volatile("s_mov_b32 m0, %2\n\ts_nop 0\n\tglobal_load_lds_dwordx4 %0, %1 sc1\n\tglobal_load_lds_dwordx4 %0, %1 offset:1024 sc1\n\t"
               "global_load_lds_dwordx4 %0, %1 offset:2048 sc1\n\tglobal_load_lds_dwordx4 %0, %1 offset:3072 sc1" :: "v"(voff), "s"(sbase), "s"(ldsaddr) : "memory");
}
DI void prio(int p) { if (p) __builtin_amdgcn_s_setprio(3); else __builtin_amdgcn_s_setprio(0); }
DI void touch(const void* sbase, unsigned voff, unsigned ldsdummy) {
  asm volatile("s_mov_b32 m0, %2\n\ts_nop 0\n\tglobal_load_lds_dword %0, %1" :: "v"(voff), "s"(sbase), "s"(ldsdummy) : "memory");
}
DI void touch_any(const void* p, unsigned ldsdummy) {
  asm volatile("s_mov_b32 m0, %1\n\ts_nop 0\n\tglobal_load_lds_dword %0, off" :: "v"(p), "s"(ldsdummy) : "memory");
}
DI void gld_dev(u32x4& r, const void* sb, unsigned vo) { asm volatile("global_load_dwordx4 %0, %1, %2 sc1" : "=v"(r) : "v"(vo), "s"(sb) : "memory"); }
DI void gld4(unsigned& r, const void* sb, unsigned vo) { asm volatile("global_load_dword %0, %1, %2" : "=v"(r) : "v"(vo), "s"(sb) : "memory"); }
template <class T> DI void pin(T& r) { asm volatile("" : "+v"(r)); }
template <class T, int N> DI void pin(T (&r)[N]) { UNR for (int i = 0; i < N; i++) pin(r[i]); }
template <class T> DI T opq(T v) { asm volatile("" : "+s"(v)); return v; }
DI f32x4 asf(u32x4 v) { return __builtin_bit_cast(f32x4, v); }
template <int N> DI void waitvm() { asm volatile("s_waitcnt vmcnt(%0)" :: "i"(N) : "memory"); }
template <int B, int LO = 0, int HI = 12> DI void waitx(int e) {
  if constexpr (LO == HI) waitvm<B + LO>();
  else if constexpr (LO == 0) { if (e == 0) waitvm<B>(); else waitx<B, 1, HI>(e); }
  else { constexpr int MID = (LO + HI + 1) / 2; if (e >= MID) waitx<B, MID, HI>(e); else waitx<B, LO, MID - 1>(e); }
}
DI void waitlgkm0() { __builtin_amdgcn_s_waitcnt(0xC07F); }
DI void lwait() { asm volatile("s_waitcnt lgkmcnt(0)" ::: "memory"); }
DI void bar() { asm volatile("s_barrier" ::: "memory"); }
DI void lbar() { asm volatile("s_waitcnt lgkmcnt(0)\n\ts_barrier" ::: "memory"); }
DI unsigned cvtpk(float a, float b) { f32x2 v = {a, b}; return __builtin_bit_cast(unsigned, __builtin_convertvector(v, bf16x2)); }
DI float rbf(float a) { return __uint_as_float(cvtpk(a, a) << 16); }
DI f32x4 rbf4(f32x4 s) { return f32x4{rbf(s[0]), rbf(s[1]), rbf(s[2]), rbf(s[3])}; }
DI float blo(unsigned u) { return __uint_as_float(u << 16); }
DI float bhi(unsigned u) { return __uint_as_float(u & 0xffff0000u); }
DI f32x4 unp(unsigned a, unsigned b) { return f32x4{blo(a), bhi(a), blo(b), bhi(b)}; }
DI u32x4 pack8u(const f32x4& a, const f32x4& b) { return u32x4{cvtpk(a[0], a[1]), cvtpk(a[2], a[3]), cvtpk(b[0], b[1]), cvtpk(b[2], b[3])}; }
DI bf16x8 pack8(const f32x4& a, const f32x4& b) { return __builtin_bit_cast(bf16x8, pack8u(a, b)); }
DI f32x4 mfma(bf16x8 a, bf16x8 b, f32x4 c) { return __builtin_amdgcn_mfma_f32_16x16x32_bf16(a, b, c, 0, 0, 0); }
DI s16x4 trd(unsigned a) { return __builtin_amdgcn_ds_read_tr16_b64_v4i16((LAS s16x4*)(size_t)a); }
DI bf16x8 cat(s16x4 lo, s16x4 hi) { return __builtin_bit_cast(bf16x8, __builtin_shufflevector(lo, hi, 0, 1, 2, 3, 4, 5, 6, 7)); }
template <class T> DI T ldsr(unsigned a) { return *(const LAS T*)(size_t)a; }
template <class T> DI void ldsw(unsigned a, T v) { *(LAS T*)(size_t)a = v; }
template <int C> DI float dpp(float x) { return __builtin_bit_cast(float, __builtin_amdgcn_update_dpp(0, __builtin_bit_cast(int, x), C, 0xf, 0xf, true)); }
DI float vmax(float a, float b) { float r; asm("v_max_f32_e32 %0, %1, %2" : "=v"(r) : "v"(a), "v"(b)); return r; }
DI float sw32_add(float a, float b) { auto r = __builtin_amdgcn_permlane32_swap(__float_as_uint(a), __float_as_uint(b), false, false); return __uint_as_float(r[0]) + __uint_as_float(r[1]); }
DI float sw16_add(float a, float b) { auto r = __builtin_amdgcn_permlane16_swap(__float_as_uint(a), __float_as_uint(b), false, false); return __uint_as_float(r[0]) + __uint_as_float(r[1]); }
DI float xg_sum(float a) { a = sw32_add(a, a); return sw16_add(a, a); }
DI float xg_max(float a) {
  auto r = __builtin_amdgcn_permlane32_swap(__float_as_uint(a), __float_as_uint(a), false, false); a = vmax(__uint_as_float(r[0]), __uint_as_float(r[1]));
  auto q = __builtin_amdgcn_permlane16_swap(__float_as_uint(a), __float_as_uint(a), false, false); return vmax(__uint_as_float(q[0]), __uint_as_float(q[1]));
}
DI float row16_sum(float a) { a += dpp<0x128>(a); a += dpp<0x124>(a); a += dpp<0x122>(a); a += dpp<0x121>(a); return a; }
DI f32x4 row16_sum4(f32x4 a) { return f32x4{row16_sum(a[0]), row16_sum(a[1]), row16_sum(a[2]), row16_sum(a[3])}; }
DI f32x4 spl(float a) { return f32x4{a, a, a, a}; }
DI f32x4 fma4(f32x4 a, f32x4 b, f32x4 c) { return __builtin_elementwise_fma(a, b, c); }
struct RowSt { float rs, nm; DI void set(unsigned a, float k) { rs = ldsr<float>(a) * k; nm = ldsr<float>(a + 512) * k; asm("" : "+v"(rs)); asm("" : "+v"(nm)); } };
DI f32x4 lnf(f32x4 acc, const RowSt& st, f32x4 u, f32x4 v) { return fma4(acc, spl(st.rs), fma4(u, spl(st.nm), v)); }
DI float fadd(float a, float b) { float r; asm("v_add_f32_e32 %0, %1, %2" : "=v"(r) : "v"(a), "v"(b)); return r; }
DI float hsum4(f32x4 a) { const f32x2 t = f32x2{a[0], a[1]} + f32x2{a[2], a[3]}; float r; asm("v_add_f32_e32 %0, %1, %2" : "=v"(r) : "v"(t[0]), "v"(t[1])); return r; }
struct rsrc_t { __amdgpu_buffer_rsrc_t r; unsigned base; };
DI rsrc_t mkrsrc(const char* ws, size_t off) { return rsrc_t{__builtin_amdgcn_make_buffer_rsrc((void*)ws, 0, 64 << 20, 0x00020000), (unsigned)off}; }
DI rsrc_t outrsrc(const void* out, size_t off) { return rsrc_t{__builtin_amdgcn_make_buffer_rsrc((void*)out, 0, 2048 * D_ * 2, 0x00020000), (unsigned)off}; }
DI unsigned xchoff(int tm, int tn, int w) { return WS_KV1 + (w >> 1) * (2 << 20) + (tn * 16 + tm) * 8192 + (w & 1) * 4096; }
DI u32x4 ld_dev(const rsrc_t& r, unsigned voff, unsigned soff) { return __builtin_amdgcn_raw_buffer_load_b128(r.r, voff, r.base + soff, 16); }
DI void st_dev(u32x4 v, const rsrc_t& r, unsigned voff, unsigned soff) { __builtin_amdgcn_raw_buffer_store_b128(v, r.r, voff + (r.base + soff), 0, 16); }
DI void st_dev8(u32x2 v, const rsrc_t& r, unsigned voff, unsigned soff) { __builtin_amdgcn_raw_buffer_store_b64(v, r.r, voff + (r.base + soff), 0, 16); }
DI unsigned umax4(u32x4 v) { return max(max(v[0], v[1]), max(v[2], v[3])); }
template <int N, class F> DI void poll(u32x4 (&v)[N], F ld) {
  _Pragma("nounroll") for (int spin = 0; spin < SPIN_CAP; spin++) { unsigned m = 0;
    UNR for (int i = 0; i < N; i++) v[i] = ld(i);
    UNR for (int i = 0; i < N; i++) m = max(m, umax4(v[i]));
    if (!__any(m == 0xffffffffu)) break;
    asm volatile("s_sleep 2" ::: "memory");
  }
}
#define EV 0
#define W2T 1
#define XF 1
#define AN 1
#define OS 16
#define AM 1
#define XA2 0
#define SLOW 2
#define SLOWC 2
#define SLOWT 99
#define XP 0
#define PW 1
DI float fmax3(float a, float b, float c) { return __builtin_elementwise_maximum(__builtin_elementwise_maximum(a, b), c); }
struct Blk { int tm, tn, rot, j, xq; };
DI Blk blk() { const int xcd = blockIdx.x & 7, j = blockIdx.x >> 3; Blk b; b.tm = (xcd >> 2) * 8 + (j >> 2); b.tn = (xcd & 3) * 4 + (j & 3); b.rot = (b.tm & 7) * 2 + (b.tm >> 3); b.j = j; b.xq = xcd & 3; return b; }
DI Blk blk1() { if (XA2) return blk(); const int xcd = blockIdx.x & 7, j = blockIdx.x >> 3; Blk b; b.tm = j >> 1; b.tn = xcd * 2 + (j & 1); b.rot = b.tm; b.j = j; b.xq = xcd; return b; }
DI Blk blk0() { const int xcd = blockIdx.x & 7, j = blockIdx.x >> 3; Blk b; b.tn = j >> 1; b.tm = xcd * 2 + (j & 1); b.rot = 0; b.j = j; b.xq = xcd; return b; }
DI unsigned ptile(int c, int lane) { const int row = 4 * (lane >> 4) + ((lane & 15) >> 2); return row * 128 + ((c ^ ((row >> 1) & 3)) * 32) + (lane & 3) * 8; }
DI unsigned htile(int c, int lane) { const int row = 4 * (lane >> 4) + ((lane & 15) >> 2); return row * 64 + ((c ^ ((row >> 2) & 1)) * 32) + (lane & 3) * 8; }
DI bf16x8 wfrag(unsigned a, unsigned ts) { return cat(trd(a), trd(a + ts)); }

DI unsigned silu2(unsigned cw) { const float c0 = blo(cw), c1 = bhi(cw); return cvtpk(c0 * __builtin_amdgcn_rcpf(1.f + __expf(-c0)), c1 * __builtin_amdgcn_rcpf(1.f + __expf(-c1))); }
DI unsigned fragpos(int k) { return (k >> 5) * 64 + ((k >> 2) & 3) * 16 + ((k & 3) + 4 * ((k >> 4) & 1)) * 2; }
DI f32x4 gemv64(unsigned a, bf16x8 s0, bf16x8 s1, int lane) { f32x4 d[4];
  UNR for (int cc = 0; cc < 4; cc++) d[cc] = mfma(wfrag(a + 4096 + ptile(cc, lane), 2048), s1, mfma(wfrag(a + ptile(cc, lane), 2048), s0, f32x4{0, 0, 0, 0}));
  const bool b0 = lane & 1, b1 = lane & 2; f32x4 o;
  UNR for (int k = 0; k < 4; k++) { const float lo = b0 ? d[1][k] : d[0][k], hi = b0 ? d[3][k] : d[2][k]; o[k] = b1 ? hi : lo; }
  return o;
}

template <int CFG> struct Cfg;
template <> struct Cfg<0> { static constexpr int NT = 6, NP = 3, HALF = 0, NB = 2, NS = 3, N = 3 * D_, KT = 16, B0 = 49152, BS0 = 0, OWN = 0; };
template <> struct Cfg<1> { static constexpr int NT = 7, NP = 3, HALF = 1, NB = 7, NS = 3, N = F_, KT = 16, B0 = 65536, BS0 = 2, OWN = 1; };
template <> struct Cfg<2> { static constexpr int NT = 2, NP = 1, HALF = 0, NB = 2, NS = 4, N = D_, KT = 16, B0 = 73728, BS0 = 0, OWN = 0; };
template <> struct Cfg<3> { static constexpr int NT = 4, NP = 2, HALF = 0, NB = 4, NS = 4, N = D_, KT = 28, B0 = 65536, BS0 = 0, OWN = 3; };
template <int CFG> struct Lay { using C = Cfg<CFG>; static constexpr int SBB = C::NP * 8192 + C::HALF * 4096, PB = 2 * C::NP + C::HALF, PER = 4 + PB; };
template <int CFG> DI unsigned btile(int tl, int lane) { using C = Cfg<CFG>; return C::B0 + (tl < 4 * C::NP ? (tl >> 2) * 8192 + ptile(tl & 3, lane) : C::NP * 8192 + htile(tl & 1, lane)); }
constexpr int L_RES = 106496, L_WPHI = 155648, L_UVT = L_WPHI + 4096, L_STT = L_UVT + 2048, L_ET = L_STT + 1024, L_GT = L_ET + 256, L_DUM = L_GT + 512, L_TOT = 163840, L_REST = 122880, L_OT = 131072;
constexpr int L_UN = 0, L_XT = 131072, L_RED = L_WPHI, L_SROW = L_RED + 2048, L_SIL2 = L_GT;
constexpr int L_GB = L_WPHI;
constexpr int L_VF = 0, L_TR = 16384, L_KX = 49152, L_RV = 90112;

template <int N> DI void waitlgkm() { __builtin_amdgcn_s_waitcnt(0xC07F | (N << 8)); }
template <int CFG> DI void compute_loop(f32x4 (&acc)[4][Cfg<CFG>::NT], unsigned lds0, int wm, int wn, int lane, unsigned& ts) {
  using C = Cfg<CFG>; using L = Lay<CFG>; constexpr int NT = C::NT, NB = C::NB, KT = C::KT, NU = 1 + NB;
  bf16x8 fa[2][4][2]; s16x4 fb[4][2][2]; unsigned va = lds0 + wm * 8192 + lane * 16, vb[NB], rawk = 0, rawv = 0;
  UNR for (int i = 0; i < NB; i++) vb[i] = lds0 + C::BS0 * L::SBB + btile<CFG>(CFG == 1 ? (i < 6 ? wn * 6 + i : 12 + wn) : wn * NB + i, lane);
  pin(va); pin(vb);
  auto bbuf = [](int par, int n) { return NT == 2 ? par * 2 + n : NT == 4 ? n : n == 6 ? 3 : n % 3; };
  auto rdb = [&](int par, int n, int i) { const int ks = i >> 1, t = i & 1;
    fb[bbuf(par, n)][ks][t] = trd(CFG == 0 ? vb[n & 1] + (n >> 1) * 8192 + ks * 4096 + t * 2048 : (CFG == 1 && n == 6) ? vb[NB - 1] + ks * 2048 + t * 1024 : vb[n] + ks * 4096 + t * 2048); };
  auto rda = [&](int par, int i) { fa[par][i >> 1][i & 1] = ldsr<bf16x8>(va + i * 1024); };
  int sa = 0;
  auto stage = [&](int par, int t, bool first, bool last) {
    int da = 0, db = 0;
    auto adv = [&](int j) { if (j == 0) { sa = opq(sa) + 1; da = 16384; if (sa == C::NS) { sa = 0; da -= C::NS * 16384; } da = opq(da); if (C::BS0 == 0 && L::SBB == 16384) db = da; }
                            else if (C::BS0 == 0) { if (L::SBB != 16384) db = opq(opq(sa) == 0 ? L::SBB - C::NS * L::SBB : L::SBB); } else db = opq(opq(sa) == C::NS - C::BS0 ? L::SBB - C::NS * L::SBB : L::SBB); };
    auto upd = [&](int u) { if (u == 0) { va += da; pin(va); } else if (u < NU) { vb[u - 1] += db; pin(vb[u - 1]); } };
    UNR for (int n = 0; n < NT; n++) {
      if (n == NT - 2) {
        if (NT == 2 && !last) { adv(0); adv(1); UNR for (int u = 0; u < NU; u++) upd(u); }
        waitlgkm<0>();
        if (!last) {
          if (C::OWN && t == C::OWN - 1) { if (CFG == 3) { waitvm<0>(); STAMP(32); bar(); } bar(); }
          if (CFG == SLOWC && SLOW && t < SLOWT) __builtin_amdgcn_s_sleep(SLOW);
          bar();
        }
        SB;
      } else if (n < NT - 2) { waitlgkm<4>(); SB; }
      UNR for (int i = 0; i < 8; i++) {
        const int m = (n & 1) ? 3 - (i >> 1) : i >> 1, ks = (i ^ (i >> 1)) & 1;
        const bf16x8 b = cat(fb[bbuf(par, n)][ks][0], fb[bbuf(par, n)][ks][1]); const f32x4 cin = first && !(i & 1) ? f32x4{0, 0, 0, 0} : acc[m][n];
        const bf16x8 o0 = (CFG == 0 && n >= 4) ? fa[par][m][ks] : b, o1 = (CFG == 0 && n >= 4) ? b : fa[par][m][ks];
        if (AM && first && !(i & 1)) asm volatile("v_mfma_f32_16x16x32_bf16 %0, %1, %2, 0" : "=&v"(acc[m][n]) : "v"(o0), "v"(o1));
        else if (XP == 2 && CFG == 2 && !last && (i & 2)) asm volatile("s_nop 1" : "+v"(acc[m][n]) : "v"(o0), "v"(o1));
        else if (AM && !last) asm volatile("v_mfma_f32_16x16x32_bf16 %0, %1, %2, %0" : "+v"(acc[m][n]) : "v"(o0), "v"(o1)); else acc[m][n] = mfma(o0, o1, cin);
        SB;
        if (n < NT - 3) { if (!(i & 1)) rdb(par, n + 2, i >> 1); else if (!last) { if (n == 0 && i < 4) adv(i >> 1); if (n == NT - 4 && 3 - (i >> 1) < NU - 4) upd(3 - (i >> 1)); } }
        else if (n == NT - 3) { if (i < 4) rdb(par, n + 2, i); else if (!last) upd(i - 4 + (NU > 4 ? NU - 4 : 0)); }
        else if (!last) { if (n == NT - 2) { if (i < 4) rdb(par ^ 1, 0, i); else rda(par ^ 1, i - 4); } else { if (i < 4) rda(par ^ 1, i + 4); else rdb(par ^ 1, 1, i - 4); } }
        if (CFG == 0 && last && n >= 3 && i >= 4 && i < 6) { const int hm = i - 4, hn = n - 1;
          ldsw<f32x4>(hn < 4 ? rawk + (hn & 1) * 4096 + hm * 1024 : rawv + hm * 2048, acc[hm][hn % NT]); }
        SB;
      }
    }
    if (CFG == 0 && last) UNR for (int hm = 1; hm >= 0; hm--) ldsw<f32x4>(rawv + hm * 2048 + 1024, acc[hm][5 % NT]);
  };
  SB; lbar();
  STAMP(CFG == 0 ? 12 : CFG == 2 ? 22 : CFG == 1 ? 28 : 36);
  UNR for (int i = 0; i < 4; i++) rdb(0, 0, i);
  UNR for (int i = 0; i < 8; i++) rda(0, i);
  UNR for (int i = 0; i < 4; i++) rdb(0, 1, i);
  SB;
  stage(0, 0, true, false);
  _Pragma("nounroll") for (int t = 1; t < KT - 1; t += 2) { stage(1, t, false, false); stage(0, t + 1, false, false); }
  if (CFG == 0) { rawk = lds0 + L_TR + (wm * 2 + wn) * 8192 + lane * 16; rawv = lds0 + L_RV + (wm * 2 + wn) * 4096 + lane * 16; pin(rawk); pin(rawv); SB; }
  stage(1, KT - 1, false, true);
}

template <int CFG> struct Loader {
  using C = Cfg<CFG>; using L = Lay<CFG>; static constexpr int N = C::N, KT = C::KT, PER = L::PER, PB = L::PB, NS = C::NS, OWN = C::OWN;
  const char *ab, *bb, *hb; unsigned av, bv, hv, la, lb, lh; int ka, kb, own;
  DI int skip(int k) const { if (k == KT) k = 0; if (OWN && k == own) { k += OWN; if (k == KT) k = 0; } return k; }
  DI void init(const void* A, int akt, const u16* W, int k0, int col0, int hcol, int rot, int own_, int tm, int lw, int lane, unsigned lds0) {
    const int r = lane >> 3, hr = lane >> 2;
    ab = (const char*)A + ((size_t)tm * akt + k0) * 16384 + lw * 4096; av = lane * 16;
    bb = (const char*)W + ((size_t)(k0 * 64 + lw * 16) * N + col0) * 2; bv = r * N * 2 + (((lane & 7) ^ (((r >> 1) & 3) << 1)) * 16);
    hb = (const char*)W + ((size_t)(lw * 16) * N + hcol) * 2; hv = hr * N * 2 + (((lane & 3) ^ (((hr >> 2) & 1) << 1)) * 16);
    if (CFG == 3) { const int t = lw & 1; bb -= (size_t)t * 16 * N * 2; hv = bv + (4 * t + 4 * (r >> 2)) * N * 2; bv += t * 16 * N * 2; }
    la = lds0 + lw * 4096; lb = lds0 + C::B0 + lw * 2048; lh = lds0 + C::B0 + C::NP * 8192 + lw * 1024; own = own_; ka = kb = skip(rot);
  }
  DI void issueA(int slot) { glds16x4(ab + (size_t)(XP == 1 && CFG == 2 ? ka & 1 : XP == 3 && CFG == 2 ? ka & 7 : ka) * 16384, av, la + slot * 16384); ka = skip(ka + 1); }
  DI void issueBk(int slot, int k) const { const char* b = bb + (size_t)k * 64 * N * 2; slot += C::BS0; if (slot >= NS) slot -= NS; const unsigned so = slot * L::SBB;
    const bool nat = CFG == 3 && ((0x1020408u >> k) & 1);
    const unsigned v0 = nat ? hv : bv, v1 = v0 + (nat ? 16 : 8) * N * 2;
    const unsigned l0 = lb + so;
    sfor<0, 2 * C::NP>([&](auto I) { constexpr int i = I.value; glds16i<(i >> 1) * 8192 + (i & 1) * 1024>(b + (i >> 1) * (CFG == 0 ? 2048 : 128) + (CFG == 3 ? 0 : (i & 1) * (8 * N * 2)), CFG == 3 && (i & 1) ? v1 : v0, l0); });
    if (C::HALF) glds16(hb + (size_t)k * 64 * N * 2, hv, lh + so);
  }
  DI void issueB(int slot) { issueBk(slot, kb); kb = skip(kb + 1); }
  DI void issue(int slot) { issueA(slot); issueB(slot); }
  DI u64 first_need(int n, int k0) const { u64 m = 0; int k = ka;
    for (int i = 0; i < n; i++) { if (CFG == 3) m |= (1ull << ((2 * (k0 + k)) / 7)) | (1ull << ((2 * (k0 + k) + 1) / 7)); else m |= 1ull << k; k = skip(k + 1); }
    return PW ? m : 0xffffull; }
  template <int E1, class HP, class HF, class HK, class H1> DI void run3(int e0, HP hp, HF hf, HK hk, H1 h1) {
    UNR for (int t = 1; t <= 6; t++) { hp(t); issue((t - 1) % 3); int e1 = hf(t); if (t == 6) e1 += hk(t); waitx<PER>(e0 + e1); lbar(); e0 = e1; }
    int slot = 0;
    _Pragma("nounroll") for (int t = 7; t <= KT - 3; t++) { issue(slot); slot++; if (slot == NS) slot = 0; const int e1 = hk(t); waitx<PER>(e0 + e1); lbar(); e0 = e1; }
    h1(); waitvm<E1>(); bar();
  }
  template <int E1, bool W1, class H0, class H1> DI void run4(H0 h0, H1 h1) {
    UNR for (int s = 0; s < 4; s++) issueA((OWN + s) & 3);
    waitvm<12>(); bar(); h0(); waitvm<8>(); bar();
    issue(OWN & 3); waitvm<4 + PER>(); bar();
    int slot = (OWN + 1) & 3;
    for (int t = 2; t <= KT - OWN - 4; t++) { issue(slot); slot = (slot + 1) & 3; waitvm<2 * PER>(); bar(); }
    if (W1) { h1(); waitvm<PER + E1>(); bar(); waitvm<0>(); bar(); } else { waitvm<PER>(); bar(); h1(); waitvm<E1>(); bar(); }
  }
};
DI u64* flagp(char* ws, int tile, int line, int word) { return (u64*)(ws + WS_FLAG) + (tile * 8 + line) * 16 + word; }
DI u64 wait_flags(u64* f, int lane, int n, u64 tag, u64 need) {
  u64 got = 0;
  _Pragma("nounroll") for (int spin = 0; spin < SPIN_CAP; spin++) { u64 v = tag;
    if (lane < n) asm volatile("global_atomic_or_x2 %0, %1, %2, off sc0 sc1\n\ts_waitcnt vmcnt(0)" : "=&v"(v) : "v"(f + lane), "v"(0ull) : "memory");
    got = __ballot(v == tag);
    if ((got & need) == need) break;
    asm volatile("s_sleep 1" ::: "memory");
  }
  return got;
}
struct Sync { u64* f; int n; u64 tag; bool all;
  template <class H> DI void run(char* ws, int line, const Blk& P, const Blk& C, int lw, int lane, u64 tag_, u64 need, H pre) {
    bar();
    tag = tag_; f = flagp(ws, C.tm * 16 + C.tn, line, 0); n = line == 0 ? 32 : 16; all = true;
    if (lw == 0) { const int i = lane & 15, grp = lane >> 4;
      if (grp < n / 16) __hip_atomic_store(grp == 0 ? flagp(ws, P.tm * 16 + i, line, P.tn) : flagp(ws, i * 16 + C.tn, 1, C.tm), tag, __ATOMIC_RELAXED, __HIP_MEMORY_SCOPE_AGENT); }
    pre();
    if (lw == 0) all = wait_flags(f, lane, n, tag, need) == ~0ull;
    lbar();
  }
  DI void rest(int lane) { if (!all) wait_flags(f, lane, n, tag, ~0ull); }
};
DI int modcol(int lane) { return 4 * (lane & 15) + 2 * (lane >> 5) + ((lane >> 4) & 1); }
DI float modvec(const char* ws, unsigned part, int lane, u32x4 (&p)[4], bool have) {
  if (!have || __any(max(max(umax4(p[0]), umax4(p[1])), max(umax4(p[2]), umax4(p[3]))) == 0xffffffffu)) { const rsrc_t r = mkrsrc(ws, part); poll(p, [&](int i) { return ld_dev(r, lane * 16, i * 1024); }); }
  const f32x4 a = (asf(p[0]) + asf(p[1])) + (asf(p[2]) + asf(p[3]));
  return rbf(sw16_add(sw32_add(a[0], a[2]), sw32_add(a[1], a[3])));
}
DI float modvec(const char* ws, unsigned part, int lane) { u32x4 p[4]; return modvec(ws, part, lane, p, false); }
DI void put_et(unsigned lds0, int vec, int k, float v) { ldsw<u16>(lds0 + L_ET + vec * 128 + fragpos(k), (u16)(__float_as_uint(v) >> 16)); }

template <int CFG, int NTL> struct UV {
  bf16x8 f[NTL][2]; unsigned ad[NTL], ts[NTL];
  DI void prep(int lw, int lane, unsigned base) { UNR for (int i = 0; i < NTL; i++) { const int tl = min(lw * NTL + i, 2 * Cfg<CFG>::NT - 1); ad[i] = base + btile<CFG>(tl, lane); ts[i] = tl < 4 * Cfg<CFG>::NP ? 2048 : 1024; pin(ad[i]); } }
  DI void read() { UNR for (int i = 0; i < NTL; i++) { f[i][0] = wfrag(ad[i], ts[i]); f[i][1] = wfrag(ad[i] + 2 * ts[i], ts[i]); } }
  bf16x8 e0, e1;
  DI void reade(int lane, unsigned lds0) { e0 = ldsr<bf16x8>(lds0 + L_ET + (lane & 1) * 128 + (lane >> 4) * 16); e1 = ldsr<bf16x8>(lds0 + L_ET + (lane & 1) * 128 + 64 + (lane >> 4) * 16); }
  DI void publish(const rsrc_t& r, int tm, int lw, int lane) {
    f32x4 d[NTL], o; const int i2 = (lane & 15) >> 1;
    UNR for (int i = 0; i < NTL; i++) d[i] = mfma(f[i][1], e1, mfma(f[i][0], e0, f32x4{0, 0, 0, 0}));
    UNR for (int k = 0; k < 4; k++) { o[k] = d[0][k]; UNR for (int i = 1; i < NTL; i++) o[k] = i2 == i ? d[i][k] : o[k]; }
    if (i2 < NTL) st_dev(__builtin_bit_cast(u32x4, o), r, i2 * 2048 + (lane & 1) * 64 + (lane >> 4) * 16, lw * NTL * 2048 + tm * 128);
  }
  u32x4 v[2 * NTL];
  DI void fetch(const char* base, int lw, int lane) { UNR for (int i = 0; i < 2 * NTL; i++) gld_dev(v[i], base + lw * NTL * 2048 + i * 1024, lane * 16); }
  DI void finish(const rsrc_t& r, int lw, int lane, unsigned tab) {
    pin(v); unsigned mx = 0;
    UNR for (int i = 0; i < 2 * NTL; i++) mx = max(mx, umax4(v[i]));
    if (__any(mx == 0xffffffffu)) poll(v, [&](int i) { return ld_dev(r, lane * 16, lw * NTL * 2048 + i * 1024); });
    UNR for (int i = 0; i < NTL; i++) { const f32x4 a = asf(v[2 * i]) + asf(v[2 * i + 1]);
      float t = sw16_add(sw32_add(a[0], a[2]), sw32_add(a[1], a[3])); t += dpp<0x128>(t);
      ldsw<float>(tab + (lw * NTL + i) * 128 + (lane & 7) * 16 + (2 * (lane >> 5) + ((lane >> 4) & 1)) * 4, t); }
  }
};
template <int NPART> struct Stats {
  u32x4 v[NPART / 4];
  DI void fetch(int i, const char* st, int tm, int lw, int lane) { gld_dev(v[i], st + (size_t)(tm * NPART + i) * 1024 + lw * 256, (lane >> 4) * (NPART * 256) + (lane & 15) * 16); }
  DI void check(const char* ws, unsigned st, int tm, int lw, int lane) { pin(v); unsigned mx = 0;
    UNR for (int i = 0; i < NPART / 4; i++) mx = max(mx, umax4(v[i]));
    if (__any(mx == 0xffffffffu)) poll(v, [&](int i) { return ld_dev(mkrsrc(ws, st + (tm * NPART + i) * 1024 + lw * 256), (lane >> 4) * (NPART * 256) + (lane & 15) * 16, 0); }); }
  DI void finish(int lw, int lane, unsigned tab) { pin(v); f32x4 s = asf(v[0]); float q0 = 0, q1 = 0;
    UNR for (int i = 1; i < NPART / 4; i++) s += asf(v[i]);
    float t = sw32_add(s[0], s[2]); t = sw16_add(t, t) * (1.f / NPART);
    const auto mm = __builtin_amdgcn_permlane32_swap(__float_as_uint(t), __float_as_uint(t), false, false);
    UNR for (int i = 0; i < NPART / 4; i++) { const float d0 = __uint_as_float(v[i][0]) - __uint_as_float(mm[0]), d1 = __uint_as_float(v[i][2]) - __uint_as_float(mm[1]); q0 = __builtin_fmaf(d0, d0, q0); q1 = __builtin_fmaf(d1, d1, q1); }
    float q = sw32_add(__builtin_fmaf((float)(D_ / NPART), q0, s[1]), __builtin_fmaf((float)(D_ / NPART), q1, s[3])); q = sw16_add(q, q);
    const float rstd = __builtin_amdgcn_rsqf(q * (1.f / D_) + 1e-5f);
    ldsw<float>(tab + ((lane >> 4) & 1) * 512 + (lw * 32 + (lane & 15) * 2 + (lane >> 5)) * 4, (lane & 16) ? -t * rstd : rstd); }
};

template <bool MAX> DI float fold4(float a, float b, float c, float d) {
  auto op = [](unsigned x, unsigned y) { return MAX ? __builtin_elementwise_maximum(__uint_as_float(x), __uint_as_float(y)) : __uint_as_float(x) + __uint_as_float(y); };
  const auto r0 = __builtin_amdgcn_permlane32_swap(__float_as_uint(a), __float_as_uint(b), false, false), r1 = __builtin_amdgcn_permlane32_swap(__float_as_uint(c), __float_as_uint(d), false, false);
  const auto r2 = __builtin_amdgcn_permlane16_swap(__float_as_uint(op(r0[0], r0[1])), __float_as_uint(op(r1[0], r1[1])), false, false);
  return op(r2[0], r2[1]);
}
DI void spread4(float t, float& a, float& b, float& c, float& d) {
  const auto r2 = __builtin_amdgcn_permlane16_swap(__float_as_uint(t), __float_as_uint(t), false, false);
  const auto r0 = __builtin_amdgcn_permlane32_swap(r2[0], r2[0], false, false), r1 = __builtin_amdgcn_permlane32_swap(r2[1], r2[1], false, false);
  a = __uint_as_float(r0[0]); b = __uint_as_float(r0[1]); c = __uint_as_float(r1[0]); d = __uint_as_float(r1[1]);
}
DI void feat2(const bf16x8 (&wf)[2], bf16x8 x0, bf16x8 x1, bf16x8& fp0, bf16x8& fn0, bf16x8& fp1, bf16x8& fn1) {
  const f32x4 zero = {0, 0, 0, 0}; f32x4 p[2][2] = {{mfma(wf[0], x0, zero), mfma(wf[1], x0, zero)}, {mfma(wf[0], x1, zero), mfma(wf[1], x1, zero)}};
  float mx[2], nx[2], sp[2], sn[2]; f32x4 a[2][2], b[2][2];
  UNR for (int i = 0; i < 2; i++) { const f32x4 u = p[i][0], v = p[i][1];
    mx[i] = fmax3(fmax3(fmax3(u[0], u[1], u[2]), u[3], v[0]), fmax3(v[1], v[2], v[3]), v[3]); nx[i] = fmax3(fmax3(fmax3(-u[0], -u[1], -u[2]), -u[3], -v[0]), fmax3(-v[1], -v[2], -v[3]), -v[3]); }
  spread4(fold4<true>(mx[0], mx[1], nx[0], nx[1]), mx[0], mx[1], nx[0], nx[1]);
  UNR for (int i = 0; i < 2; i++) UNR for (int h = 0; h < 2; h++) UNR for (int j = 0; j < 4; j += 2) { const f32x2 t = {p[i][h][j], p[i][h][j + 1]};
    f32x2 d = t - f32x2{mx[i], mx[i]}, e = -t - f32x2{nx[i], nx[i]}; asm("" : "+v"(d), "+v"(e));
    a[i][h][j] = __builtin_amdgcn_exp2f(d[0]); a[i][h][j + 1] = __builtin_amdgcn_exp2f(d[1]); b[i][h][j] = __builtin_amdgcn_exp2f(e[0]); b[i][h][j + 1] = __builtin_amdgcn_exp2f(e[1]); }
  UNR for (int i = 0; i < 2; i++) { sp[i] = hsum4(a[i][0] + a[i][1]); sn[i] = hsum4(b[i][0] + b[i][1]); }
  spread4(__builtin_amdgcn_rcpf(fold4<false>(sp[0], sp[1], sn[0], sn[1])), sp[0], sp[1], sn[0], sn[1]);
  fp0 = pack8(a[0][0] * sp[0], a[0][1] * sp[0]); fn0 = pack8(b[0][0] * sn[0], b[0][1] * sn[0]); fp1 = pack8(a[1][0] * sp[1], a[1][1] * sp[1]); fn1 = pack8(b[1][0] * sn[1], b[1][1] * sn[1]);
}

constexpr float L2E = 1.4426950408889634f;
DI void kv_prep(const f32x4 (&k)[2][2], const f32x4 (&vv)[2][2], const bf16x8 (&wf)[2], int hf, int w, int lane, unsigned lds0) {
  const int g = lane >> 4, r = lane & 15, row0 = (w >> 1) * 64 + hf * 32; const unsigned uva = lds0 + L_UVT + (w & 1) * 256;
  RowSt st[2]; float u[2], v[2]; f32x4 rs[2], nm[2];
  UNR for (int m = 0; m < 2; m++) st[m].set(lds0 + L_STT + (row0 + m * 16 + r) * 4, L2E);
  const f32x4 u0 = ldsr<f32x4>(uva + 512 + g * 16), v0 = ldsr<f32x4>(uva + 576 + g * 16) * L2E, u1 = ldsr<f32x4>(uva + 640 + g * 16), v1 = ldsr<f32x4>(uva + 704 + g * 16) * L2E;
  UNR for (int c = 0; c < 2; c++) { u[c] = ldsr<float>(uva + 1024 + c * 128 + r * 4); v[c] = ldsr<float>(uva + 1088 + c * 128 + r * 4); asm("" : "+v"(u[c])); asm("" : "+v"(v[c])); }
  UNR for (int m = 0; m < 2; m++) { const unsigned a = lds0 + L_STT + (row0 + m * 16 + 4 * g) * 4; rs[m] = ldsr<f32x4>(a); nm[m] = ldsr<f32x4>(a + 512); }
  bf16x8 a, b, c, d; feat2(wf, pack8(lnf(k[0][0], st[0], u0, v0), lnf(k[0][1], st[0], u1, v1)), pack8(lnf(k[1][0], st[1], u0, v0), lnf(k[1][1], st[1], u1, v1)), a, b, c, d);
  UNR for (int cc = 0; cc < 2; cc++) ldsw<bf16x8>(lds0 + L_VF + w * 4096 + hf * 2048 + cc * 1024 + lane * 16,
      pack8(fma4(vv[0][cc], rs[0], fma4(nm[0], spl(u[cc]), spl(v[cc]))), fma4(vv[1][cc], rs[1], fma4(nm[1], spl(u[cc]), spl(v[cc])))));
  const unsigned trw = lds0 + L_TR + w * 8192 + hf * 2048 + r * 64 + ((g ^ ((r >> 1) & 3)) * 16);
  ldsw<bf16x8>(trw, a); ldsw<bf16x8>(trw + 4096, b); ldsw<bf16x8>(trw + 1024, c); ldsw<bf16x8>(trw + 1024 + 4096, d);
}

DI void touch_code(int lane, unsigned ldsdummy) {
  const char* pc; unsigned len, back;
  asm volatile("s_getpc_b64 %0\n.Lhere%=:\n\ts_mov_b32 %2, .Lhere%=-.Lcode_begin\n\ts_mov_b32 %1, .Lcode_end-.Lcode_begin" : "=s"(pc), "=s"(len), "=s"(back));
  pc -= back;
  const unsigned o = ((blockIdx.x >> 3) * 16 + (lane & 15)) * 128;
  touch(pc, min(o, len), ldsdummy);
}
struct Args { const u16 *x, *wqkv, *wphi, *wo, *w1, *b1, *w2, *b2, *c, *wm; u16* out; char* ws; };

DI int gxcol(int lane) { return 2 * (lane & 31) + (lane >> 5); }
template <int N> DI void gx_gather(const char* ws, unsigned off, int lane, unsigned tag, float (&t)[N]) {
  const rsrc_t r = mkrsrc(ws, WS_GX + off); u32x4 v[N];
  _Pragma("nounroll") for (int spin = 0; spin < SPIN_CAP; spin++) { unsigned bad = 0;
    UNR for (int i = 0; i < N; i++) v[i] = ld_dev(r, lane * 16, i * 1024);
    UNR for (int i = 0; i < N; i++) bad |= (v[i][1] ^ tag) | (v[i][3] ^ tag);
    if (!__any(bad != 0)) break;
    asm volatile("s_sleep 2" ::: "memory");
  }
  UNR for (int i = 0; i < N; i++) t[i] = sw32_add(__uint_as_float(v[i][0]), __uint_as_float(v[i][2]));
}
DI float sum8(const float (&p)[8]) { return fadd(fadd(fadd(p[0], p[1]), fadd(p[2], p[3])), fadd(fadd(p[4], p[5]), fadd(p[6], p[7]))); }

DI u64 phase0(const Args& A, int w, unsigned lds0, unsigned& ts) {
  char* ws = A.ws; const Blk B = blk0(), B1 = blk1(); const f32x4 zero = {0, 0, 0, 0};
  const int lane = threadIdx.x & 63, r8 = lane >> 3, g = lane >> 4, r = lane & 15;
  u64 ep; asm volatile("s_load_dwordx2 %0, %1, 0x0" : "=s"(ep) : "s"(ws + WS_EPOCH) : "memory");
  unsigned cw; gld4(cw, A.c, B.tm * 128 + (lane & 31) * 4);
  { const char* wb = (const char*)A.wm + (size_t)(B.tm * 8 + w) * 16 * MODN + B.tn * 128; const unsigned wv = r8 * MODN * 2 + (((lane & 7) ^ (((r8 >> 1) & 3) << 1)) * 16);
    glds16(wb + 2 * D_, wv, lds0 + L_UN + w * 1024); glds16(wb, wv, lds0 + L_UN + 8192 + w * 1024); }
  bar();
  const unsigned rv0 = r8 * 2048 + (((lane & 7) ^ (r8 >> 1)) * 16);
  UNR for (int i = 0; i < 2; i++) glds16((const char*)A.x + ((size_t)(B.tm * 128 + w * 16 + i * 8) * D_ + B.tn * 64) * 2, i ? rv0 ^ 64 : rv0, lds0 + L_XT + (w * 2 + i) * 1024);
  if (XA2 == 1) { const int e = min(w * 48 + lane, 383), row = e / 12, l = e - row * 12;
    if (lane < 48) touch((const char*)A.wqkv + (size_t)B.j * 32 * 6144 + B1.xq * 512, row * 6144 + (l >> 2) * 2048 + (l & 3) * 128, lds0 + L_DUM); }
  else if (XA2 == 2) { const int e = min(w * 24 + lane, 191), row = e / 12, l = e - row * 12;
    if (lane < 24) touch((const char*)A.wqkv + (size_t)(B1.rot * 64 + (B.j & 3) * 16) * 6144 + B1.xq * 512, row * 6144 + (l >> 2) * 2048 + (l & 3) * 128, lds0 + L_DUM); }
  else { const int e = min(w * 24 + lane, 191), row = e / 6, l = e - row * 6;
    if (lane < 24) touch((const char*)A.wqkv + (size_t)B.j * 32 * 6144 + B.xq * 256, row * 6144 + (l >> 1) * 2048 + (l & 1) * 128, lds0 + L_DUM); }
  unsigned pa = lds0 + L_UN + (w >> 2) * 8192 + ptile(w & 3, lane); pin(pa);
  waitvm<5>(); pin(cw);
  asm volatile("s_waitcnt lgkmcnt(0)" : "+s"(ep) :: "memory");
  const u64 tag = ep + 0x9E3779B97F4A7C15ull;
  if (w == 7) ldsw<unsigned>(lds0 + L_SIL2 + fragpos(2 * (lane & 31)), silu2(cw));
  waitvm<3>(); lbar();
  STAMP(1);
  { const f32x4 d = mfma(wfrag(pa + 4096, 2048), ldsr<bf16x8>(lds0 + L_SIL2 + 64 + g * 16), mfma(wfrag(pa, 2048), ldsr<bf16x8>(lds0 + L_SIL2 + g * 16), zero));
    const bool o = r & 1; const u32x4 gr = {__float_as_uint(o ? d[2] : d[0]), (unsigned)tag, __float_as_uint(o ? d[3] : d[1]), (unsigned)tag};
    if (r < 2) st_dev(gr, mkrsrc(ws, WS_GX + (((w >> 2) * 16 + B.tn) * 16 + B.tm) * 512), g * 32 + r * 16, (w & 3) * 128); }
  asm volatile("" ::: "memory");
  if (w < 3) {
    const int l = w * 8 + r8;
    if (l < 20) st_dev(u32x4{~0u, ~0u, ~0u, ~0u}, mkrsrc(ws, WS_MODP), (l < 8 ? (((l >> 1) * 16 + B.tn) * 16 + B.tm) * 256 + (l & 1) * 128 : 4 * 65536 + (B1.tn * 12 + l - 8) * 2048 + B1.tm * 128) + (lane & 7) * 16, 0); }
  if (w == 7) { const Blk C = blk(); if (lane < 7) touch_any(flagp(ws, lane < 2 || lane == 6 ? B1.tm * 16 + B1.tn : C.tm * 16 + C.tn, lane, 0), lds0 + L_DUM); }
  if (w < 3 || w == 7) waitvm<3>(); else waitvm<2>();
  f32x4 xv[4]; f32x2 stat;
  UNR for (int cc = 0; cc < 4; cc++) { const u32x2 t = ldsr<u32x2>(lds0 + L_XT + (w * 16 + r) * 128 + (((cc * 2 + (g >> 1)) ^ (r >> 1)) * 16) + (g & 1) * 8); xv[cc] = unp(t[0], t[1]); }
  { const float mean = xg_sum(hsum4((xv[0] + xv[1]) + (xv[2] + xv[3]))) * (1.f / 64); f32x4 q = zero;
    UNR for (int cc = 0; cc < 4; cc++) { const f32x4 d = xv[cc] - mean; q = fma4(d, d, q); }
    stat = f32x2{mean, xg_sum(hsum4(q))}; pin(stat); pin(xv); }
  waitvm<0>();
  STAMP(2);
  { float t[1]; gx_gather<1>(ws, B.tn * 8192 + w * 1024, lane, (unsigned)tag, t); ldsw<float>(lds0 + L_RED + w * 256 + gxcol(lane) * 4, t[0]); }
  STAMP(3);
  lbar();
  float p[8]; UNR for (int i = 0; i < 8; i++) p[i] = ldsr<float>(lds0 + L_RED + i * 256 + lane * 4);
  ldsw<float>(lds0 + L_SROW + w * 256 + lane * 4, rbf(1.f + rbf(sum8(p))));
  asm volatile("" ::: "memory");
  const rsrc_t ry = outrsrc(A.out, (B.tm * 16 + B.tn) * 16384);
  UNR for (int ks = 0; ks < 2; ks++)
    st_dev(pack8u(xv[2 * ks] * ldsr<f32x4>(lds0 + L_SROW + w * 256 + ks * 128 + g * 16), xv[2 * ks + 1] * ldsr<f32x4>(lds0 + L_SROW + w * 256 + ks * 128 + 64 + g * 16)), ry, lane * 16, w * 2048 + ks * 1024);
  if (g == 0) st_dev8(__builtin_bit_cast(u32x2, stat), mkrsrc(ws, WS_ST1 + (size_t)(B.tm * 16 + B.tn) * 1024), r * 8, w * 128);
  asm volatile("" ::: "memory");
  STAMP(4);
  return tag;
}

DI void loader_role(const Args& A, int lw, unsigned lds0, u64 tag, unsigned& ts) {
  char* ws = A.ws; const Blk B2 = blk(), B1 = blk1(), B0 = blk0(); const f32x4 zero = {0, 0, 0, 0}; const u32x4 mark = {~0u, ~0u, ~0u, ~0u};
  int lane = threadIdx.x & 63; pin(lane);
  Loader<2> G2; Sync Y; auto none = [](int) { return 0; };
  {
    const Blk& B = B1; const int g = lane >> 4, r = lane & 15, q = r >> 2, p = lane & 3, r8 = lane >> 3;
    Loader<0> G; G.init(A.out, 16, A.wqkv, 0, B.tn * 64, 0, B.rot, 0, B.tm, lw, lane, lds0);
    G.issueB(0); G.issueB(1);
    const rsrc_t ruv = mkrsrc(ws, WS_UV1 + B.tn * 12 * 2048);
    UV<0, 3> U; Stats<16> S; U.prep(lw, lane, lds0);
    waitvm<12>();
    STAMP(5);
    Y.run(ws, 0, B0, B, lw, lane, tag, G.first_need(3, 0), [&]() {});
    STAMP(11);
    const int t3 = 6 + (B.j >> 2);
    G.issueA(0); G.issueA(1); waitvm<4>(); bar();
    U.read(); U.reade(lane, lds0); G.issue(2); Y.rest(lane); waitvm<10>(); lbar();
    G.run3<7>(0, none,
      [&](int t) { int e = 0;
        if (t == 1) { UNR for (int c = 0; c < 2; c++) st_dev(mark, mkrsrc(ws, WS_KV1 + (size_t)(B.tn * 4 + lw) * 32768), lane * 16, B.tm * 2048 + c * 1024); e = 2; }
        if (t == 2) { UNR for (int c = 0; c < 2; c++) st_dev(mark, mkrsrc(ws, WS_KV2 + (size_t)(B.tn * 16 + B.tm) * 8192), lane * 16, lw * 2048 + c * 1024); e = 2; }
        if (t == 3) { if (r8 < 5) st_dev(mark, mkrsrc(ws, WS_ZP), (r8 == 0 ? (B.tn * 4 + lw) * 2048 : (1 << 17) + (B.tn * 16 + lw * 4 + r8 - 1) * 2048) + (lane & 7) * 16, B.tm * 128);
          if (lane < 32) st_dev(mark, mkrsrc(ws, WS_ST2 + blockIdx.x * 2048), lane * 16, lw * 512); e = 2; }
        if (t == 4 || t == 5) { UNR for (int i = 0; i < 2; i++) S.fetch(2 * t - 8 + i, ws + WS_ST1, B.tm, lw, lane); e = 2; }
        if (t == 6) { prio(1); U.publish(ruv, B.tm, lw, lane); prio(0); e = 1; }
        return e; },
      [&](int t) { int e = 0;
        if (t == t3 && !(EV & 2)) { touch(opq((const char*)A.w1) + (size_t)(blockIdx.x * 4 + lw) * 7168, min(lane, 55) * 128, lds0 + L_DUM); touch(opq((const char*)A.wo) + (size_t)(blockIdx.x * 4 + lw) * 2048, (lane & 15) * 128, lds0 + L_DUM); e += 2; }
        if (t == 11 && lw == 0) { touch_code(lane, lds0 + L_DUM); e += 1; }
        return e; },
      [&]() { U.fetch(opq(ws) + ruv.base, lw, lane); glds16((const char*)A.wphi + B.tn * 4096 + lw * 1024, lane * 16, lds0 + L_WPHI + lw * 1024); });
    {
      const char* wb = (const char*)A.wm + (size_t)B0.tm * 128 * MODN + (lw + 2) * 2 * D_ + B0.tn * 128; const unsigned wv = r8 * MODN * 2 + (((lane & 7) ^ (((r8 >> 1) & 3) << 1)) * 16);
      UNR for (int i = 0; i < 8; i++) glds16(wb + i * 16 * MODN, wv, lds0 + L_REST + lw * 8192 + i * 1024); }
    prio(1);
    S.finish(lw, lane, lds0 + L_STT);
    waitvm<8>(); U.finish(ruv, lw, lane, lds0 + L_UVT);
    G2.init(ws + WS_O, 16, A.wo, 0, B2.tn * 64, 0, B2.rot, 0, B2.tm, lw, lane, lds0);
    prio(0);
    { const unsigned rv0 = r8 * 2048 + (((lane & 7) ^ (r8 >> 1)) * 16);
      UNR for (int i = 0; i < 4; i++) glds16((const char*)A.x + ((size_t)(B2.tm * 128 + (lw * 4 + i) * 8) * D_ + B2.tn * 64) * 2, (i & 1) ? rv0 ^ 64 : rv0, lds0 + L_RES + (lw * 4 + i) * 1024); }
    lbar();
    { bf16x8 wf[2]; f32x4 k[2][2], vv[2][2];
      UNR for (int m = 0; m < 2; m++) UNR for (int c = 0; c < 2; c++) { k[m][c] = ldsr<f32x4>(lds0 + L_TR + lw * 8192 + c * 4096 + m * 1024 + lane * 16); vv[m][c] = ldsr<f32x4>(lds0 + L_RV + lw * 4096 + (m * 2 + c) * 1024 + lane * 16); }
      UNR for (int c = 0; c < 2; c++) wf[c] = wfrag(lds0 + L_WPHI + (lw & 1) * 2048 + (4 * g + q) * 64 + c * 32 + p * 8, 1024);
      kv_prep(k, vv, wf, 0, lw, lane, lds0); }
    if (AN && lw == 0 && lane < 16) __hip_atomic_store(flagp(ws, lane * 16 + B.tn, 6, B.tm), tag, __ATOMIC_RELAXED, __HIP_MEMORY_SCOPE_AGENT);
    lbar();
    prio(1);
    const int hd = lw >> 1, sg = lw & 1;
    const u32x4 one4 = {0x3f803f80u, 0x3f803f80u, 0x3f803f80u, 0x3f803f80u}; const bf16x8 ones = __builtin_bit_cast(bf16x8, one4);
    const unsigned tra = lds0 + L_TR + hd * 8192 + sg * 4096 + (4 * g + q) * 64 + ((p ^ (((4 * g + q) >> 1) & 3)) * 16), vfa = lds0 + L_VF + hd * 4096 + lane * 16;
    f32x4 kv[2][2] = {{zero, zero}, {zero, zero}}, z[2] = {zero, zero};
    UNR for (int k = 0; k < 4; k++) { const bf16x8 v0 = ldsr<bf16x8>(vfa + (k >> 1) * 8192 + (k & 1) * 2048), v1 = ldsr<bf16x8>(vfa + (k >> 1) * 8192 + (k & 1) * 2048 + 1024);
      UNR for (int ft = 0; ft < 2; ft++) { const bf16x8 af = wfrag(tra + (k >> 1) * 16384 + (k & 1) * 2048 + ft * 8, 1024);
        kv[ft][0] = mfma(af, v0, kv[ft][0]); kv[ft][1] = mfma(af, v1, kv[ft][1]); z[ft] = mfma(af, ones, z[ft]); } }
    const rsrc_t rk = mkrsrc(ws, WS_KV1 + (size_t)(B.tn * 4 + lw) * 32768), rz = mkrsrc(ws, WS_ZP + (size_t)(B.tn * 4 + lw) * 2048), r2 = mkrsrc(ws, WS_KV2 + (size_t)B.tn * 131072);
    UNR for (int c = 0; c < 2; c++) st_dev(pack8u(kv[0][c], kv[1][c]), rk, lane * 16, B.tm * 2048 + c * 1024);
    { const bool o = lane & 1; const f32x4 zs = {o ? z[1][0] : z[0][0], o ? z[1][1] : z[0][1], o ? z[1][2] : z[0][2], o ? z[1][3] : z[0][3]};
      if ((lane & 14) == 0) st_dev(__builtin_bit_cast(u32x4, zs), rz, (lane & 1) * 64 + g * 16, B.tm * 128); }
    STAMP(16);
    waitvm<3>();
    { const f32x4 d = gemv64(lds0 + L_REST + lw * 8192, ldsr<bf16x8>(lds0 + L_SIL2 + g * 16), ldsr<bf16x8>(lds0 + L_SIL2 + 64 + g * 16), lane);
      if (r < 4) st_dev(__builtin_bit_cast(u32x4, d), mkrsrc(ws, WS_MODP + (size_t)((lw * 16 + B0.tn) * 16 + B0.tm) * 256), r * 64 + g * 16, 0); }
    if (AN) wait_flags(flagp(ws, B.tm * 16 + B.tn, 6, 0), lane, 16, tag, ~0ull);
    waitvm<1>();
    u32x4 h[2], zz[2]; poll(h, [&](int i) { return ld_dev(rk, (lane >> 3) * 2048 + (lane & 7) * 16, B.tm * 128 + i * 16384); });
    UNR for (int i = 0; i < 2; i++) gld_dev(zz[i], ws + rz.base + i * 64, r * 128 + g * 16);
    pin(h);
    { float e[4], o[4];
      UNR for (int k = 0; k < 4; k++) { e[k] = fadd(blo(h[0][k]), blo(h[1][k])); o[k] = fadd(bhi(h[0][k]), bhi(h[1][k])); }
      float te = fold4<false>(e[0], e[1], e[2], e[3]), to = fold4<false>(o[0], o[1], o[2], o[3]); te += dpp<0x128>(te); to += dpp<0x128>(to);
      spread4(__uint_as_float(cvtpk(te, to)), e[0], e[1], e[2], e[3]);
      const u32x4 sum = {__float_as_uint(e[0]), __float_as_uint(e[1]), __float_as_uint(e[2]), __float_as_uint(e[3])};
      UNR for (int i = 0; i < 2; i++) st_dev(sum, r2, (lane >> 3) * 8192 + (lane & 7) * 16, i * 65536 + lw * 2048 + B.tm * 128); }
    STAMP(17);
    waitvm<2>(); pin(zz);
    if (__any(max(umax4(zz[0]), umax4(zz[1])) == 0xffffffffu)) poll(zz, [&](int i) { return ld_dev(rz, r * 128 + g * 16, i * 64); });
    ldsw<bf16x8>(lds0 + L_KX + lw * 1024 + lane * 16, pack8(row16_sum4(asf(zz[0])), row16_sum4(asf(zz[1]))));
    waitvm<0>();
    lbar();
    prio(0);
  }
  const Blk& B = B2; const int odd = B.tn & 1, q4 = (B.tm >> 3) + 2 * ((B.tm >> 2) & 1);
  Loader<1> G3;
  {
    UNR for (int s = 0; s < 4; s++) G2.issueB(s);
    Y.run(ws, 2, B1, B, lw, lane, tag, G2.first_need(4, 0), [&]() {});
    STAMP(21);
    G3.init(A.out, 16, A.w1, 0, B.tn * 224 + odd * 32, B.tn * 224 + (odd ? 0 : 192), q4 * 4, B.tn, B.tm, lw, lane, lds0);
    G2.run4<7, false>([&]() { Y.rest(lane); }, [&]() { G3.issueBk(0, B.tn); });
    if (blockIdx.x == 0 && lw == 0 && lane == 0) __hip_atomic_store((u64*)(ws + WS_EPOCH), tag, __ATOMIC_RELAXED, __HIP_MEMORY_SCOPE_AGENT);
  }
  Loader<3> G4; const int own4 = (7 * (B.tn & 7) + 1) >> 1;
  {
    const rsrc_t ruv = mkrsrc(ws, WS_UV2 + B.tn * 16 * 2048);
    UV<1, 4> U; Stats<32> S; const int ku = q4 * 4 + (B.tm & 3), tu = ku == B.tn ? 0 : 1 + (B.tm & 3) - (B.tn >= q4 * 4 && B.tn < ku), t3 = 6 + (B.j & 7), t4 = W2T == 1 ? 7 + ((B.j + 16 * (B.tm >> 3)) & 31) * 7 / 32 : 10 + (B.j & 3);
    U.prep(lw, lane, lds0 + ((tu + 2) % 3) * Lay<1>::SBB);
    bar();
    G3.issueB(1);
    waitvm<7>();
    Y.run(ws, 3, B, B, lw, lane, tag, G3.first_need(2, 0), [&]() { U.read(); G3.issueB(2); });
    STAMP(27);
    G3.issueA(1); G3.issueA(2); Y.rest(lane); waitvm<4>(); bar();
    G3.run3<8>(0,
      [&](int t) { if (t < 5 && t == tu) U.read(); if (t == 5) U.reade(lane, lds0); },
      [&](int t) { int e = 0;
        if (t <= 3) { UNR for (int i = 3 * t - 3; i < 3 * t && i < 8; i++) { S.fetch(i, ws + WS_ST2, B.tm, lw, lane); e++; } }
        if (t == 5) { prio(1); U.publish(ruv, B.tm, lw, lane); prio(0); e = 1; }
        if (t == 6) { prio(1); S.check(ws, WS_ST2, B.tm, lw, lane); S.finish(lw, lane, lds0 + L_STT); prio(0); }
        return e; },
      [&](int t) { int e = 0;
        if (!XF && t >= 7 && t < 11) { st_dev(mark, mkrsrc(ws, xchoff(B.tm, B.tn, lw)), lane * 16, (t - 7) * 1024); e = 1; }
        if (W2T == 0 && t == t3) { touch(opq((const char*)A.w2) + (size_t)(blockIdx.x * 4 + lw) * 7168, min(lane, 55) * 128, lds0 + L_DUM); e += 1; }
        if (W2T && t == t4) {
          const char* p = opq((const char*)A.w2) + (size_t)((B.xq >> 1) * 1792 + B.j * 56 + lw * 14) * 2048 + (B.xq & 1) * 1024; const unsigned v = min(lane >> 3, 6) * 2048 + (lane & 7) * 128;
          touch(p, v, lds0 + L_DUM); touch(p + 7 * 2048, v, lds0 + L_DUM); e += 2; }
        if (t == 11 && lw == 0) { touch_code(lane, lds0 + L_DUM); e += 1; }
        return e; },
      [&]() { U.fetch(opq(ws) + ruv.base, lw, lane); });
    { const int r0 = (B.rot * 25) >> 4; G4.init(ws + WS_H1, 56, A.w2, (B.tn >> 3) * 28, (B.tn & 7) * 128, 0, r0 < own4 ? r0 : r0 + 3, own4, B.tm, lw, lane, lds0); }
    G4.issueBk(0, own4);
    prio(1);
    waitvm<4>(); U.finish(ruv, lw, lane, lds0 + L_UVT);
    prio(0);
    G4.issueBk(1, own4 + 1);
    lbar();
    G4.issueBk(2, own4 + 2);
  }
  {
    waitvm<0>(); bar();
    G4.issueB(3); bar(); G4.issueB(0); bar(); G4.issueB(1);
    Y.run(ws, 4, B, B, lw, lane, tag, G4.first_need(4, (B.tn >> 3) * 28), [&]() { G4.issueB(2); });
    STAMP(35);
    G4.run4<4, true>([&]() { Y.rest(lane); }, [&]() {
      glds16x4(opq(ws) + WS_X1 + (size_t)(B.tm * 16 + (B.tn & 7) * 2 + (lw & 1)) * 16384 + ((lw >> 1) * 4 + (B.tn >> 3) * 2) * 2048, lane * 16, lds0 + L_OT + lw * 4096); });
    asm volatile(".Lcode_end:");
  }
}

DI void compute_role(const Args& A, int w, unsigned lds0, u64 tag, unsigned& ts) {
  char* ws = A.ws; const Blk B = blk(); const f32x4 zero = {0, 0, 0, 0}; const u32x4 mark = {~0u, ~0u, ~0u, ~0u};
  const int wm = w >> 1, wn = w & 1; int lane = threadIdx.x & 63; pin(lane);
  {
    const Blk B = blk1(); const int g = lane >> 4, r = lane & 15, q = r >> 2, p = lane & 3;
    waitvm<0>();
    STAMP(5);
    bar();
    if (w < 2) { float t[8]; gx_gather<8>(ws, (w * 16 + B.rot) * 8192, lane, (unsigned)tag, t); const float v = rbf(sum8(t)); put_et(lds0, w, gxcol(lane), w ? v : rbf(1.f + v)); }
    bar();
    f32x4 acc[4][6];
    compute_loop<0>(acc, lds0, wm, wn, lane, ts);
    STAMP(13);
    lbar();
    STAMP(14);
    bf16x8 wf[2];
    UNR for (int c = 0; c < 2; c++) wf[c] = wfrag(lds0 + L_WPHI + wn * 2048 + (4 * g + q) * 64 + c * 32 + p * 8, 1024);
    { const f32x4 k[2][2] = {{acc[2][2], acc[2][3]}, {acc[3][2], acc[3][3]}}, vv[2][2] = {{acc[2][4], acc[2][5]}, {acc[3][4], acc[3][5]}}; kv_prep(k, vv, wf, 1, w, lane, lds0); }
    lbar();
    STAMP(15);
    asm volatile("" : "+v"(acc[0][0]), "+v"(acc[0][1]), "+v"(acc[1][0]), "+v"(acc[1][1]), "+v"(acc[2][0]), "+v"(acc[2][1]), "+v"(acc[3][0]), "+v"(acc[3][1]));
    bf16x8 qp[4], qn[4]; RowSt st[4]; const unsigned uva = lds0 + L_UVT + wn * 256;
    UNR for (int m = 0; m < 4; m++) st[m].set(lds0 + L_STT + (wm * 64 + m * 16 + r) * 4, L2E);
    { const f32x4 u0 = ldsr<f32x4>(uva + g * 16), v0 = ldsr<f32x4>(uva + 64 + g * 16) * L2E, u1 = ldsr<f32x4>(uva + 128 + g * 16), v1 = ldsr<f32x4>(uva + 192 + g * 16) * L2E;
      UNR for (int m = 0; m < 4; m += 2) feat2(wf, pack8(lnf(acc[m][0], st[m], u0, v0), lnf(acc[m][1], st[m], u1, v1)),
                                               pack8(lnf(acc[m + 1][0], st[m + 1], u0, v0), lnf(acc[m + 1][1], st[m + 1], u1, v1)), qp[m], qn[m], qp[m + 1], qn[m + 1]); }
    asm volatile("" : "+v"(qp[0]), "+v"(qp[1]), "+v"(qp[2]), "+v"(qp[3]), "+v"(qn[0]), "+v"(qn[1]), "+v"(qn[2]), "+v"(qn[3]) :: "memory");
    STAMP(16);
    bar();
    STAMP(17);
    const rsrc_t r2 = mkrsrc(ws, WS_KV2 + (size_t)(B.tn * 16 + B.tm) * 8192 + wn * 4096);
    u32x4 kk[4]; UNR for (int i = 0; i < 4; i++) kk[i] = ld_dev(r2, lane * 16, i * 1024);
    const bf16x8 z0 = ldsr<bf16x8>(lds0 + L_KX + wn * 2048 + lane * 16), z1 = ldsr<bf16x8>(lds0 + L_KX + wn * 2048 + 1024 + lane * 16);
    float rd[4];
    UNR for (int m = 0; m < 4; m++) rd[m] = __builtin_amdgcn_rcpf(mfma(z1, qn[m], mfma(z0, qp[m], zero))[0] + 1e-6f);
    asm volatile("" : "+v"(rd[0]), "+v"(rd[1]), "+v"(rd[2]), "+v"(rd[3]));
    if (__any(max(max(umax4(kk[0]), umax4(kk[1])), max(umax4(kk[2]), umax4(kk[3]))) == 0xffffffffu)) poll(kk, [&](int i) { return ld_dev(r2, lane * 16, i * 1024); });
    const rsrc_t ro = mkrsrc(ws, WS_O + (size_t)(B.tm * 16 + B.tn) * 16384);
    UNR for (int m = 0; m < 4; m++) {
      const f32x4 n0 = mfma(__builtin_bit_cast(bf16x8, kk[2]), qn[m], mfma(__builtin_bit_cast(bf16x8, kk[0]), qp[m], zero)), n1 = mfma(__builtin_bit_cast(bf16x8, kk[3]), qn[m], mfma(__builtin_bit_cast(bf16x8, kk[1]), qp[m], zero));
      st_dev(pack8u(n0 * rd[m], n1 * rd[m]), ro, lane * 16, (wm * 8 + wn) * 1024 + m * 2048);
    }
    STAMP(18);
    waitvm<0>(); bar();
    STAMP(19);
  }
  pin(lane);
  const int fp0 = B.tn * 7 + (B.tn & 1) + wn * 3, fph = (B.tn & 1) ? B.tn * 7 : B.tn * 7 + 6;
  u32x2 bb[7];
  {
    const int g = lane >> 4, r = lane & 15;
    UNR for (int n = 0; n < 7; n++) bb[n] = *(const u32x2*)(A.b1 + (n < 6 ? fp0 * 32 + n * 16 : fph * 32 + wn * 16) + 4 * g);
    const unsigned b2v = A.b2[((B.tn & 7) * 2 + wn) * 64 + lane];
    u32x4 gp[4]; UNR for (int i = 0; i < 4; i++) gp[i] = ld_dev(mkrsrc(ws, WS_MODP + (48 + (B.tn & 7) * 2 + wn) * 4096), lane * 16, i * 1024);
    { const float v = modvec(ws, WS_MODP + (size_t)((w == 0 ? 0 : w == 3 ? 1 : 2) * 16 + (w < 2 ? B.tn : ((B.tm >> 3) + 2 * ((B.tm >> 2) & 1)) * 4 + (B.tm & 3))) * 4096, lane), s = (w == 1 || w == 2) ? rbf(1.f + v) : v;
      if (w < 2) ldsw<float>(lds0 + L_GT + w * 256 + modcol(lane) * 4, s); else put_et(lds0, w - 2, modcol(lane), s); }
    pin(bb);
    f32x4 acc[4][2];
    bar();
    if (w < 2) ldsw<float>(lds0 + L_GB + w * 256 + modcol(lane) * 4, modvec(ws, WS_MODP + (size_t)(48 + (B.tn & 7) * 2 + w) * 4096, lane, gp, true));
    else ldsw<float>(lds0 + L_GB + w * 256 + lane * 4, blo(b2v));
    compute_loop<2>(acc, lds0, wm, wn, lane, ts);
    STAMP(23);
    bar();
    f32x4 gate[2], sc[2]; u32x2 xr[4][2];
    UNR for (int n = 0; n < 2; n++) { gate[n] = ldsr<f32x4>(lds0 + L_GT + wn * 128 + n * 64 + g * 16); sc[n] = ldsr<f32x4>(lds0 + L_GT + 256 + wn * 128 + n * 64 + g * 16); }
    UNR for (int m = 0; m < 4; m++) UNR for (int n = 0; n < 2; n++) xr[m][n] = ldsr<u32x2>(lds0 + L_RES + (wm * 64 + m * 16 + r) * 128 + (((wn * 4 + n * 2 + (g >> 1)) ^ (r >> 1)) * 16) + (g & 1) * 8);
    const size_t tb = (size_t)(B.tm * 16 + B.tn) * 16384; const rsrc_t ry = outrsrc(A.out, tb), rst = mkrsrc(ws, WS_ST2 + (size_t)(B.tm * 32 + B.tn * 2 + wn) * 1024);
    f32x4 v[4][2]; float mean[4], m2[4]; u32x4 o[4];
    UNR for (int m = 0; m < 4; m++) {
      o[m] = pack8u(fma4(gate[0], acc[m][0], unp(xr[m][0][0], xr[m][0][1])), fma4(gate[1], acc[m][1], unp(xr[m][1][0], xr[m][1][1]))); v[m][0] = unp(o[m][0], o[m][1]); v[m][1] = unp(o[m][2], o[m][3]);
      const u32x4 y = pack8u(v[m][0] * sc[0], v[m][1] * sc[1]);
      st_dev(y, ry, lane * 16, (wm * 8 + wn) * 1024 + m * 2048); ldsw<u32x4>(lds0 + (wm * 8 + wn) * 1024 + m * 2048 + lane * 16, y); }
    asm volatile("" ::: "memory");
    UNR for (int m = 0; m < 4; m++) mean[m] = hsum4(v[m][0] + v[m][1]);
    const float mf = fold4<false>(mean[0], mean[1], mean[2], mean[3]) * (1.f / 32); spread4(mf, mean[0], mean[1], mean[2], mean[3]);
    UNR for (int m = 0; m < 4; m++) { const f32x4 d0 = v[m][0] - mean[m], d1 = v[m][1] - mean[m]; m2[m] = hsum4(fma4(d0, d0, d1 * d1)); }
    st_dev8(__builtin_bit_cast(u32x2, f32x2{mf, fold4<false>(m2[0], m2[1], m2[2], m2[3])}), rst, (wm * 64 + ((g >> 1) + 2 * (g & 1)) * 16 + r) * 8, 0);
    STAMP(24);
    asm volatile("" ::: "memory");
    UNR for (int m = 0; m < 4; m++) st_dev(o[m], mkrsrc(ws, WS_X1 + tb), lane * 16, (wm * 8 + wn) * 1024 + m * 2048);
    waitvm<5>();
    STAMP(25);
  }
  pin(lane);
  {
    const int g = lane >> 4, r = lane & 15;
    f32x4 acc[4][7];
    compute_loop<1>(acc, lds0, wm, wn, lane, ts);
    STAMP(29);
    bar();
    STAMP(30);
    f32x4 u[7], v[7];
    UNR for (int n = 0; n < 7; n++) { const unsigned a = lds0 + L_UVT + (n < 6 ? wn * 6 + n : 12 + wn) * 128 + g * 16; u[n] = ldsr<f32x4>(a); v[n] = ldsr<f32x4>(a + 64) + unp(bb[n][0], bb[n][1]); }
    const rsrc_t rh = mkrsrc(ws, WS_H1 + (size_t)B.tm * 56 * 16384);
    RowSt st[4]; u32x2 h6[4];
    UNR for (int m = 0; m < 4; m++) st[m].set(lds0 + L_STT + (wm * 64 + m * 16 + r) * 4, 1.f);
    auto act = [&](int m, int n) { const f32x4 t = __builtin_elementwise_max(lnf(acc[m][n], st[m], u[n], v[n]), zero); return t * t; };
    UNR for (int n = 0; n < 6; n += 2) { const int fp = fp0 + (n >> 1);
      const int gi = wn * 3 + (n >> 1); const unsigned la = lds0 + (gi >> 1) * 16384 + (gi & 1) * 1024 + wm * 8192 + lane * 16;
      UNR for (int m = 0; m < 4; m++) { const u32x4 hh = pack8u(act(m, n), act(m, n + 1)); st_dev(hh, rh, lane * 16, (fp >> 1) * 16384 + (fp & 1) * 1024 + wm * 8192 + m * 2048); ldsw<u32x4>(la + m * 2048, hh); } }
    UNR for (int m = 0; m < 4; m++) { const f32x4 t = act(m, 6); h6[m] = u32x2{cvtpk(t[0], t[1]), cvtpk(t[2], t[3])}; }
    UNR for (int m = 0; m < 4; m += 2) { const auto a = __builtin_amdgcn_permlane16_swap(h6[m][0], h6[m + 1][0], false, false), b = __builtin_amdgcn_permlane16_swap(h6[m][1], h6[m + 1][1], false, false);
      st_dev(u32x4{a[0], b[0], a[1], b[1]}, rh, (g & 1) * 2048 + (g >> 1) * 256 + r * 16, (fph >> 1) * 16384 + (fph & 1) * 1024 + wm * 8192 + m * 2048 + wn * 512); }
    STAMP(31);
  }
  pin(lane);
  {
    const int g = lane >> 4, r = lane & 15, kh = B.tn >> 3, cg = (B.tn & 7) * 2 + wn;
    u32x4 xr[2][2]; f32x4 acc[4][4];
    compute_loop<3>(acc, lds0, wm, wn, lane, ts);
    STAMP(37);
    UNR for (int mm = 0; mm < 2; mm++) UNR for (int ks = 0; ks < 2; ks++) xr[mm][ks] = ldsr<u32x4>(lds0 + L_OT + w * 4096 + (mm * 2 + ks) * 1024 + lane * 16);
    const rsrc_t rx = mkrsrc(ws, xchoff(B.tm, B.tn, w)), rp = mkrsrc(ws, xchoff(B.tm, B.tn ^ 8, w));
    f32x4 keep[2][4], gate[4], bias[4];
    if (kh) { UNR for (int i = 0; i < 4; i++) st_dev(pack8u(acc[i >> 1][(i & 1) * 2], acc[i >> 1][(i & 1) * 2 + 1]), rx, lane * 16, i * 1024);
              UNR for (int n = 0; n < 4; n++) { keep[0][n] = acc[2][n]; keep[1][n] = acc[3][n]; } }
    else    { UNR for (int i = 0; i < 4; i++) st_dev(pack8u(acc[2 + (i >> 1)][(i & 1) * 2], acc[2 + (i >> 1)][(i & 1) * 2 + 1]), rx, lane * 16, i * 1024);
              UNR for (int n = 0; n < 4; n++) { keep[0][n] = acc[0][n]; keep[1][n] = acc[1][n]; } }
    UNR for (int n = 0; n < 4; n++) { gate[n] = ldsr<f32x4>(lds0 + L_GB + wn * 256 + n * 64 + g * 16); bias[n] = ldsr<f32x4>(lds0 + L_GB + 512 + wn * 256 + n * 64 + g * 16); }
    UNR for (int mm = 0; mm < 2; mm++) UNR for (int n = 0; n < 4; n++) keep[mm][n] = fma4(gate[n], keep[mm][n] + bias[n], unp(xr[mm][n >> 1][(n & 1) * 2], xr[mm][n >> 1][(n & 1) * 2 + 1]));
    pin(keep[0]); pin(keep[1]);
    waitvm<0>();
    u32x4 pp[4];
    if (XF) { if (lane == 0) __hip_atomic_store(flagp(ws, B.tm * 16 + (B.tn ^ 8), 5, w), tag, __ATOMIC_RELAXED, __HIP_MEMORY_SCOPE_AGENT);
      wait_flags(flagp(ws, B.tm * 16 + B.tn, 5, w), lane, 1, tag, 1);
      UNR for (int i = 0; i < 4; i++) pp[i] = ld_dev(rp, lane * 16, i * 1024); pin(pp); }
    else poll(pp, [&](int i) { return ld_dev(rp, lane * 16, i * 1024); });
    STAMP(38);
    const unsigned ot = lds0 + L_OT + w * 4096;
    UNR for (int mm = 0; mm < 2; mm++) UNR for (int n = 0; n < 4; n++) { const u32x4 t = pp[mm * 2 + (n >> 1)]; const f32x4 y = fma4(gate[n], unp(t[(n & 1) * 2], t[(n & 1) * 2 + 1]), keep[mm][n]);
      ldsw<u32x2>(ot + (mm * 16 + r) * 128 + (((n * 2 + (g >> 1)) ^ (r >> 1)) * 16) + (g & 1) * 8, u32x2{cvtpk(y[0], y[1]), cvtpk(y[2], y[3])}); }
    lwait();
    const int r8 = lane >> 3; const unsigned rv0 = r8 * 2048 + (((lane & 7) ^ (r8 >> 1)) * 16);
    char* op = (char*)A.out + ((size_t)(B.tm * 128 + wm * 64 + kh * 32) * D_ + cg * 64) * 2;
    UNR for (int i = 0; i < 4; i++) { const u32x4 t = ldsr<u32x4>(ot + i * 1024 + lane * 16);
      if (OS == 0) *(u32x4*)(op + i * 16384 + ((i & 1) ? rv0 ^ 64 : rv0)) = t;
      else __builtin_amdgcn_raw_buffer_store_b128(t, outrsrc(A.out, 0).r, (unsigned)(op - (char*)A.out) + i * 16384 + ((i & 1) ? rv0 ^ 64 : rv0), 0, OS); }
    STAMP(39);
  }
}

__global__ __launch_bounds__(512, 2) void k_block(Args A) {
  __shared__ __attribute__((aligned(1024))) char smem[L_TOT];
  const int w = __builtin_amdgcn_readfirstlane(threadIdx.x >> 6);
  const unsigned lds0 = (unsigned)(size_t)(LAS char*)smem;
  asm volatile(".Lcode_begin:");
  if (w == 3) touch_code(threadIdx.x & 63, lds0 + L_DUM);
  unsigned ts = 0; STAMP(0);
  const u64 tag = phase0(A, w, lds0, ts);
  if (w >= 4) loader_role(A, w - 4, lds0, tag, ts); else compute_role(A, w, lds0, tag, ts);
}

extern "C" void kernel_launch(void* const* d_in, const int* in_sizes, int n_in, void* d_out, int out_size, void* d_ws, size_t ws_size, hipStream_t stream) {
  Args A; A.x = (const u16*)d_in[0]; A.wqkv = (const u16*)d_in[1]; A.wphi = (const u16*)d_in[2]; A.wo = (const u16*)d_in[3]; A.w1 = (const u16*)d_in[5]; A.b1 = (const u16*)d_in[6];
  A.w2 = (const u16*)d_in[7]; A.b2 = (const u16*)d_in[8]; A.c = (const u16*)d_in[9]; A.wm = (const u16*)d_in[10]; A.out = (u16*)d_out; A.ws = (char*)d_ws;
  k_block<<<256, 512, 0, stream>>>(A);
}
```

```cpp
#include <hip/hip_runtime.h>
#include <hip/hip_bf16.h>
#include <type_traits>

typedef unsigned short u16;
typedef unsigned u32x4 __attribute__((ext_vector_type(4)));
typedef unsigned u32x2 __attribute__((ext_vector_type(2)));
typedef short s16x4 __attribute__((ext_vector_type(4)));
typedef __bf16 bf16x8 __attribute__((ext_vector_type(8)));
typedef __bf16 bf16x2 __attribute__((ext_vector_type(2)));
typedef float f32x2 __attribute__((ext_vector_type(2)));
typedef float f32x4 __attribute__((ext_vector_type(4)));
#define LAS __attribute__((address_space(3)))
#define DI __device__ __forceinline__
#define SB __builtin_amdgcn_sched_barrier(0)
#define UNR _Pragma("unroll")

constexpr int D_ = 1024, F_ = 3584, MODN = 6 * D_;
constexpr size_t WS_EPOCH = 0;
constexpr size_t WS_FLAG  = 4 << 20;
constexpr size_t WS_GX    = 1 << 18;
constexpr size_t WS_ST1   = 1 << 19;
constexpr size_t WS_MODP  = 3 << 19;
constexpr size_t WS_UV1   = WS_MODP + 4 * 65536;
constexpr size_t WS_ZP    = WS_UV1 + 16 * 12 * 2048;
constexpr size_t WS_UV2   = WS_ZP + (1 << 17);
constexpr size_t WS_ST2   = WS_UV2 + (1 << 19);
constexpr size_t WS_XCH   = 6 << 20;
constexpr size_t WS_KV1   = 10 << 20;
constexpr size_t WS_KV2   = WS_KV1 + (2 << 20);
constexpr size_t WS_O     = 14 << 20;
constexpr size_t WS_X1    = 18 << 20;
constexpr size_t WS_H1    = 22 << 20;
constexpr size_t WS_STAMP = 40 << 20;
constexpr int SPIN_CAP = 1 << 14;
typedef unsigned long long u64;

#define STAMP(i) do { } while (0)

DI void glds16(const void* sbase, unsigned voff, unsigned ldsaddr) {
  asm volatile("s_mov_b32 m0, %2\n\ts_nop 0\n\tglobal_load_lds_dwordx4 %0, %1" :: "v"(voff), "s"(sbase), "s"(ldsaddr) : "memory");
}
template <int IMM> DI void glds16i(const void* sbase, unsigned voff, unsigned ldsbase) {
  asm volatile("s_add_i32 m0, %2, %3\n\ts_nop 0\n\tglobal_load_lds_dwordx4 %0, %1" :: "v"(voff), "s"(sbase), "s"(ldsbase), "i"(IMM) : "memory", "scc");
}
template <int I, int N, class F> DI void sfor(F f) { if constexpr (I < N) { f(std::integral_constant<int, I>()); sfor<I + 1, N>(f); } }
DI void glds16x4(const void* sbase, unsigned voff, unsigned ldsaddr) {
  asm volatile("s_mov_b32 m0, %2\n\ts_nop 0\n\tglobal_load_lds_dwordx4 %0, %1 sc1\n\tglobal_load_lds_dwordx4 %0, %1 offset:1024 sc1\n\t"
               "global_load_lds_dwordx4 %0, %1 offset:2048 sc1\n\tglobal_load_lds_dwordx4 %0, %1 offset:3072 sc1" :: "v"(voff), "s"(sbase), "s"(ldsaddr) : "memory");
}
DI void prio(int p) { if (p) __builtin_amdgcn_s_setprio(3); else __builtin_amdgcn_s_setprio(0); }
DI void touch(const void* sbase, unsigned voff, unsigned ldsdummy) {
  asm volatile("s_mov_b32 m0, %2\n\ts_nop 0\n\tglobal_load_lds_dword %0, %1" :: "v"(voff), "s"(sbase), "s"(ldsdummy) : "memory");
}
DI void touch_any(const void* p, unsigned ldsdummy) {
  asm volatile("s_mov_b32 m0, %1\n\ts_nop 0\n\tglobal_load_lds_dword %0, off" :: "v"(p), "s"(ldsdummy) : "memory");
}
DI void gld_dev(u32x4& r, const void* sb, unsigned vo) { asm volatile("global_load_dwordx4 %0, %1, %2 sc1" : "=v"(r) : "v"(vo), "s"(sb) : "memory"); }
DI void gld4(unsigned& r, const void* sb, unsigned vo) { asm volatile("global_load_dword %0, %1, %2" : "=v"(r) : "v"(vo), "s"(sb) : "memory"); }
template <class T> DI void pin(T& r) { asm volatile("" : "+v"(r)); }
template <class T, int N> DI void pin(T (&r)[N]) { UNR for (int i = 0; i < N; i++) pin(r[i]); }
template <class T> DI T opq(T v) { asm volatile("" : "+s"(v)); return v; }
DI f32x4 asf(u32x4 v) { return __builtin_bit_cast(f32x4, v); }
template <int N> DI void waitvm() { asm volatile("s_waitcnt vmcnt(%0)" :: "i"(N) : "memory"); }
template <int B, int LO = 0, int HI = 12> DI void waitx(int e) {
  if constexpr (LO == HI) waitvm<B + LO>();
  else if constexpr (LO == 0) { if (e == 0) waitvm<B>(); else waitx<B, 1, HI>(e); }
  else { constexpr int MID = (LO + HI + 1) / 2; if (e >= MID) waitx<B, MID, HI>(e); else waitx<B, LO, MID - 1>(e); }
}
DI void waitlgkm0() { __builtin_amdgcn_s_waitcnt(0xC07F); }
DI void lwait() { asm volatile("s_waitcnt lgkmcnt(0)" ::: "memory"); }
DI void bar() { asm volatile("s_barrier" ::: "memory"); }
DI void lbar() { asm volatile("s_waitcnt lgkmcnt(0)\n\ts_barrier" ::: "memory"); }
DI unsigned cvtpk(float a, float b) { f32x2 v = {a, b}; return __builtin_bit_cast(unsigned, __builtin_convertvector(v, bf16x2)); }
DI float rbf(float a) { return __uint_as_float(cvtpk(a, a) << 16); }
DI f32x4 rbf4(f32x4 s) { return f32x4{rbf(s[0]), rbf(s[1]), rbf(s[2]), rbf(s[3])}; }
DI float blo(unsigned u) { return __uint_as_float(u << 16); }
DI float bhi(unsigned u) { return __uint_as_float(u & 0xffff0000u); }
DI f32x4 unp(unsigned a, unsigned b) { return f32x4{blo(a), bhi(a), blo(b), bhi(b)}; }
DI u32x4 pack8u(const f32x4& a, const f32x4& b) { return u32x4{cvtpk(a[0], a[1]), cvtpk(a[2], a[3]), cvtpk(b[0], b[1]), cvtpk(b[2], b[3])}; }
DI bf16x8 pack8(const f32x4& a, const f32x4& b) { return __builtin_bit_cast(bf16x8, pack8u(a, b)); }
DI f32x4 mfma(bf16x8 a, bf16x8 b, f32x4 c) { return __builtin_amdgcn_mfma_f32_16x16x32_bf16(a, b, c, 0, 0, 0); }
DI s16x4 trd(unsigned a) { return __builtin_amdgcn_ds_read_tr16_b64_v4i16((LAS s16x4*)(size_t)a); }
DI bf16x8 cat(s16x4 lo, s16x4 hi) { return __builtin_bit_cast(bf16x8, __builtin_shufflevector(lo, hi, 0, 1, 2, 3, 4, 5, 6, 7)); }
template <class T> DI T ldsr(unsigned a) { return *(const LAS T*)(size_t)a; }
template <class T> DI void ldsw(unsigned a, T v) { *(LAS T*)(size_t)a = v; }
template <int C> DI float dpp(float x) { return __builtin_bit_cast(float, __builtin_amdgcn_update_dpp(0, __builtin_bit_cast(int, x), C, 0xf, 0xf, true)); }
DI float vmax(float a, float b) { float r; asm("v_max_f32_e32 %0, %1, %2" : "=v"(r) : "v"(a), "v"(b)); return r; }
DI float sw32_add(float a, float b) { auto r = __builtin_amdgcn_permlane32_swap(__float_as_uint(a), __float_as_uint(b), false, false); return __uint_as_float(r[0]) + __uint_as_float(r[1]); }
DI float sw16_add(float a, float b) { auto r = __builtin_amdgcn_permlane16_swap(__float_as_uint(a), __float_as_uint(b), false, false); return __uint_as_float(r[0]) + __uint_as_float(r[1]); }
DI float xg_sum(float a) { a = sw32_add(a, a); return sw16_add(a, a); }
DI float xg_max(float a) {
  auto r = __builtin_amdgcn_permlane32_swap(__float_as_uint(a), __float_as_uint(a), false, false); a = vmax(__uint_as_float(r[0]), __uint_as_float(r[1]));
  auto q = __builtin_amdgcn_permlane16_swap(__float_as_uint(a), __float_as_uint(a), false, false); return vmax(__uint_as_float(q[0]), __uint_as_float(q[1]));
}
DI float row16_sum(float a) { a += dpp<0x128>(a); a += dpp<0x124>(a); a += dpp<0x122>(a); a += dpp<0x121>(a); return a; }
DI f32x4 row16_sum4(f32x4 a) { return f32x4{row16_sum(a[0]), row16_sum(a[1]), row16_sum(a[2]), row16_sum(a[3])}; }
DI f32x4 spl(float a) { return f32x4{a, a, a, a}; }
DI f32x4 fma4(f32x4 a, f32x4 b, f32x4 c) { return __builtin_elementwise_fma(a, b, c); }
struct RowSt { float rs, nm; DI void set(unsigned a, float k) { rs = ldsr<float>(a) * k; nm = ldsr<float>(a + 512) * k; asm("" : "+v"(rs)); asm("" : "+v"(nm)); } };
DI f32x4 lnf(f32x4 acc, const RowSt& st, f32x4 u, f32x4 v) { return fma4(acc, spl(st.rs), fma4(u, spl(st.nm), v)); }
DI float fadd(float a, float b) { float r; asm("v_add_f32_e32 %0, %1, %2" : "=v"(r) : "v"(a), "v"(b)); return r; }
DI float hsum4(f32x4 a) { const f32x2 t = f32x2{a[0], a[1]} + f32x2{a[2], a[3]}; float r; asm("v_add_f32_e32 %0, %1, %2" : "=v"(r) : "v"(t[0]), "v"(t[1])); return r; }
struct rsrc_t { __amdgpu_buffer_rsrc_t r; unsigned base; };
DI rsrc_t mkrsrc(const char* ws, size_t off) { return rsrc_t{__builtin_amdgcn_make_buffer_rsrc((void*)ws, 0, 64 << 20, 0x00020000), (unsigned)off}; }
DI rsrc_t outrsrc(const void* out, size_t off) { return rsrc_t{__builtin_amdgcn_make_buffer_rsrc((void*)out, 0, 2048 * D_ * 2, 0x00020000), (unsigned)off}; }
DI unsigned xchoff(int tm, int tn, int w) { return WS_XCH + (tm * 16 + tn) * 16384 + w * 4096; }
DI u32x4 ld_dev(const rsrc_t& r, unsigned voff, unsigned soff) { return __builtin_amdgcn_raw_buffer_load_b128(r.r, voff, r.base + soff, 16); }
DI void st_dev(u32x4 v, const rsrc_t& r, unsigned voff, unsigned soff) { __builtin_amdgcn_raw_buffer_store_b128(v, r.r, voff + (r.base + soff), 0, 16); }
DI void st_dev8(u32x2 v, const rsrc_t& r, unsigned voff, unsigned soff) { __builtin_amdgcn_raw_buffer_store_b64(v, r.r, voff + (r.base + soff), 0, 16); }
DI unsigned umax4(u32x4 v) { return max(max(v[0], v[1]), max(v[2], v[3])); }
template <int N, class F> DI void poll(u32x4 (&v)[N], F ld) {
  _Pragma("nounroll") for (int spin = 0; spin < SPIN_CAP; spin++) { unsigned m = 0;
    UNR for (int i = 0; i < N; i++) v[i] = ld(i);
    UNR for (int i = 0; i < N; i++) m = max(m, umax4(v[i]));
    if (!__any(m == 0xffffffffu)) break;
    asm volatile("s_sleep 2" ::: "memory");
  }
}
#define RA 0
#define W1M 12
#define XA2 3
#define MS 0
#define QS 0
#define MW 0
#define TV 2
#define SLOW 0
DI float fmax3(float a, float b, float c) { return __builtin_elementwise_maximum(__builtin_elementwise_maximum(a, b), c); }
struct Blk { int tm, tn, rot, j, xq; };
DI Blk blk() { const int xcd = blockIdx.x & 7, j = blockIdx.x >> 3; Blk b; b.tm = (xcd >> 2) * 8 + (j >> 2); b.tn = (xcd & 3) * 4 + (j & 3); b.rot = (b.tm & 7) * 2 + (b.tm >> 3); b.j = j; b.xq = xcd & 3; return b; }
DI Blk blk1() { if (XA2) { Blk b = blk(); b.rot = b.tm; return b; } const int xcd = blockIdx.x & 7, j = blockIdx.x >> 3; Blk b; b.tm = j >> 1; b.tn = xcd * 2 + (j & 1); b.rot = b.tm; b.j = j; b.xq = xcd; return b; }
DI Blk blk0() { const int xcd = blockIdx.x & 7, j = blockIdx.x >> 3; Blk b; b.tn = j >> 1; b.tm = xcd * 2 + (j & 1); b.rot = 0; b.j = j; b.xq = xcd; return b; }
DI unsigned ptile(int c, int lane) { const int row = 4 * (lane >> 4) + ((lane & 15) >> 2); return row * 128 + ((c ^ ((row >> 1) & 3)) * 32) + (lane & 3) * 8; }
DI unsigned htile(int c, int lane) { const int row = 4 * (lane >> 4) + ((lane & 15) >> 2); return row * 64 + ((c ^ ((row >> 2) & 1)) * 32) + (lane & 3) * 8; }
DI bf16x8 wfrag(unsigned a, unsigned ts) { return cat(trd(a), trd(a + ts)); }

DI unsigned silu2(unsigned cw) { const float c0 = blo(cw), c1 = bhi(cw); return cvtpk(c0 * __builtin_amdgcn_rcpf(1.f + __expf(-c0)), c1 * __builtin_amdgcn_rcpf(1.f + __expf(-c1))); }
DI unsigned fragpos(int k) { return (k >> 5) * 64 + ((k >> 2) & 3) * 16 + ((k & 3) + 4 * ((k >> 4) & 1)) * 2; }
DI f32x4 gemv64(unsigned a, bf16x8 s0, bf16x8 s1, int lane) { f32x4 d[4];
  UNR for (int cc = 0; cc < 4; cc++) d[cc] = mfma(wfrag(a + 4096 + ptile(cc, lane), 2048), s1, mfma(wfrag(a + ptile(cc, lane), 2048), s0, f32x4{0, 0, 0, 0}));
  const bool b0 = lane & 1, b1 = lane & 2; f32x4 o;
  UNR for (int k = 0; k < 4; k++) { const float lo = b0 ? d[1][k] : d[0][k], hi = b0 ? d[3][k] : d[2][k]; o[k] = b1 ? hi : lo; }
  return o;
}

template <int CFG> struct Cfg;
template <> struct Cfg<0> { static constexpr int NT = 6, NP = 3, HALF = 0, NB = 2, NS = 3, N = 3 * D_, KT = 16, B0 = 49152, BS0 = 0, OWN = 0; };
template <> struct Cfg<1> { static constexpr int NT = 7, NP = 3, HALF = 1, NB = 3, NS = 3, N = F_, KT = 16, B0 = 65536, BS0 = 2, OWN = 1; };
template <> struct Cfg<2> { static constexpr int NT = 2, NP = 1, HALF = 0, NB = 2, NS = 4, N = D_, KT = 16, B0 = 73728, BS0 = 0, OWN = 0; };
template <> struct Cfg<3> { static constexpr int NT = 4, NP = 2, HALF = 0, NB = 4, NS = 4, N = D_, KT = 28, B0 = 65536, BS0 = 0, OWN = 3; };
template <int CFG> struct Lay { using C = Cfg<CFG>; static constexpr int SBB = C::NP * 8192 + C::HALF * 4096, PB = 2 * C::NP + C::HALF, PER = 4 + PB; };
template <int CFG> DI unsigned btile(int tl, int lane) { using C = Cfg<CFG>; return C::B0 + (tl < 4 * C::NP ? (tl >> 2) * 8192 + ptile(tl & 3, lane) : C::NP * 8192 + htile(tl & 1, lane)); }
constexpr int L_RES = 106496, L_WPHI = 155648, L_UVT = L_WPHI + 4096, L_STT = L_UVT + 2048, L_ET = L_STT + 1024, L_GT = L_ET + 256, L_DUM = L_GT + 512, L_TOT = 163840, L_REST = 122880, L_OT = 131072;
constexpr int L_UN = 0, L_XT = 131072, L_RED = L_WPHI, L_SROW = L_RED + 2048, L_SIL2 = L_GT;
constexpr int L_GB = L_WPHI;
constexpr int L_VF = 0, L_TR = 16384, L_KX = 49152, L_RV = L_RES;

template <int N> DI void waitlgkm() { __builtin_amdgcn_s_waitcnt(0xC07F | (N << 8)); }
template <int CFG, class H> DI void compute_loop(f32x4 (&acc)[4][Cfg<CFG>::NT], unsigned lds0, int wm, int wn, int lane, unsigned& ts, H side) {
  using C = Cfg<CFG>; using L = Lay<CFG>; constexpr int NT = C::NT, NB = C::NB, KT = C::KT, NU = 1 + NB;
  bf16x8 fa[2][4][2]; s16x4 fb[4][2][2]; unsigned va = lds0 + wm * 8192 + lane * 16, vb[NB], rawk = 0, rawv = 0;
  UNR for (int i = 0; i < NB; i++) vb[i] = lds0 + C::BS0 * L::SBB + btile<CFG>(CFG == 1 ? (i < 2 ? wn * 2 + i : 12 + wn) : wn * NB + i, lane);
  pin(va); pin(vb);
  auto bbuf = [](int par, int n) { return NT == 2 ? par * 2 + n : NT == 4 ? n : n == 6 ? 3 : n % 3; };
  auto rdb = [&](int par, int n, int i) { const int ks = i >> 1, t = i & 1;
    fb[bbuf(par, n)][ks][t] = trd(CFG == 1 && n == 6 ? vb[NB - 1] + ks * 2048 + t * 1024 : CFG <= 1 ? vb[n & 1] + (n >> 1) * 8192 + ks * 4096 + t * 2048 : vb[n % NB] + ks * 4096 + t * 2048); };
  auto rda = [&](int par, int i) { fa[par][i >> 1][i & 1] = ldsr<bf16x8>(va + i * 1024); };
  int sa = 0;
  auto stage = [&](int par, int t, bool first, bool last) {
    int da = 0, db = 0;
    auto adv = [&](int j) { if (j == 0) { sa = opq(sa) + 1; da = 16384; if (sa == C::NS) { sa = 0; da -= C::NS * 16384; } da = opq(da); if (C::BS0 == 0 && L::SBB == 16384) db = da; }
                            else if (C::BS0 == 0) { if (L::SBB != 16384) db = opq(opq(sa) == 0 ? L::SBB - C::NS * L::SBB : L::SBB); } else db = opq(opq(sa) == C::NS - C::BS0 ? L::SBB - C::NS * L::SBB : L::SBB); };
    auto upd = [&](int u) { if (u == 0) { va += da; pin(va); } else if (u < NU) { vb[u - 1] += db; pin(vb[u - 1]); } };
    UNR for (int n = 0; n < NT; n++) {
      if (n == NT - 2) {
        if (NT == 2 && !last) { adv(0); adv(1); UNR for (int u = 0; u < NU; u++) upd(u); }
        waitlgkm<0>();
        if (!last) {
          if (C::OWN && t == C::OWN - 1) { if (CFG == 3) { waitvm<0>(); STAMP(32); bar(); } bar(); }
          side(t);
          bar();
        }
        SB;
      } else if (n < NT - 2) { waitlgkm<4>(); SB; }
      UNR for (int i = 0; i < 8; i++) {
        const int m = (n & 1) ? 3 - (i >> 1) : i >> 1, ks = (i ^ (i >> 1)) & 1;
        const bf16x8 b = cat(fb[bbuf(par, n)][ks][0], fb[bbuf(par, n)][ks][1]); const f32x4 cin = first && !(i & 1) ? f32x4{0, 0, 0, 0} : acc[m][n];
        const bf16x8 o0 = (CFG == 0 && n >= 4) ? fa[par][m][ks] : b, o1 = (CFG == 0 && n >= 4) ? b : fa[par][m][ks];
        if (first && !(i & 1)) asm volatile("v_mfma_f32_16x16x32_bf16 %0, %1, %2, 0" : "=&v"(acc[m][n]) : "v"(o0), "v"(o1));
        else if (!last) asm volatile("v_mfma_f32_16x16x32_bf16 %0, %1, %2, %0" : "+v"(acc[m][n]) : "v"(o0), "v"(o1)); else acc[m][n] = mfma(o0, o1, cin);
        SB;
        if (n < NT - 3) { if (!(i & 1)) rdb(par, n + 2, i >> 1); else if (!last) { if (n == 0 && i < 4) adv(i >> 1); if (n == NT - 4 && 3 - (i >> 1) < NU - 4) upd(3 - (i >> 1)); } }
        else if (n == NT - 3) { if (i < 4) rdb(par, n + 2, i); else if (!last) upd(i - 4 + (NU > 4 ? NU - 4 : 0)); }
        else if (!last) { if (n == NT - 2) { if (i < 4) rdb(par ^ 1, 0, i); else rda(par ^ 1, i - 4); } else { if (i < 4) rda(par ^ 1, i + 4); else rdb(par ^ 1, 1, i - 4); } }
        if (CFG == 0 && last && n >= 3 && i >= 4 && i < 6) { const int hm = i - 4, hn = n - 1;
          ldsw<f32x4>(hn < 4 ? rawk + (hn & 1) * 4096 + hm * 1024 : rawv + hm * 2048, acc[hm][hn % NT]); }
        SB;
      }
    }
    if (CFG == 0 && last) UNR for (int hm = 1; hm >= 0; hm--) ldsw<f32x4>(rawv + hm * 2048 + 1024, acc[hm][5 % NT]);
  };
  SB; lbar();
  STAMP(CFG == 0 ? 12 : CFG == 2 ? 22 : CFG == 1 ? 28 : 36);
  UNR for (int i = 0; i < 4; i++) rdb(0, 0, i);
  UNR for (int i = 0; i < 8; i++) rda(0, i);
  UNR for (int i = 0; i < 4; i++) rdb(0, 1, i);
  SB;
  stage(0, 0, true, false);
  _Pragma("nounroll") for (int t = 1; t < KT - 1; t += 2) { stage(1, t, false, false); stage(0, t + 1, false, false); }
  if (CFG == 0) { rawk = lds0 + L_TR + (wm * 2 + wn) * 8192 + lane * 16; rawv = lds0 + L_RV + (wm * 2 + wn) * 4096 + lane * 16; pin(rawk); pin(rawv); SB; }
  stage(1, KT - 1, false, true);
}

template <int CFG> struct Loader {
  using C = Cfg<CFG>; using L = Lay<CFG>; static constexpr int N = C::N, KT = C::KT, PER = L::PER, PB = L::PB, NS = C::NS, OWN = C::OWN;
  const char *ab, *bb, *hb; unsigned av, bv, hv, la, lb, lh; int ka, kb, own;
  int r0;
  DI int nxt(int k) const { if (CFG > 1 || (CFG == 0 && !XA2)) return k + 1 == KT ? 0 : k + 1; const int n = (k & 8) | ((k + 1) & 7); return n == r0 ? (r0 ^ 8) & (CFG == 0 && XA2 == 2 ? 14 : CFG == 0 && XA2 == 3 ? 15 : W1M) : n; }
  DI int skip(int k) const { if (OWN && k == own) k = nxt(k + OWN - 1); return k; }
  DI void init(const void* A, int akt, const u16* W, int k0, int col0, int hcol, int rot, int own_, int tm, int lw, int lane, unsigned lds0) {
    const int r = lane >> 3, hr = lane >> 2;
    ab = (const char*)A + ((size_t)tm * akt + k0) * 16384 + lw * 4096; av = lane * 16;
    bb = (const char*)W + ((size_t)(k0 * 64 + lw * 16) * N + col0) * 2; bv = r * N * 2 + (((lane & 7) ^ (((r >> 1) & 3) << 1)) * 16);
    hb = (const char*)W + ((size_t)(lw * 16) * N + hcol) * 2; hv = hr * N * 2 + (((lane & 3) ^ (((hr >> 2) & 1) << 1)) * 16);
    if (CFG == 3) { const int t = lw & 1; bb -= (size_t)t * 16 * N * 2; hv = bv + (4 * t + 4 * (r >> 2)) * N * 2; bv += t * 16 * N * 2; }
    la = lds0 + lw * 4096; lb = lds0 + C::B0 + lw * 2048; lh = lds0 + C::B0 + C::NP * 8192 + lw * 1024; own = own_; r0 = rot; ka = kb = skip(rot);
  }
  DI void issueA(int slot) { glds16x4(ab + (size_t)ka * 16384, av, la + slot * 16384); ka = skip(nxt(ka)); }
  DI void issueBk(int slot, int k) const { const char* b = bb + (size_t)k * 64 * N * 2; slot += C::BS0; if (slot >= NS) slot -= NS; const unsigned so = slot * L::SBB;
    const bool nat = CFG == 3 && ((0x1020408u >> k) & 1);
    const unsigned v0 = nat ? hv : bv, v1 = v0 + (nat ? 16 : 8) * N * 2;
    const unsigned l0 = lb + so;
    sfor<0, 2 * C::NP>([&](auto I) { constexpr int i = I.value; glds16i<(i >> 1) * 8192 + (i & 1) * 1024>(b + (i >> 1) * (CFG == 0 ? 2048 : 128) + (CFG == 3 ? 0 : (i & 1) * (8 * N * 2)), CFG == 3 && (i & 1) ? v1 : v0, l0); });
    if (C::HALF) glds16(hb + (size_t)k * 64 * N * 2, hv, lh + so);
  }
  DI void issueB(int slot) { issueBk(slot, kb); kb = skip(nxt(kb)); }
  DI void issue(int slot) { issueA(slot); issueB(slot); }
  DI u64 first_need(int n, int k0) const { u64 m = 0; int k = ka;
    for (int i = 0; i < n; i++) { if (CFG == 3) m |= (1ull << ((2 * (k0 + k)) / 7)) | (1ull << ((2 * (k0 + k) + 1) / 7)); else m |= 1ull << k; k = skip(nxt(k)); }
    return m; }
  template <int E1, class HF, class HK, class H1> DI void run3(HF hf, HK hk, H1 h1) {
    int e0 = 0;
    UNR for (int t = 1; t <= 5; t++) { issue((t - 1) % 3); const int e1 = hf(t); waitx<PER>(e0 + e1); bar(); e0 = e1; }
    int slot = 2;
    _Pragma("nounroll") for (int t = 6; t <= KT - 3; t++) { issue(slot); slot++; if (slot == NS) slot = 0; const int e1 = hk(t); waitx<PER, 0, 9>(e0 + e1); bar(); e0 = e1; }
    h1(); waitvm<E1>(); bar();
  }
  template <int E1, bool W1, class H0, class H1, class HK> DI void run4(H0 h0, H1 h1, HK hk) {
    UNR for (int s = 0; s < 4; s++) issueA((OWN + s) & 3);
    waitvm<12>(); bar(); h0(); waitvm<8>(); bar();
    issue(OWN & 3); waitvm<4 + PER>(); bar();
    int slot = (OWN + 1) & 3, e0 = 0, e1 = 0;
    for (int t = 2; t <= KT - OWN - 4; t++) { issue(slot); slot = (slot + 1) & 3;
      const int e2 = hk(t); waitx<2 * PER, 0, 7>(e0 + e1 + e2); e0 = e1; e1 = e2; bar(); }
    if (W1) { h1(); waitvm<PER + E1>(); bar(); waitvm<0>(); bar(); } else { waitvm<PER>(); bar(); h1(); waitvm<E1>(); bar(); }
  }
};
DI u64* flagp(char* ws, int tile, int line, int word) { return (u64*)(ws + WS_FLAG) + (tile * 8 + line) * 16 + word; }
DI u64 wait_flags(u64* f, int lane, int n, u64 tag, u64 need) {
  u64 got = 0;
  _Pragma("nounroll") for (int spin = 0; spin < SPIN_CAP; spin++) { u64 v = tag;
    if (lane < n) asm volatile("global_atomic_or_x2 %0, %1, %2, off sc0 sc1\n\ts_waitcnt vmcnt(0)" : "=&v"(v) : "v"(f + lane), "v"(0ull) : "memory");
    got = __ballot(v == tag);
    if ((got & need) == need) break;
    asm volatile("s_sleep 1" ::: "memory");
  }
  return got;
}
struct Sync { u64* f; int n; u64 tag; bool all;
  template <class H> DI void run(char* ws, int line, const Blk& P, const Blk& C, int lw, int lane, u64 tag_, u64 need, H pre) {
    bar();
    tag = tag_; f = flagp(ws, C.tm * 16 + C.tn, line, 0); n = line == 0 ? 32 : 16; all = true;
    if (lw == 0) { const int i = lane & 15, grp = lane >> 4;
      if (grp < n / 16) __hip_atomic_store(grp == 0 ? flagp(ws, P.tm * 16 + i, line, P.tn) : flagp(ws, i * 16 + C.tn, 1, C.tm), tag, __ATOMIC_RELAXED, __HIP_MEMORY_SCOPE_AGENT); }
    pre();
    if (lw == 0) all = wait_flags(f, lane, n, tag, need) == ~0ull;
    lbar();
  }
  DI void rest(int lane) { if (!all) wait_flags(f, lane, n, tag, ~0ull); }
};
DI int modcol(int lane) { return 4 * (lane & 15) + 2 * (lane >> 5) + ((lane >> 4) & 1); }
DI float modvec(const char* ws, unsigned part, int lane, u32x4 (&p)[4], bool have) {
  if (!have || __any(max(max(umax4(p[0]), umax4(p[1])), max(umax4(p[2]), umax4(p[3]))) == 0xffffffffu)) { const rsrc_t r = mkrsrc(ws, part); poll(p, [&](int i) { return ld_dev(r, lane * 16, i * 1024); }); }
  const f32x4 a = (asf(p[0]) + asf(p[1])) + (asf(p[2]) + asf(p[3]));
  return rbf(sw16_add(sw32_add(a[0], a[2]), sw32_add(a[1], a[3])));
}
DI float modvec(const char* ws, unsigned part, int lane) { u32x4 p[4]; return modvec(ws, part, lane, p, false); }
DI void put_et(unsigned lds0, int vec, int k, float v) { ldsw<u16>(lds0 + L_ET + vec * 128 + fragpos(k), (u16)(__float_as_uint(v) >> 16)); }

template <int CFG, int NTL> struct UV {
  u32x4 o; unsigned oa;
  DI void form(const rsrc_t& r, int tm, int lw, int lane, unsigned lds0, unsigned slotoff) {
    const bf16x8 e0 = ldsr<bf16x8>(lds0 + L_ET + (lane & 1) * 128 + (lane >> 4) * 16), e1 = ldsr<bf16x8>(lds0 + L_ET + (lane & 1) * 128 + 64 + (lane >> 4) * 16);
    f32x4 d[NTL]; const int i2 = min((lane & 15) >> 1, NTL - 1);
    UNR for (int i = 0; i < NTL; i++) { const int tl = min(lw * NTL + i, 2 * Cfg<CFG>::NT - 1); const unsigned a = lds0 + slotoff + btile<CFG>(tl, lane), ts = tl < 4 * Cfg<CFG>::NP ? 2048 : 1024;
      d[i] = mfma(wfrag(a + 2 * ts, ts), e1, mfma(wfrag(a, ts), e0, f32x4{0, 0, 0, 0})); }
    UNR for (int k = 0; k < 4; k++) { float t = d[0][k]; UNR for (int i = 1; i < NTL; i++) t = i2 == i ? d[i][k] : t; o[k] = __float_as_uint(t); }
    oa = r.base + (lw * NTL + i2) * 2048 + tm * 128 + (lane & 1) * 64 + (lane >> 4) * 16; pin(o); pin(oa);
  }
  DI void send(const char* ws) { st_dev(o, mkrsrc(ws, 0), oa, 0); }
  u32x4 v[2 * NTL];
  DI void fetch(const char* base, int lw, int lane) { UNR for (int i = 0; i < 2 * NTL; i++) gld_dev(v[i], base + lw * NTL * 2048 + i * 1024, lane * 16); }
  DI void finish(const rsrc_t& r, int lw, int lane, unsigned tab) {
    pin(v); unsigned mx = 0;
    UNR for (int i = 0; i < 2 * NTL; i++) mx = max(mx, umax4(v[i]));
    if (__any(mx == 0xffffffffu)) poll(v, [&](int i) { return ld_dev(r, lane * 16, lw * NTL * 2048 + i * 1024); });
    UNR for (int i = 0; i < NTL; i++) { const f32x4 a = asf(v[2 * i]) + asf(v[2 * i + 1]);
      float t = sw16_add(sw32_add(a[0], a[2]), sw32_add(a[1], a[3])); t += dpp<0x128>(t);
      ldsw<float>(tab + (lw * NTL + i) * 128 + (lane & 7) * 16 + (2 * (lane >> 5) + ((lane >> 4) & 1)) * 4, t); }
  }
};
template <int NPART> struct Stats {
  u32x4 v[NPART / 4];
  DI void fetch(int i, const char* st, int tm, int lw, int lane) { gld_dev(v[i], st + (size_t)(tm * NPART + i) * 1024 + lw * 256, (lane >> 4) * (NPART * 256) + (lane & 15) * 16); }
  DI void check(const char* ws, unsigned st, int tm, int lw, int lane) { pin(v); unsigned mx = 0;
    UNR for (int i = 0; i < NPART / 4; i++) mx = max(mx, umax4(v[i]));
    if (__any(mx == 0xffffffffu)) poll(v, [&](int i) { return ld_dev(mkrsrc(ws, st + (tm * NPART + i) * 1024 + lw * 256), (lane >> 4) * (NPART * 256) + (lane & 15) * 16, 0); }); }
  DI void finish(int lw, int lane, unsigned tab) { pin(v); f32x4 s = asf(v[0]); float q0 = 0, q1 = 0;
    UNR for (int i = 1; i < NPART / 4; i++) s += asf(v[i]);
    float t = sw32_add(s[0], s[2]); t = sw16_add(t, t) * (1.f / NPART);
    const auto mm = __builtin_amdgcn_permlane32_swap(__float_as_uint(t), __float_as_uint(t), false, false);
    UNR for (int i = 0; i < NPART / 4; i++) { const float d0 = __uint_as_float(v[i][0]) - __uint_as_float(mm[0]), d1 = __uint_as_float(v[i][2]) - __uint_as_float(mm[1]); q0 = __builtin_fmaf(d0, d0, q0); q1 = __builtin_fmaf(d1, d1, q1); }
    float q = sw32_add(__builtin_fmaf((float)(D_ / NPART), q0, s[1]), __builtin_fmaf((float)(D_ / NPART), q1, s[3])); q = sw16_add(q, q);
    const float rstd = __builtin_amdgcn_rsqf(q * (1.f / D_) + 1e-5f);
    ldsw<float>(tab + ((lane >> 4) & 1) * 512 + (lw * 32 + (lane & 15) * 2 + (lane >> 5)) * 4, (lane & 16) ? -t * rstd : rstd); }
};

template <bool MAX> DI float fold4(float a, float b, float c, float d) {
  auto op = [](unsigned x, unsigned y) { return MAX ? __builtin_elementwise_maximum(__uint_as_float(x), __uint_as_float(y)) : __uint_as_float(x) + __uint_as_float(y); };
  const auto r0 = __builtin_amdgcn_permlane32_swap(__float_as_uint(a), __float_as_uint(b), false, false), r1 = __builtin_amdgcn_permlane32_swap(__float_as_uint(c), __float_as_uint(d), false, false);
  const auto r2 = __builtin_amdgcn_permlane16_swap(__float_as_uint(op(r0[0], r0[1])), __float_as_uint(op(r1[0], r1[1])), false, false);
  return op(r2[0], r2[1]);
}
DI void spread4(float t, float& a, float& b, float& c, float& d) {
  const auto r2 = __builtin_amdgcn_permlane16_swap(__float_as_uint(t), __float_as_uint(t), false, false);
  const auto r0 = __builtin_amdgcn_permlane32_swap(r2[0], r2[0], false, false), r1 = __builtin_amdgcn_permlane32_swap(r2[1], r2[1], false, false);
  a = __uint_as_float(r0[0]); b = __uint_as_float(r0[1]); c = __uint_as_float(r1[0]); d = __uint_as_float(r1[1]);
}
DI void feat2(const bf16x8 (&wf)[2], bf16x8 x0, bf16x8 x1, bf16x8& fp0, bf16x8& fn0, bf16x8& fp1, bf16x8& fn1) {
  const f32x4 zero = {0, 0, 0, 0}; f32x4 p[2][2] = {{mfma(wf[0], x0, zero), mfma(wf[1], x0, zero)}, {mfma(wf[0], x1, zero), mfma(wf[1], x1, zero)}};
  float mx[2], nx[2], sp[2], sn[2]; f32x4 a[2][2], b[2][2];
  UNR for (int i = 0; i < 2; i++) { const f32x4 u = p[i][0], v = p[i][1];
    mx[i] = fmax3(fmax3(fmax3(u[0], u[1], u[2]), u[3], v[0]), fmax3(v[1], v[2], v[3]), v[3]); nx[i] = fmax3(fmax3(fmax3(-u[0], -u[1], -u[2]), -u[3], -v[0]), fmax3(-v[1], -v[2], -v[3]), -v[3]); }
  spread4(fold4<true>(mx[0], mx[1], nx[0], nx[1]), mx[0], mx[1], nx[0], nx[1]);
  UNR for (int i = 0; i < 2; i++) UNR for (int h = 0; h < 2; h++) UNR for (int j = 0; j < 4; j += 2) { const f32x2 t = {p[i][h][j], p[i][h][j + 1]};
    f32x2 d = t - f32x2{mx[i], mx[i]}, e = -t - f32x2{nx[i], nx[i]}; asm("" : "+v"(d), "+v"(e));
    a[i][h][j] = __builtin_amdgcn_exp2f(d[0]); a[i][h][j + 1] = __builtin_amdgcn_exp2f(d[1]); b[i][h][j] = __builtin_amdgcn_exp2f(e[0]); b[i][h][j + 1] = __builtin_amdgcn_exp2f(e[1]); }
  UNR for (int i = 0; i < 2; i++) { sp[i] = hsum4(a[i][0] + a[i][1]); sn[i] = hsum4(b[i][0] + b[i][1]); }
  spread4(__builtin_amdgcn_rcpf(fold4<false>(sp[0], sp[1], sn[0], sn[1])), sp[0], sp[1], sn[0], sn[1]);
  fp0 = pack8(a[0][0] * sp[0], a[0][1] * sp[0]); fn0 = pack8(b[0][0] * sn[0], b[0][1] * sn[0]); fp1 = pack8(a[1][0] * sp[1], a[1][1] * sp[1]); fn1 = pack8(b[1][0] * sn[1], b[1][1] * sn[1]);
}

constexpr float L2E = 1.4426950408889634f;
DI void kv_prep(const f32x4 (&k)[2][2], const f32x4 (&vv)[2][2], const bf16x8 (&wf)[2], int hf, int w, int lane, unsigned lds0) {
  const int g = lane >> 4, r = lane & 15, row0 = (w >> 1) * 64 + hf * 32; const unsigned uva = lds0 + L_UVT + (w & 1) * 256;
  RowSt st[2]; float u[2], v[2]; f32x4 rs[2], nm[2];
  UNR for (int m = 0; m < 2; m++) st[m].set(lds0 + L_STT + (row0 + m * 16 + r) * 4, L2E);
  const f32x4 u0 = ldsr<f32x4>(uva + 512 + g * 16), v0 = ldsr<f32x4>(uva + 576 + g * 16) * L2E, u1 = ldsr<f32x4>(uva + 640 + g * 16), v1 = ldsr<f32x4>(uva + 704 + g * 16) * L2E;
  UNR for (int c = 0; c < 2; c++) { u[c] = ldsr<float>(uva + 1024 + c * 128 + r * 4); v[c] = ldsr<float>(uva + 1088 + c * 128 + r * 4); asm("" : "+v"(u[c])); asm("" : "+v"(v[c])); }
  UNR for (int m = 0; m < 2; m++) { const unsigned a = lds0 + L_STT + (row0 + m * 16 + 4 * g) * 4; rs[m] = ldsr<f32x4>(a); nm[m] = ldsr<f32x4>(a + 512); }
  bf16x8 a, b, c, d; feat2(wf, pack8(lnf(k[0][0], st[0], u0, v0), lnf(k[0][1], st[0], u1, v1)), pack8(lnf(k[1][0], st[1], u0, v0), lnf(k[1][1], st[1], u1, v1)), a, b, c, d);
  UNR for (int cc = 0; cc < 2; cc++) ldsw<bf16x8>(lds0 + L_VF + w * 4096 + hf * 2048 + cc * 1024 + lane * 16,
      pack8(fma4(vv[0][cc], rs[0], fma4(nm[0], spl(u[cc]), spl(v[cc]))), fma4(vv[1][cc], rs[1], fma4(nm[1], spl(u[cc]), spl(v[cc])))));
  const unsigned trw = lds0 + L_TR + w * 8192 + hf * 2048 + r * 64 + ((g ^ ((r >> 1) & 3)) * 16);
  ldsw<bf16x8>(trw, a); ldsw<bf16x8>(trw + 4096, b); ldsw<bf16x8>(trw + 1024, c); ldsw<bf16x8>(trw + 1024 + 4096, d);
}

DI void touch_code(int lane, unsigned ldsdummy) {
  const char* pc; unsigned len, back;
  asm volatile("s_getpc_b64 %0\n.Lhere%=:\n\ts_mov_b32 %2, .Lhere%=-.Lcode_begin\n\ts_mov_b32 %1, .Lcode_end-.Lcode_begin" : "=s"(pc), "=s"(len), "=s"(back));
  pc -= back;
  const unsigned o = ((blockIdx.x >> 3) * 16 + (lane & 15)) * 128;
  touch(pc, min(o, len), ldsdummy);
}
struct Args { const u16 *x, *wqkv, *wphi, *wo, *w1, *b1, *w2, *b2, *c, *wm; u16* out; char* ws; };

DI int gxcol(int lane) { return 2 * (lane & 31) + (lane >> 5); }
template <int N> DI void gx_load(const char* ws, unsigned off, int lane, u32x4 (&v)[N]) { UNR for (int i = 0; i < N; i++) v[i] = ld_dev(mkrsrc(ws, WS_GX + off), lane * 16, i * 1024); }
template <int N> DI void gx_gather(const char* ws, unsigned off, int lane, unsigned tag, u32x4 (&v)[N], float (&t)[N]) {
  _Pragma("nounroll") for (int spin = 0; spin < SPIN_CAP; spin++) { unsigned bad = 0;
    UNR for (int i = 0; i < N; i++) bad |= (v[i][1] ^ tag) | (v[i][3] ^ tag);
    if (!__any(bad != 0)) break;
    asm volatile("s_sleep 2" ::: "memory");
    gx_load(ws, off, lane, v);
  }
  UNR for (int i = 0; i < N; i++) t[i] = sw32_add(__uint_as_float(v[i][0]), __uint_as_float(v[i][2]));
}
DI float sum8(const float (&p)[8]) { return fadd(fadd(fadd(p[0], p[1]), fadd(p[2], p[3])), fadd(fadd(p[4], p[5]), fadd(p[6], p[7]))); }

DI u64 phase0(const Args& A, int w, unsigned lds0, unsigned& ts) {
  char* ws = A.ws; const Blk B = blk0(), B1 = blk1(); const f32x4 zero = {0, 0, 0, 0};
  const int lane = threadIdx.x & 63, r8 = lane >> 3, g = lane >> 4, r = lane & 15;
  u64 ep; asm volatile("s_load_dwordx2 %0, %1, 0x0" : "=s"(ep) : "s"(ws + WS_EPOCH) : "memory");
  unsigned cw; gld4(cw, A.c, B.tm * 128 + (lane & 31) * 4);
  { const char* wb = (const char*)A.wm + (size_t)(B.tm * 8 + w) * 16 * MODN + B.tn * 128; const unsigned wv = r8 * MODN * 2 + (((lane & 7) ^ (((r8 >> 1) & 3) << 1)) * 16);
    glds16(wb + 2 * D_, wv, lds0 + L_UN + w * 1024); glds16(wb, wv, lds0 + L_UN + 8192 + w * 1024); }
  bar();
  const unsigned rv0 = r8 * 2048 + (((lane & 7) ^ (r8 >> 1)) * 16);
  UNR for (int i = 0; i < 2; i++) glds16((const char*)A.x + ((size_t)(B.tm * 128 + w * 16 + i * 8) * D_ + B.tn * 64) * 2, i ? rv0 ^ 64 : rv0, lds0 + L_XT + (w * 2 + i) * 1024);
  if (XA2) { const int e = min(w * 24 + lane, 191), row = e / 12, l = e - row * 12;
    if (lane < 24) touch((const char*)A.wqkv + (size_t)((B1.tm >> 3) * 512 + B.j * 16) * 6144 + B1.xq * 512, row * 6144 + (l >> 2) * 2048 + (l & 3) * 128, lds0 + L_DUM); }
  else { const int e = min(w * 24 + lane, 191), row = e / 6, l = e - row * 6;
    if (lane < 24) touch((const char*)A.wqkv + (size_t)B.j * 32 * 6144 + B.xq * 256, row * 6144 + (l >> 1) * 2048 + (l & 1) * 128, lds0 + L_DUM); }
  unsigned pa = lds0 + L_UN + (w >> 2) * 8192 + ptile(w & 3, lane); pin(pa);
  waitvm<5>(); pin(cw);
  asm volatile("s_waitcnt lgkmcnt(0)" : "+s"(ep) :: "memory");
  const u64 tag = ep + 0x9E3779B97F4A7C15ull;
  if (w == 7) ldsw<unsigned>(lds0 + L_SIL2 + fragpos(2 * (lane & 31)), silu2(cw));
  waitvm<3>(); lbar();
  STAMP(1);
  { const f32x4 d = mfma(wfrag(pa + 4096, 2048), ldsr<bf16x8>(lds0 + L_SIL2 + 64 + g * 16), mfma(wfrag(pa, 2048), ldsr<bf16x8>(lds0 + L_SIL2 + g * 16), zero));
    const bool o = r & 1; const u32x4 gr = {__float_as_uint(o ? d[2] : d[0]), (unsigned)tag, __float_as_uint(o ? d[3] : d[1]), (unsigned)tag};
    if (r < 2) st_dev(gr, mkrsrc(ws, WS_GX + (((w >> 2) * 16 + B.tn) * 16 + B.tm) * 512), g * 32 + r * 16, (w & 3) * 128); }
  asm volatile("" ::: "memory");
  if (w < 3) {
    const int l = w * 8 + r8;
    if (l < 20) st_dev(u32x4{~0u, ~0u, ~0u, ~0u}, mkrsrc(ws, 0), (l < 8 ? WS_MODP + (((l >> 1) * 16 + B.tn) * 16 + B.tm) * 256 + (l & 1) * 128 : WS_UV1 + (B1.tn * 12 + l - 8) * 2048 + B1.tm * 128) + (lane & 7) * 16, 0); }
  if (w == 7) { const Blk C = blk(); if (lane < 6) touch_any(flagp(ws, lane < 2 || lane == 5 ? B1.tm * 16 + B1.tn : C.tm * 16 + C.tn, lane, 0), lds0 + L_DUM); }
  if (w < 3 || w == 7) waitvm<3>(); else waitvm<2>();
  f32x4 xv[4]; f32x2 stat;
  UNR for (int cc = 0; cc < 4; cc++) { const u32x2 t = ldsr<u32x2>(lds0 + L_XT + (w * 16 + r) * 128 + (((cc * 2 + (g >> 1)) ^ (r >> 1)) * 16) + (g & 1) * 8); xv[cc] = unp(t[0], t[1]); }
  { const float mean = xg_sum(hsum4((xv[0] + xv[1]) + (xv[2] + xv[3]))) * (1.f / 64); f32x4 q = zero;
    UNR for (int cc = 0; cc < 4; cc++) { const f32x4 d = xv[cc] - mean; q = fma4(d, d, q); }
    stat = f32x2{mean, xg_sum(hsum4(q))}; pin(stat); pin(xv); }
  waitvm<0>();
  STAMP(2);
  { float t[1]; u32x4 v[1]; gx_load(ws, B.tn * 8192 + w * 1024, lane, v); gx_gather(ws, B.tn * 8192 + w * 1024, lane, (unsigned)tag, v, t); ldsw<float>(lds0 + L_RED + w * 256 + gxcol(lane) * 4, t[0]); }
  STAMP(3);
  lbar();
  float p[8]; UNR for (int i = 0; i < 8; i++) p[i] = ldsr<float>(lds0 + L_RED + i * 256 + lane * 4);
  ldsw<float>(lds0 + L_SROW + w * 256 + lane * 4, rbf(1.f + rbf(sum8(p))));
  asm volatile("" ::: "memory");
  const rsrc_t ry = outrsrc(A.out, (B.tm * 16 + B.tn) * 16384);
  UNR for (int ks = 0; ks < 2; ks++)
    st_dev(pack8u(xv[2 * ks] * ldsr<f32x4>(lds0 + L_SROW + w * 256 + ks * 128 + g * 16), xv[2 * ks + 1] * ldsr<f32x4>(lds0 + L_SROW + w * 256 + ks * 128 + 64 + g * 16)), ry, lane * 16, w * 2048 + ks * 1024);
  if (g == 0) st_dev8(__builtin_bit_cast(u32x2, stat), mkrsrc(ws, WS_ST1 + (size_t)(B.tm * 16 + B.tn) * 1024), r * 8, w * 128);
  asm volatile("" ::: "memory");
  STAMP(4);
  return tag;
}

DI void loader_role(const Args& A, int lw, unsigned lds0, u64 tag, unsigned& ts) {
  char* ws = A.ws; const Blk B2 = blk(), B1 = blk1(), B0 = blk0(); const f32x4 zero = {0, 0, 0, 0};
  int lane = threadIdx.x & 63; pin(lane);
  Loader<2> G2; Sync Y; auto none = [](int) { return 0; };
  unsigned tv; { const int e = min(lane, 55), row = e / 14; tv = row * 7168 + (e - row * 14) * 128; pin(tv); }
  auto w1half = [&]() { touch(opq((const char*)A.w1) + (size_t)((B2.tm >> 3) * 512 + B2.j * 16 + lw * 4) * 7168 + B2.xq * 1792, tv, lds0 + L_DUM); };
  {
    const Blk& B = B1; const int g = lane >> 4, r = lane & 15, q = r >> 2, p = lane & 3, r8 = lane >> 3;
    Loader<0> G; G.init(A.out, 16, A.wqkv, 0, B.tn * 64, 0, B.rot, 0, B.tm, lw, lane, lds0);
    G.issueB(0); G.issueB(1);
    const rsrc_t ruv = mkrsrc(ws, WS_UV1 + B.tn * 12 * 2048);
    UV<0, 3> U; Stats<16> S;
    unsigned mk[3] = {(unsigned)(WS_KV1 + (B.tn * 4 + lw) * 32768 + B.tm * 2048 + lane * 16), (unsigned)(WS_KV2 + (B.tn * 16 + B.tm) * 8192 + lw * 2048 + lane * 16),
      (unsigned)((r8 == 0 ? WS_ZP + (B.tn * 4 + lw) * 2048 + B.tm * 128 : r8 < 5 ? WS_UV2 + (B.tn * 16 + lw * 4 + r8 - 1) * 2048 + B.tm * 128 : WS_ST2 + blockIdx.x * 1024 + (lw * 2 + (r8 & 1)) * 128) + (lane & 7) * 16)};
    u32x4 mark = {~0u, ~0u, ~0u, ~0u}; unsigned rwv = r8 * MODN * 2 + (((lane & 7) ^ (((r8 >> 1) & 3) << 1)) * 16); pin(mk); pin(mark); pin(rwv);
    waitvm<12>();
    STAMP(5);
    Y.run(ws, 0, B0, B, lw, lane, tag, G.first_need(3, 0), [&]() { waitvm<6>(); });
    STAMP(11);
    G.issueA(0); G.issueA(1); U.form(ruv, B.tm, lw, lane, lds0, 0); waitvm<4>(); bar();
    G.issue(2); Y.rest(lane); waitvm<10>(); bar();
    auto rest = [&]() {
      const char* wb = opq((const char*)A.wm) + (size_t)B0.tm * 128 * MODN + (lw + 2) * 2 * D_ + B0.tn * 128; const unsigned l0 = lds0 + L_REST + lw * 8192;
      sfor<0, 8>([&](auto I) { glds16i<I.value * 1024>(wb + I.value * 16 * MODN, rwv, l0); }); };
    const int t1 = 1 + (B.j * 5 >> 5);
    G.run3<7>(
      [&](int t) { int e = 1;
        if (MW) { if (t <= 4) S.fetch(t - 1, ws + WS_ST1, B.tm, lw, lane); else e = 0; }
        else if (MS) { e = 0; if (t <= 4) { S.fetch(t - 1, ws + WS_ST1, B.tm, lw, lane); e = 1; } if (t == 5) { U.send(ws); e = 1; } }
        else if (t <= 4) { st_dev(mark, mkrsrc(ws, 0), mk[(t - 1) >> 1], ((t - 1) & 1) * 1024); S.fetch(t - 1, ws + WS_ST1, B.tm, lw, lane); e = 2; } else st_dev(mark, mkrsrc(ws, 0), mk[2], 0);
        if ((RA & 1) && t == t1) { rest(); e += 8; }
        return e; },
      [&](int t) { int e = 0;
        if (MS) { if (t <= 10) { st_dev(mark, mkrsrc(ws, 0), t == 6 ? mk[0] : t == 7 ? mk[0] + 1024 : t == 8 ? mk[1] : t == 9 ? mk[1] + 1024 : mk[2], 0); e = 1; } }
        else if (t == 8) { U.send(ws); e = 1; }
        if (t == 11 && lw == 0) { touch_code(lane, lds0 + L_DUM); e += 1; }
        return e; },
      [&]() { U.fetch(opq(ws) + ruv.base, lw, lane); glds16((const char*)A.wphi + B.tn * 4096 + lw * 1024, lane * 16, lds0 + L_WPHI + lw * 1024); });
    if (!(RA & 1)) rest();
    if (RA & 2) w1half();
    prio(1);
    S.finish(lw, lane, lds0 + L_STT);
    waitvm<((RA & 1) ? 0 : 8) + ((RA & 2) ? 1 : 0)>(); U.finish(ruv, lw, lane, lds0 + L_UVT);
    G2.init(ws + WS_O, 16, A.wo, 0, B2.tn * 64, 0, B2.rot, 0, B2.tm, lw, lane, lds0);
    prio(0);
    lbar();
    { bf16x8 wf[2]; f32x4 k[2][2], vv[2][2];
      UNR for (int m = 0; m < 2; m++) UNR for (int c = 0; c < 2; c++) { k[m][c] = ldsr<f32x4>(lds0 + L_TR + lw * 8192 + c * 4096 + m * 1024 + lane * 16); vv[m][c] = ldsr<f32x4>(lds0 + L_RV + lw * 4096 + (m * 2 + c) * 1024 + lane * 16); }
      UNR for (int c = 0; c < 2; c++) wf[c] = wfrag(lds0 + L_WPHI + (lw & 1) * 2048 + (4 * g + q) * 64 + c * 32 + p * 8, 1024);
      UNR for (int s = 0; s < 4; s++) G2.issueB(s);
      kv_prep(k, vv, wf, 0, lw, lane, lds0); }
    if (lw == 0 && lane < 16) __hip_atomic_store(flagp(ws, lane * 16 + B.tn, 5, B.tm), tag, __ATOMIC_RELAXED, __HIP_MEMORY_SCOPE_AGENT);
    lbar();
    prio(1);
    const int hd = lw >> 1, sg = lw & 1;
    const u32x4 one4 = {0x3f803f80u, 0x3f803f80u, 0x3f803f80u, 0x3f803f80u}; const bf16x8 ones = __builtin_bit_cast(bf16x8, one4);
    const unsigned tra = lds0 + L_TR + hd * 8192 + sg * 4096 + (4 * g + q) * 64 + ((p ^ (((4 * g + q) >> 1) & 3)) * 16), vfa = lds0 + L_VF + hd * 4096 + lane * 16;
    f32x4 kv[2][2] = {{zero, zero}, {zero, zero}}, z[2] = {zero, zero};
    UNR for (int k = 0; k < 4; k++) { const bf16x8 v0 = ldsr<bf16x8>(vfa + (k >> 1) * 8192 + (k & 1) * 2048), v1 = ldsr<bf16x8>(vfa + (k >> 1) * 8192 + (k & 1) * 2048 + 1024);
      UNR for (int ft = 0; ft < 2; ft++) { const bf16x8 af = wfrag(tra + (k >> 1) * 16384 + (k & 1) * 2048 + ft * 8, 1024);
        kv[ft][0] = mfma(af, v0, kv[ft][0]); kv[ft][1] = mfma(af, v1, kv[ft][1]); z[ft] = mfma(af, ones, z[ft]); } }
    const rsrc_t rk = mkrsrc(ws, WS_KV1 + (size_t)(B.tn * 4 + lw) * 32768), rz = mkrsrc(ws, WS_ZP + (size_t)(B.tn * 4 + lw) * 2048), r2 = mkrsrc(ws, WS_KV2 + (size_t)B.tn * 131072);
    UNR for (int c = 0; c < 2; c++) st_dev(pack8u(kv[0][c], kv[1][c]), rk, lane * 16, B.tm * 2048 + c * 1024);
    { const bool o = lane & 1; const f32x4 zs = {o ? z[1][0] : z[0][0], o ? z[1][1] : z[0][1], o ? z[1][2] : z[0][2], o ? z[1][3] : z[0][3]};
      if ((lane & 14) == 0) st_dev(__builtin_bit_cast(u32x4, zs), rz, (lane & 1) * 64 + g * 16, B.tm * 128); }
    STAMP(16);
    waitvm<3>();
    { const f32x4 d = gemv64(lds0 + L_REST + lw * 8192, ldsr<bf16x8>(lds0 + L_SIL2 + g * 16), ldsr<bf16x8>(lds0 + L_SIL2 + 64 + g * 16), lane);
      if (r < 4) st_dev(__builtin_bit_cast(u32x4, d), mkrsrc(ws, WS_MODP + (size_t)((lw * 16 + B0.tn) * 16 + B0.tm) * 256), r * 64 + g * 16, 0); }
    wait_flags(flagp(ws, B.tm * 16 + B.tn, 5, 0), lane, 16, tag, ~0ull);
    waitvm<1>();
    u32x4 h[2], zz[2]; poll(h, [&](int i) { return ld_dev(rk, (lane >> 3) * 2048 + (lane & 7) * 16, B.tm * 128 + i * 16384); });
    UNR for (int i = 0; i < 2; i++) gld_dev(zz[i], ws + rz.base + i * 64, r * 128 + g * 16);
    pin(h);
    { float e[4], o[4];
      UNR for (int k = 0; k < 4; k++) { e[k] = fadd(blo(h[0][k]), blo(h[1][k])); o[k] = fadd(bhi(h[0][k]), bhi(h[1][k])); }
      float te = fold4<false>(e[0], e[1], e[2], e[3]), to = fold4<false>(o[0], o[1], o[2], o[3]); te += dpp<0x128>(te); to += dpp<0x128>(to);
      spread4(__uint_as_float(cvtpk(te, to)), e[0], e[1], e[2], e[3]);
      const u32x4 sum = {__float_as_uint(e[0]), __float_as_uint(e[1]), __float_as_uint(e[2]), __float_as_uint(e[3])};
      UNR for (int i = 0; i < 2; i++) st_dev(sum, r2, (lane >> 3) * 8192 + (lane & 7) * 16, i * 65536 + lw * 2048 + B.tm * 128); }
    STAMP(17);
    waitvm<2>(); pin(zz);
    if (__any(max(umax4(zz[0]), umax4(zz[1])) == 0xffffffffu)) poll(zz, [&](int i) { return ld_dev(rz, r * 128 + g * 16, i * 64); });
    ldsw<bf16x8>(lds0 + L_KX + lw * 1024 + lane * 16, pack8(row16_sum4(asf(zz[0])), row16_sum4(asf(zz[1]))));
    waitvm<0>();
    lbar();
    prio(0);
  }
  const Blk& B = B2; const int odd = B.tn & 1;
  Loader<1> G3;
  {
    Y.run(ws, 2, B1, B, lw, lane, tag, G2.first_need(4, 0), [&]() {});
    STAMP(21);
    G3.init(A.out, 16, A.w1, 0, B.tn * 224 + odd * 32, B.tn * 224 + (odd ? 0 : 192), B.tm, B.tn, B.tm, lw, lane, lds0);
    const int tk = 4 + (B.j >> 3);
    G2.run4<7, false>([&]() { Y.rest(lane); }, [&]() { G3.issueBk(0, B.tn); }, [&](int t) { if (!(RA & 2) && t == tk) { w1half(); return 1; } return 0; });
    if (blockIdx.x == 0 && lw == 0 && lane == 0) __hip_atomic_store((u64*)(ws + WS_EPOCH), tag, __ATOMIC_RELAXED, __HIP_MEMORY_SCOPE_AGENT);
  }
  Loader<3> G4; const int own4 = (7 * (B.tn & 7) + 1) >> 1;
  {
    const rsrc_t ruv = mkrsrc(ws, WS_UV2 + B.tn * 16 * 2048);
    UV<1, 4> U; Stats<16> S; const int t4 = 7 + ((B.j + 16 * (B.tm >> 3)) & 31) * 7 / 32;
    bar();
    G3.issueB(1);
    waitvm<7>();
    Y.run(ws, 3, B, B, lw, lane, tag, G3.first_need(2, 0), [&]() {
      if (lw == 1 || lw == 2) { const f32x4 s = ldsr<f32x4>(lds0 + L_UVT + (lw - 1) * 1024 + lane * 16); const float d = s[0] - s[2];
        st_dev8(__builtin_bit_cast(u32x2, f32x2{0.5f * fadd(s[0], s[2]), __builtin_fmaf(16.f * d, d, fadd(s[1], s[3]))}), mkrsrc(ws, WS_ST2 + (B.tm * 16 + B.tn) * 1024), lane * 8, (lw - 1) * 512); }
      G3.issueB(2); waitvm<7>(); });
    STAMP(27);
    G3.issueA(1); G3.issueA(2); U.form(ruv, B.tm, lw, lane, lds0, B.tm == B.tn ? 2 * Lay<1>::SBB : 0); Y.rest(lane); waitvm<4>(); bar();
    G3.run3<8>(
      [&](int t) { if (t == 1) U.send(ws); else S.fetch(t - 2, ws + WS_ST2, B.tm, lw, lane); return 1; },
      [&](int t) { int e = 0;
        if (t == t4) {
          const char* p = opq((const char*)A.w2) + (size_t)((B.xq >> 1) * 1792 + B.j * 56 + lw * 14) * 2048 + (B.xq & 1) * 1024; const unsigned v = min(lane >> 3, 6) * 2048 + (lane & 7) * 128;
          touch(p, v, lds0 + L_DUM); touch(p + 7 * 2048, v, lds0 + L_DUM); e += 2; }
        if (t == 11 && lw == 0) { touch_code(lane, lds0 + L_DUM); e += 1; }
        return e; },
      [&]() { if (TV == 3) { prio(1); S.check(ws, WS_ST2, B.tm, lw, lane); S.finish(lw, lane, lds0 + L_STT); prio(0); }
              U.fetch(opq(ws) + ruv.base, lw, lane);
              if (TV == 2) { prio(1); S.check(ws, WS_ST2, B.tm, lw, lane); S.finish(lw, lane, lds0 + L_STT); prio(0); } });
    { const int r0 = (B.rot * 25) >> 4; G4.init(ws + WS_H1, 56, A.w2, (B.tn >> 3) * 28, (B.tn & 7) * 128, 0, r0 < own4 ? r0 : r0 + 3, own4, B.tm, lw, lane, lds0); }
    if (TV) G4.issueBk(0, own4);
    prio(1);
    if (TV < 2) { S.check(ws, WS_ST2, B.tm, lw, lane); S.finish(lw, lane, lds0 + L_STT); }
    waitvm<TV ? 4 : 0>(); U.finish(ruv, lw, lane, lds0 + L_UVT);
    prio(0);
    lbar();
    UNR for (int s = TV != 0; s < 3; s++) G4.issueBk(s, own4 + s);
  }
  {
    waitvm<0>(); bar();
    G4.issueB(3); bar(); G4.issueB(0); bar(); G4.issueB(1);
    Y.run(ws, 4, B, B, lw, lane, tag, G4.first_need(4, (B.tn >> 3) * 28), [&]() { G4.issueB(2); });
    STAMP(35);
    G4.run4<4, true>([&]() { Y.rest(lane); }, [&]() {
      glds16x4(opq(ws) + WS_X1 + (size_t)(B.tm * 16 + (B.tn & 7) * 2 + (lw & 1)) * 16384 + ((lw >> 1) * 4 + (B.tn >> 3) * 2) * 2048, lane * 16, lds0 + L_OT + lw * 4096); }, none);
    asm volatile(".Lcode_end:");
  }
}

DI void compute_role(const Args& A, int w, unsigned lds0, u64 tag, unsigned& ts) {
  char* ws = A.ws; const Blk B = blk(); const f32x4 zero = {0, 0, 0, 0}; const u32x4 mark = {~0u, ~0u, ~0u, ~0u};
  const int wm = w >> 1, wn = w & 1; int lane = threadIdx.x & 63; pin(lane);
  {
    const Blk B = blk1(); const int g = lane >> 4, r = lane & 15, q = r >> 2, p = lane & 3;
    auto marks = [&](int lw) { const int r8 = lane >> 3;
      UNR for (int c = 0; c < 4; c++) st_dev(mark, mkrsrc(ws, c < 2 ? WS_KV1 + (B.tn * 4 + lw) * 32768 + B.tm * 2048 : WS_KV2 + (B.tn * 16 + B.tm) * 8192 + lw * 2048), lane * 16, (c & 1) * 1024);
      st_dev(mark, mkrsrc(ws, 0), (r8 == 0 ? WS_ZP + (B.tn * 4 + lw) * 2048 + B.tm * 128 : r8 < 5 ? WS_UV2 + (B.tn * 16 + lw * 4 + r8 - 1) * 2048 + B.tm * 128 : WS_ST2 + blockIdx.x * 1024 + (lw * 2 + (r8 & 1)) * 128) + (lane & 7) * 16, 0); };
    waitvm<0>();
    STAMP(5);
    bar();
    if (w < 2) { float t[8]; u32x4 gv[8]; gx_load(ws, (w * 16 + B.rot) * 8192, lane, gv); gx_gather(ws, (w * 16 + B.rot) * 8192, lane, (unsigned)tag, gv, t);
      const float v = rbf(sum8(t)); put_et(lds0, w, gxcol(lane), w ? v : rbf(1.f + v)); }
    else if (MW == 1) { __builtin_amdgcn_s_sleep(2); marks(w * 2 - 4); marks(w * 2 - 3); }
    lbar();
    if (MW == 2) { __builtin_amdgcn_s_sleep(6); marks(w); }
    f32x4 acc[4][6];
    compute_loop<0>(acc, lds0, wm, wn, lane, ts, [](int t) { if (MW && (t & 1)) waitvm<0>(); if (QS) __builtin_amdgcn_s_sleep(QS); });
    STAMP(13);
    lbar();
    STAMP(14);
    bf16x8 wf[2];
    UNR for (int c = 0; c < 2; c++) wf[c] = wfrag(lds0 + L_WPHI + wn * 2048 + (4 * g + q) * 64 + c * 32 + p * 8, 1024);
    { const f32x4 k[2][2] = {{acc[2][2], acc[2][3]}, {acc[3][2], acc[3][3]}}, vv[2][2] = {{acc[2][4], acc[2][5]}, {acc[3][4], acc[3][5]}}; kv_prep(k, vv, wf, 1, w, lane, lds0); }
    lbar();
    STAMP(15);
    { const Blk C = blk(); const int r8 = lane >> 3; const unsigned rv0 = r8 * 2048 + (((lane & 7) ^ (r8 >> 1)) * 16);
      UNR for (int i = 0; i < 4; i++) glds16((const char*)A.x + ((size_t)(C.tm * 128 + (w * 4 + i) * 8) * D_ + C.tn * 64) * 2, (i & 1) ? rv0 ^ 64 : rv0, lds0 + L_RES + (w * 4 + i) * 1024); }
    asm volatile("" : "+v"(acc[0][0]), "+v"(acc[0][1]), "+v"(acc[1][0]), "+v"(acc[1][1]), "+v"(acc[2][0]), "+v"(acc[2][1]), "+v"(acc[3][0]), "+v"(acc[3][1]));
    bf16x8 qp[4], qn[4]; RowSt st[4]; const unsigned uva = lds0 + L_UVT + wn * 256;
    UNR for (int m = 0; m < 4; m++) st[m].set(lds0 + L_STT + (wm * 64 + m * 16 + r) * 4, L2E);
    { const f32x4 u0 = ldsr<f32x4>(uva + g * 16), v0 = ldsr<f32x4>(uva + 64 + g * 16) * L2E, u1 = ldsr<f32x4>(uva + 128 + g * 16), v1 = ldsr<f32x4>(uva + 192 + g * 16) * L2E;
      UNR for (int m = 0; m < 4; m += 2) feat2(wf, pack8(lnf(acc[m][0], st[m], u0, v0), lnf(acc[m][1], st[m], u1, v1)),
                                               pack8(lnf(acc[m + 1][0], st[m + 1], u0, v0), lnf(acc[m + 1][1], st[m + 1], u1, v1)), qp[m], qn[m], qp[m + 1], qn[m + 1]); }
    asm volatile("" : "+v"(qp[0]), "+v"(qp[1]), "+v"(qp[2]), "+v"(qp[3]), "+v"(qn[0]), "+v"(qn[1]), "+v"(qn[2]), "+v"(qn[3]) :: "memory");
    STAMP(16);
    bar();
    STAMP(17);
    const rsrc_t r2 = mkrsrc(ws, WS_KV2 + (size_t)(B.tn * 16 + B.tm) * 8192 + wn * 4096);
    u32x4 kk[4]; UNR for (int i = 0; i < 4; i++) kk[i] = ld_dev(r2, lane * 16, i * 1024);
    const bf16x8 z0 = ldsr<bf16x8>(lds0 + L_KX + wn * 2048 + lane * 16), z1 = ldsr<bf16x8>(lds0 + L_KX + wn * 2048 + 1024 + lane * 16);
    float rd[4];
    UNR for (int m = 0; m < 4; m++) rd[m] = __builtin_amdgcn_rcpf(mfma(z1, qn[m], mfma(z0, qp[m], zero))[0] + 1e-6f);
    asm volatile("" : "+v"(rd[0]), "+v"(rd[1]), "+v"(rd[2]), "+v"(rd[3]));
    if (__any(max(max(umax4(kk[0]), umax4(kk[1])), max(umax4(kk[2]), umax4(kk[3]))) == 0xffffffffu)) poll(kk, [&](int i) { return ld_dev(r2, lane * 16, i * 1024); });
    const rsrc_t ro = mkrsrc(ws, WS_O + (size_t)(B.tm * 16 + B.tn) * 16384);
    UNR for (int m = 0; m < 4; m++) {
      const f32x4 n0 = mfma(__builtin_bit_cast(bf16x8, kk[2]), qn[m], mfma(__builtin_bit_cast(bf16x8, kk[0]), qp[m], zero)), n1 = mfma(__builtin_bit_cast(bf16x8, kk[3]), qn[m], mfma(__builtin_bit_cast(bf16x8, kk[1]), qp[m], zero));
      st_dev(pack8u(n0 * rd[m], n1 * rd[m]), ro, lane * 16, (wm * 8 + wn) * 1024 + m * 2048);
    }
    STAMP(18);
    waitvm<0>(); bar();
    STAMP(19);
  }
  pin(lane);
  const int fp0 = B.tn * 7 + (B.tn & 1) + wn, fph = (B.tn & 1) ? B.tn * 7 : B.tn * 7 + 6;
  u32x2 bb[7];
  {
    const int g = lane >> 4, r = lane & 15;
    UNR for (int n = 0; n < 7; n++) bb[n] = *(const u32x2*)(A.b1 + (n < 6 ? fp0 * 32 + (n >> 1) * 64 + (n & 1) * 16 : fph * 32 + wn * 16) + 4 * g);
    const unsigned b2v = A.b2[((B.tn & 7) * 2 + wn) * 64 + lane];
    u32x4 gp[4]; UNR for (int i = 0; i < 4; i++) gp[i] = ld_dev(mkrsrc(ws, WS_MODP + (48 + (B.tn & 7) * 2 + wn) * 4096), lane * 16, i * 1024);
    { const float v = modvec(ws, WS_MODP + (size_t)((w == 0 ? 0 : w == 3 ? 1 : 2) * 16 + (w < 2 ? B.tn : B.tm)) * 4096, lane), s = (w == 1 || w == 2) ? rbf(1.f + v) : v;
      if (w < 2) ldsw<float>(lds0 + L_GT + w * 256 + modcol(lane) * 4, s); else put_et(lds0, w - 2, modcol(lane), s); }
    pin(bb);
    f32x4 acc[4][2];
    bar();
    if (w < 2) ldsw<float>(lds0 + L_GB + w * 256 + modcol(lane) * 4, modvec(ws, WS_MODP + (size_t)(48 + (B.tn & 7) * 2 + w) * 4096, lane, gp, true));
    else ldsw<float>(lds0 + L_GB + w * 256 + lane * 4, blo(b2v));
    unsigned mo = xchoff(B.tm, B.tn, w) + lane * 16; pin(mo);
    compute_loop<2>(acc, lds0, wm, wn, lane, ts, [&](int t) {
      if ((t & 3) == 1) { st_dev(mark, mkrsrc(ws, 0), mo, 0); mo += 1024; }
      else if (SLOW) __builtin_amdgcn_s_sleep(SLOW); });
    STAMP(23);
    bar();
    f32x4 gate[2], sc[2]; u32x2 xr[4][2];
    UNR for (int n = 0; n < 2; n++) { gate[n] = ldsr<f32x4>(lds0 + L_GT + wn * 128 + n * 64 + g * 16); sc[n] = ldsr<f32x4>(lds0 + L_GT + 256 + wn * 128 + n * 64 + g * 16); }
    UNR for (int m = 0; m < 4; m++) UNR for (int n = 0; n < 2; n++) xr[m][n] = ldsr<u32x2>(lds0 + L_RES + (wm * 64 + m * 16 + r) * 128 + (((wn * 4 + n * 2 + (g >> 1)) ^ (r >> 1)) * 16) + (g & 1) * 8);
    const size_t tb = (size_t)(B.tm * 16 + B.tn) * 16384; const rsrc_t ry = outrsrc(A.out, tb);
    f32x4 v[4][2]; float mean[4], m2[4]; u32x4 o[4];
    UNR for (int m = 0; m < 4; m++) {
      o[m] = pack8u(fma4(gate[0], acc[m][0], unp(xr[m][0][0], xr[m][0][1])), fma4(gate[1], acc[m][1], unp(xr[m][1][0], xr[m][1][1]))); v[m][0] = unp(o[m][0], o[m][1]); v[m][1] = unp(o[m][2], o[m][3]);
      const u32x4 y = pack8u(v[m][0] * sc[0], v[m][1] * sc[1]);
      st_dev(y, ry, lane * 16, (wm * 8 + wn) * 1024 + m * 2048); ldsw<u32x4>(lds0 + (wm * 8 + wn) * 1024 + m * 2048 + lane * 16, y); }
    asm volatile("" ::: "memory");
    UNR for (int m = 0; m < 4; m++) mean[m] = hsum4(v[m][0] + v[m][1]);
    const float mf = fold4<false>(mean[0], mean[1], mean[2], mean[3]) * (1.f / 32); spread4(mf, mean[0], mean[1], mean[2], mean[3]);
    UNR for (int m = 0; m < 4; m++) { const f32x4 d0 = v[m][0] - mean[m], d1 = v[m][1] - mean[m]; m2[m] = hsum4(fma4(d0, d0, d1 * d1)); }
    ldsw<f32x2>(lds0 + L_UVT + (wm * 64 + ((g >> 1) + 2 * (g & 1)) * 16 + r) * 16 + wn * 8, f32x2{mf, fold4<false>(m2[0], m2[1], m2[2], m2[3])});
    STAMP(24);
    asm volatile("" ::: "memory");
    UNR for (int m = 0; m < 4; m++) st_dev(o[m], mkrsrc(ws, WS_X1 + tb), lane * 16, (wm * 8 + wn) * 1024 + m * 2048);
    waitvm<4>();
    STAMP(25);
  }
  pin(lane);
  {
    const int g = lane >> 4, r = lane & 15;
    f32x4 acc[4][7];
    compute_loop<1>(acc, lds0, wm, wn, lane, ts, [](int) {});
    STAMP(29);
    bar();
    STAMP(30);
    f32x4 u[7], v[7];
    UNR for (int n = 0; n < 7; n++) { const unsigned a = lds0 + L_UVT + (n < 6 ? (n >> 1) * 4 + wn * 2 + (n & 1) : 12 + wn) * 128 + g * 16; u[n] = ldsr<f32x4>(a); v[n] = ldsr<f32x4>(a + 64) + unp(bb[n][0], bb[n][1]); }
    const rsrc_t rh = mkrsrc(ws, WS_H1 + (size_t)B.tm * 56 * 16384);
    RowSt st[4]; u32x2 h6[4];
    UNR for (int m = 0; m < 4; m++) st[m].set(lds0 + L_STT + (wm * 64 + m * 16 + r) * 4, 1.f);
    auto act = [&](int m, int n) { const f32x4 t = __builtin_elementwise_max(lnf(acc[m][n], st[m], u[n], v[n]), zero); return t * t; };
    UNR for (int n = 0; n < 6; n += 2) { const int fp = fp0 + n;
      const unsigned la = lds0 + (n >> 1) * 16384 + wn * 1024 + wm * 8192 + lane * 16;
      UNR for (int m = 0; m < 4; m++) { const u32x4 hh = pack8u(act(m, n), act(m, n + 1)); st_dev(hh, rh, lane * 16, (fp >> 1) * 16384 + (fp & 1) * 1024 + wm * 8192 + m * 2048); ldsw<u32x4>(la + m * 2048, hh); }
      asm volatile("" ::: "memory"); }
    UNR for (int m = 0; m < 4; m++) { const f32x4 t = act(m, 6); h6[m] = u32x2{cvtpk(t[0], t[1]), cvtpk(t[2], t[3])}; }
    UNR for (int m = 0; m < 4; m += 2) { const auto a = __builtin_amdgcn_permlane16_swap(h6[m][0], h6[m + 1][0], false, false), b = __builtin_amdgcn_permlane16_swap(h6[m][1], h6[m + 1][1], false, false);
      st_dev(u32x4{a[0], b[0], a[1], b[1]}, rh, (g & 1) * 2048 + (g >> 1) * 256 + r * 16, (fph >> 1) * 16384 + (fph & 1) * 1024 + wm * 8192 + m * 2048 + wn * 512); }
    STAMP(31);
  }
  pin(lane);
  {
    const int g = lane >> 4, r = lane & 15, kh = B.tn >> 3, cg = (B.tn & 7) * 2 + wn;
    u32x4 xr[2][2]; f32x4 acc[4][4];
    compute_loop<3>(acc, lds0, wm, wn, lane, ts, [](int) {});
    STAMP(37);
    UNR for (int mm = 0; mm < 2; mm++) UNR for (int ks = 0; ks < 2; ks++) xr[mm][ks] = ldsr<u32x4>(lds0 + L_OT + w * 4096 + (mm * 2 + ks) * 1024 + lane * 16);
    const rsrc_t rx = mkrsrc(ws, xchoff(B.tm, B.tn, w)), rp = mkrsrc(ws, xchoff(B.tm, B.tn ^ 8, w));
    f32x4 keep[2][4], gate[4], bias[4];
    if (kh) { UNR for (int i = 0; i < 4; i++) st_dev(pack8u(acc[i >> 1][(i & 1) * 2], acc[i >> 1][(i & 1) * 2 + 1]), rx, lane * 16, i * 1024);
              UNR for (int n = 0; n < 4; n++) { keep[0][n] = acc[2][n]; keep[1][n] = acc[3][n]; } }
    else    { UNR for (int i = 0; i < 4; i++) st_dev(pack8u(acc[2 + (i >> 1)][(i & 1) * 2], acc[2 + (i >> 1)][(i & 1) * 2 + 1]), rx, lane * 16, i * 1024);
              UNR for (int n = 0; n < 4; n++) { keep[0][n] = acc[0][n]; keep[1][n] = acc[1][n]; } }
    UNR for (int n = 0; n < 4; n++) { gate[n] = ldsr<f32x4>(lds0 + L_GB + wn * 256 + n * 64 + g * 16); bias[n] = ldsr<f32x4>(lds0 + L_GB + 512 + wn * 256 + n * 64 + g * 16); }
    UNR for (int mm = 0; mm < 2; mm++) UNR for (int n = 0; n < 4; n++) keep[mm][n] = fma4(gate[n], keep[mm][n] + bias[n], unp(xr[mm][n >> 1][(n & 1) * 2], xr[mm][n >> 1][(n & 1) * 2 + 1]));
    pin(keep[0]); pin(keep[1]);
    waitvm<0>();
    u32x4 pp[4]; poll(pp, [&](int i) { return ld_dev(rp, lane * 16, i * 1024); });
    STAMP(38);
    const unsigned ot = lds0 + L_OT + w * 4096;
    UNR for (int mm = 0; mm < 2; mm++) UNR for (int n = 0; n < 4; n++) { const u32x4 t = pp[mm * 2 + (n >> 1)]; const f32x4 y = fma4(gate[n], unp(t[(n & 1) * 2], t[(n & 1) * 2 + 1]), keep[mm][n]);
      ldsw<u32x2>(ot + (mm * 16 + r) * 128 + (((n * 2 + (g >> 1)) ^ (r >> 1)) * 16) + (g & 1) * 8, u32x2{cvtpk(y[0], y[1]), cvtpk(y[2], y[3])}); }
    lwait();
    const int r8 = lane >> 3; const unsigned rv0 = r8 * 2048 + (((lane & 7) ^ (r8 >> 1)) * 16);
    const rsrc_t ro = outrsrc(A.out, ((B.tm * 128 + wm * 64 + kh * 32) * D_ + cg * 64) * 2);
    UNR for (int i = 0; i < 4; i++) st_dev(ldsr<u32x4>(ot + i * 1024 + lane * 16), ro, (i & 1) ? rv0 ^ 64 : rv0, i * 16384);
    STAMP(39);
  }
}

__global__ __launch_bounds__(512, 2) void k_block(Args A) {
  __shared__ __attribute__((aligned(1024))) char smem[L_TOT];
  const int w = __builtin_amdgcn_readfirstlane(threadIdx.x >> 6);
  const unsigned lds0 = (unsigned)(size_t)(LAS char*)smem;
  asm volatile(".Lcode_begin:");
  if (w == 3) touch_code(threadIdx.x & 63, lds0 + L_DUM);
  unsigned ts = 0; STAMP(0);
  const u64 tag = phase0(A, w, lds0, ts);
  if (w >= 4) loader_role(A, w - 4, lds0, tag, ts); else compute_role(A, w, lds0, tag, ts);
}

extern "C" void kernel_launch(void* const* d_in, const int* in_sizes, int n_in, void* d_out, int out_size, void* d_ws, size_t ws_size, hipStream_t stream) {
  Args A; A.x = (const u16*)d_in[0]; A.wqkv = (const u16*)d_in[1]; A.wphi = (const u16*)d_in[2]; A.wo = (const u16*)d_in[3]; A.w1 = (const u16*)d_in[5]; A.b1 = (const u16*)d_in[6];
  A.w2 = (const u16*)d_in[7]; A.b2 = (const u16*)d_in[8]; A.c = (const u16*)d_in[9]; A.wm = (const u16*)d_in[10]; A.out = (u16*)d_out; A.ws = (char*)d_ws;
  k_block<<<256, 512, 0, stream>>>(A);
}
```

```cpp
#include <hip/hip_runtime.h>
#include <hip/hip_bf16.h>
#include <type_traits>

typedef unsigned short u16;
typedef unsigned u32x4 __attribute__((ext_vector_type(4)));
typedef unsigned u32x2 __attribute__((ext_vector_type(2)));
typedef short s16x4 __attribute__((ext_vector_type(4)));
typedef __bf16 bf16x8 __attribute__((ext_vector_type(8)));
typedef __bf16 bf16x2 __attribute__((ext_vector_type(2)));
typedef float f32x2 __attribute__((ext_vector_type(2)));
typedef float f32x4 __attribute__((ext_vector_type(4)));
#define LAS __attribute__((address_space(3)))
#define DI __device__ __forceinline__
#define SB __builtin_amdgcn_sched_barrier(0)
#define UNR _Pragma("unroll")

constexpr int D_ = 1024, F_ = 3584, MODN = 6 * D_;
constexpr size_t WS_EPOCH = 0;
constexpr size_t WS_FLAG  = 4 << 20;
constexpr size_t WS_GX    = 1 << 18;
constexpr size_t WS_ST1   = 1 << 19;
constexpr size_t WS_MODP  = 3 << 19;
constexpr size_t WS_UV1   = WS_MODP + 4 * 65536;
constexpr size_t WS_ZP    = WS_UV1 + 16 * 12 * 2048;
constexpr size_t WS_UV2   = WS_ZP + (1 << 17);
constexpr size_t WS_ST2   = WS_UV2 + (1 << 19);
constexpr size_t WS_XCH   = 6 << 20;
constexpr size_t WS_KV1   = 10 << 20;
constexpr size_t WS_KV2   = WS_KV1 + (2 << 20);
constexpr size_t WS_O     = 14 << 20;
constexpr size_t WS_X1    = 18 << 20;
constexpr size_t WS_H1    = 22 << 20;
constexpr size_t WS_STAMP = 40 << 20;
constexpr int SPIN_CAP = 1 << 14;
typedef unsigned long long u64;

#define STAMP(i) do { } while (0)

DI void glds16(const void* sbase, unsigned voff, unsigned ldsaddr) {
  asm volatile("s_mov_b32 m0, %2\n\ts_nop 0\n\tglobal_load_lds_dwordx4 %0, %1" :: "v"(voff), "s"(sbase), "s"(ldsaddr) : "memory");
}
template <int IMM, int OFF = 0> DI void glds16i(const void* sbase, unsigned voff, unsigned ldsbase) {
  asm volatile("s_add_i32 m0, %2, %3\n\ts_nop 0\n\tglobal_load_lds_dwordx4 %0, %1 offset:%4" :: "v"(voff), "s"(sbase), "s"(ldsbase), "i"(IMM - OFF), "i"(OFF) : "memory", "scc");
}
template <int I, int N, class F> DI void sfor(F f) { if constexpr (I < N) { f(std::integral_constant<int, I>()); sfor<I + 1, N>(f); } }
DI void glds16x4(const void* sbase, unsigned voff, unsigned ldsaddr) {
  asm volatile("s_mov_b32 m0, %2\n\ts_nop 0\n\tglobal_load_lds_dwordx4 %0, %1 sc1\n\tglobal_load_lds_dwordx4 %0, %1 offset:1024 sc1\n\t"
               "global_load_lds_dwordx4 %0, %1 offset:2048 sc1\n\tglobal_load_lds_dwordx4 %0, %1 offset:3072 sc1" :: "v"(voff), "s"(sbase), "s"(ldsaddr) : "memory");
}
DI void prio(int p) { if (p) __builtin_amdgcn_s_setprio(3); else __builtin_amdgcn_s_setprio(0); }
DI void touch(const void* sbase, unsigned voff, unsigned ldsdummy) {
  asm volatile("s_mov_b32 m0, %2\n\ts_nop 0\n\tglobal_load_lds_dword %0, %1" :: "v"(voff), "s"(sbase), "s"(ldsdummy) : "memory");
}
template <int OFF> DI void gld_dev(u32x4& r, const void* sb, unsigned vo) { asm volatile("global_load_dwordx4 %0, %1, %2 offset:%3 sc1" : "=v"(r) : "v"(vo), "s"(sb), "i"(OFF) : "memory"); }
DI void gld2(unsigned& r, const void* sb, unsigned vo) { asm volatile("global_load_ushort %0, %1, %2" : "=v"(r) : "v"(vo), "s"(sb) : "memory"); }
DI void gld4(unsigned& r, const void* sb, unsigned vo) { asm volatile("global_load_dword %0, %1, %2" : "=v"(r) : "v"(vo), "s"(sb) : "memory"); }
template <class T> DI void pin(T& r) { asm volatile("" : "+v"(r)); }
template <class T, int N> DI void pin(T (&r)[N]) { UNR for (int i = 0; i < N; i++) pin(r[i]); }
template <class T> DI T opq(T v) { asm volatile("" : "+s"(v)); return v; }
DI f32x4 asf(u32x4 v) { return __builtin_bit_cast(f32x4, v); }
template <int N> DI void waitvm() { asm volatile("s_waitcnt vmcnt(%0)" :: "i"(N) : "memory"); }
template <int B, int LO = 0, int HI = 12> DI void waitx(int e) {
  if constexpr (LO == HI) waitvm<B + LO>();
  else if constexpr (LO == 0) { if (e == 0) waitvm<B>(); else waitx<B, 1, HI>(e); }
  else { constexpr int MID = (LO + HI + 1) / 2; if (e >= MID) waitx<B, MID, HI>(e); else waitx<B, LO, MID - 1>(e); }
}
DI void lwait() { asm volatile("s_waitcnt lgkmcnt(0)" ::: "memory"); }
DI void bar() { asm volatile("s_barrier" ::: "memory"); }
DI void lbar() { asm volatile("s_waitcnt lgkmcnt(0)\n\ts_barrier" ::: "memory"); }
DI unsigned cvtpk(float a, float b) { f32x2 v = {a, b}; return __builtin_bit_cast(unsigned, __builtin_convertvector(v, bf16x2)); }
DI float rbf(float a) { return __uint_as_float(cvtpk(a, a) << 16); }
DI float blo(unsigned u) { return __uint_as_float(u << 16); }
DI float bhi(unsigned u) { return __uint_as_float(u & 0xffff0000u); }
DI f32x4 unp(unsigned a, unsigned b) { return f32x4{blo(a), bhi(a), blo(b), bhi(b)}; }
DI u32x4 pack8u(const f32x4& a, const f32x4& b) { return u32x4{cvtpk(a[0], a[1]), cvtpk(a[2], a[3]), cvtpk(b[0], b[1]), cvtpk(b[2], b[3])}; }
DI bf16x8 pack8(const f32x4& a, const f32x4& b) { return __builtin_bit_cast(bf16x8, pack8u(a, b)); }
DI f32x4 mfma(bf16x8 a, bf16x8 b, f32x4 c) { return __builtin_amdgcn_mfma_f32_16x16x32_bf16(a, b, c, 0, 0, 0); }
DI s16x4 trd(unsigned a) { return __builtin_amdgcn_ds_read_tr16_b64_v4i16((LAS s16x4*)(size_t)a); }
DI bf16x8 cat(s16x4 lo, s16x4 hi) { return __builtin_bit_cast(bf16x8, __builtin_shufflevector(lo, hi, 0, 1, 2, 3, 4, 5, 6, 7)); }
template <class T> DI T ldsr(unsigned a) { return *(const LAS T*)(size_t)a; }
template <class T> DI void ldsw(unsigned a, T v) { *(LAS T*)(size_t)a = v; }
template <int C> DI float dpp(float x) { return __builtin_bit_cast(float, __builtin_amdgcn_update_dpp(0, __builtin_bit_cast(int, x), C, 0xf, 0xf, true)); }
DI float sw32_add(float a, float b) { auto r = __builtin_amdgcn_permlane32_swap(__float_as_uint(a), __float_as_uint(b), false, false); return __uint_as_float(r[0]) + __uint_as_float(r[1]); }
DI float sw16_add(float a, float b) { auto r = __builtin_amdgcn_permlane16_swap(__float_as_uint(a), __float_as_uint(b), false, false); return __uint_as_float(r[0]) + __uint_as_float(r[1]); }
DI float xg_sum(float a) { a = sw32_add(a, a); return sw16_add(a, a); }
DI float row16_sum(float a) { a += dpp<0x128>(a); a += dpp<0x124>(a); a += dpp<0x122>(a); a += dpp<0x121>(a); return a; }
DI f32x4 row16_sum4(f32x4 a) { return f32x4{row16_sum(a[0]), row16_sum(a[1]), row16_sum(a[2]), row16_sum(a[3])}; }
DI f32x4 spl(float a) { return f32x4{a, a, a, a}; }
DI f32x4 fma4(f32x4 a, f32x4 b, f32x4 c) { return __builtin_elementwise_fma(a, b, c); }
struct RowSt { float rs, nm; DI void set(unsigned a, float k) { rs = ldsr<float>(a) * k; nm = ldsr<float>(a + 512) * k; asm("" : "+v"(rs)); asm("" : "+v"(nm)); } };
DI f32x4 lnf(f32x4 acc, const RowSt& st, f32x4 u, f32x4 v) { return fma4(acc, spl(st.rs), fma4(u, spl(st.nm), v)); }
DI float fadd(float a, float b) { float r; asm("v_add_f32_e32 %0, %1, %2" : "=v"(r) : "v"(a), "v"(b)); return r; }
DI float hsum4(f32x4 a) { const f32x2 t = f32x2{a[0], a[1]} + f32x2{a[2], a[3]}; float r; asm("v_add_f32_e32 %0, %1, %2" : "=v"(r) : "v"(t[0]), "v"(t[1])); return r; }
DI bool hasnan(f32x4 a) { return __builtin_isunordered(a[0], a[1]) || __builtin_isunordered(a[2], a[3]); }
struct rsrc_t { __amdgpu_buffer_rsrc_t r; unsigned base; };
DI rsrc_t mkrsrc(const char* ws, size_t off) { return rsrc_t{__builtin_amdgcn_make_buffer_rsrc((void*)ws, 0, 64 << 20, 0x00020000), (unsigned)off}; }
DI rsrc_t outrsrc(const void* out, size_t off) { return rsrc_t{__builtin_amdgcn_make_buffer_rsrc((void*)out, 0, 2048 * D_ * 2, 0x00020000), (unsigned)off}; }
DI unsigned xchoff(int tm, int tn, int w) { return WS_XCH + (tm * 16 + tn) * 16384 + w * 4096; }
DI u32x4 ld_dev(const rsrc_t& r, unsigned voff, unsigned soff) { return __builtin_amdgcn_raw_buffer_load_b128(r.r, voff, r.base + soff, 16); }
DI void st_dev(u32x4 v, const rsrc_t& r, unsigned voff, unsigned soff) { __builtin_amdgcn_raw_buffer_store_b128(v, r.r, voff + (r.base + soff), 0, 16); }
DI void st_dev8(u32x2 v, const rsrc_t& r, unsigned voff, unsigned soff) { __builtin_amdgcn_raw_buffer_store_b64(v, r.r, voff + (r.base + soff), 0, 16); }
DI unsigned umax4(u32x4 v) { return max(max(v[0], v[1]), max(v[2], v[3])); }
template <int N, class F> DI void poll(u32x4 (&v)[N], F ld) {
  _Pragma("nounroll") for (int spin = 0; spin < SPIN_CAP; spin++) { unsigned m = 0;
    UNR for (int i = 0; i < N; i++) v[i] = ld(i);
    UNR for (int i = 0; i < N; i++) m = max(m, umax4(v[i]));
    if (!__any(m == 0xffffffffu)) break;
    asm volatile("s_sleep 2" ::: "memory");
  }
}
DI float fmax3(float a, float b, float c) { return __builtin_elementwise_maximum(__builtin_elementwise_maximum(a, b), c); }
struct Blk { int tm, tn, rot, j, xq; };
DI Blk blk() { const int xcd = blockIdx.x & 7, j = blockIdx.x >> 3; Blk b; b.tm = (xcd >> 2) * 8 + (j >> 2); b.tn = (xcd & 3) * 4 + (j & 3); b.rot = (b.tm & 7) * 2 + (b.tm >> 3); b.j = j; b.xq = xcd & 3; return b; }
DI Blk blk0() { const int xcd = blockIdx.x & 7, j = blockIdx.x >> 3; Blk b; b.tn = j >> 1; b.tm = xcd * 2 + (j & 1); b.rot = 0; b.j = j; b.xq = xcd; return b; }
DI unsigned ptile(int c, int lane) { const int row = 4 * (lane >> 4) + ((lane & 15) >> 2); return row * 128 + ((c ^ ((row >> 1) & 3)) * 32) + (lane & 3) * 8; }
DI unsigned htile(int c, int lane) { const int row = 4 * (lane >> 4) + ((lane & 15) >> 2); return row * 64 + ((c ^ ((row >> 2) & 1)) * 32) + (lane & 3) * 8; }
DI bf16x8 wfrag(unsigned a, unsigned ts) { return cat(trd(a), trd(a + ts)); }

DI unsigned silu2(unsigned cw) { const float c0 = blo(cw), c1 = bhi(cw); return cvtpk(c0 * __builtin_amdgcn_rcpf(1.f + __expf(-c0)), c1 * __builtin_amdgcn_rcpf(1.f + __expf(-c1))); }
DI unsigned fragpos(int k) { return (k >> 5) * 64 + ((k >> 2) & 3) * 16 + ((k & 3) + 4 * ((k >> 4) & 1)) * 2; }
DI f32x4 gemv64(unsigned a, bf16x8 s0, bf16x8 s1, int lane) { f32x4 d[4];
  UNR for (int cc = 0; cc < 4; cc++) d[cc] = mfma(wfrag(a + 4096 + ptile(cc, lane), 2048), s1, mfma(wfrag(a + ptile(cc, lane), 2048), s0, f32x4{0, 0, 0, 0}));
  const bool b0 = lane & 1, b1 = lane & 2; f32x4 o;
  UNR for (int k = 0; k < 4; k++) { const float lo = b0 ? d[1][k] : d[0][k], hi = b0 ? d[3][k] : d[2][k]; o[k] = b1 ? hi : lo; }
  return o;
}

template <int CFG> struct Cfg;
template <> struct Cfg<0> { static constexpr int NT = 6, NP = 3, HALF = 0, NB = 2, NS = 3, N = 3 * D_, KT = 16, B0 = 49152, BS0 = 0, OWN = 0; };
template <> struct Cfg<1> { static constexpr int NT = 7, NP = 3, HALF = 1, NB = 3, NS = 3, N = F_, KT = 16, B0 = 65536, BS0 = 2, OWN = 1; };
template <> struct Cfg<2> { static constexpr int NT = 2, NP = 1, HALF = 0, NB = 2, NS = 4, N = D_, KT = 16, B0 = 73728, BS0 = 0, OWN = 0; };
template <> struct Cfg<3> { static constexpr int NT = 4, NP = 2, HALF = 0, NB = 4, NS = 4, N = D_, KT = 28, B0 = 65536, BS0 = 0, OWN = 3; };
template <int CFG> struct Lay { using C = Cfg<CFG>; static constexpr int SBB = C::NP * 8192 + C::HALF * 4096, PB = 2 * C::NP + C::HALF, PER = 4 + PB; };
template <int CFG> DI unsigned btile(int tl, int lane) { using C = Cfg<CFG>; return C::B0 + (tl < 4 * C::NP ? (tl >> 2) * 8192 + ptile(tl & 3, lane) : C::NP * 8192 + htile(tl & 1, lane)); }
constexpr int L_RES = 106496, L_WPHI = 155648, L_UVT = L_WPHI + 4096, L_STT = L_UVT + 2048, L_ET = L_STT + 1024, L_GT = L_ET + 256, L_DUM = L_GT + 512, L_TOT = 163840, L_REST = 122880, L_OT = 131072;
constexpr int L_UN = 0, L_XT = 131072, L_RED = L_WPHI, L_SROW = L_RED + 2048, L_SIL2 = L_GT;
constexpr int L_GB = L_WPHI;
constexpr int L_VF = 0, L_TR = 16384, L_KX = 49152, L_RV = L_RES;

template <int N> DI void waitlgkm() { __builtin_amdgcn_s_waitcnt(0xC07F | (N << 8)); }
template <int CFG, class H> DI void compute_loop(f32x4 (&acc)[4][Cfg<CFG>::NT], unsigned lds0, int wm, int wn, int lane, unsigned& ts, H send) {
  using C = Cfg<CFG>; using L = Lay<CFG>; constexpr int NT = C::NT, NB = C::NB, KT = C::KT, NU = 1 + NB;
  bf16x8 fa[2][4][2]; s16x4 fb[4][2][2]; unsigned va = lds0 + wm * 8192 + lane * 16, vb[NB], rawk = 0, rawv = 0;
  UNR for (int i = 0; i < NB; i++) vb[i] = lds0 + C::BS0 * L::SBB + btile<CFG>(CFG == 1 ? (i < 2 ? wn * 2 + i : 12 + wn) : wn * NB + i, lane);
  pin(va); pin(vb);
  auto bbuf = [](int par, int n) { return NT == 2 ? par * 2 + n : NT == 4 ? n : n == 6 ? 3 : n % 3; };
  auto rdb = [&](int par, int n, int i) { const int ks = i >> 1, t = i & 1;
    fb[bbuf(par, n)][ks][t] = trd(CFG == 1 && n == 6 ? vb[NB - 1] + ks * 2048 + t * 1024 : CFG <= 1 ? vb[n & 1] + (n >> 1) * 8192 + ks * 4096 + t * 2048 : vb[n % NB] + ks * 4096 + t * 2048); };
  auto rda = [&](int par, int i) { fa[par][i >> 1][i & 1] = ldsr<bf16x8>(va + i * 1024); };
  int sa = 0;
  auto stage = [&](int par, int t, bool first, bool last) {
    int da = 0, db = 0;
    auto adv = [&](int j) { if (j == 0) { sa = opq(sa) + 1; da = 16384; if (sa == C::NS) { sa = 0; da -= C::NS * 16384; } da = opq(da); if (C::BS0 == 0 && L::SBB == 16384) db = da; }
                            else if (C::BS0 == 0) { if (L::SBB != 16384) db = opq(opq(sa) == 0 ? L::SBB - C::NS * L::SBB : L::SBB); } else db = opq(opq(sa) == C::NS - C::BS0 ? L::SBB - C::NS * L::SBB : L::SBB); };
    auto upd = [&](int u) { if (u == 0) { va += da; pin(va); } else if (u < NU) { vb[u - 1] += db; pin(vb[u - 1]); } };
    UNR for (int n = 0; n < NT; n++) {
      if (n == NT - 2) {
        if (NT == 2 && !last) { adv(0); adv(1); UNR for (int u = 0; u < NU; u++) upd(u); }
        waitlgkm<0>();
        if (CFG == 2 && last) bar();
        if (!last) {
          if (C::OWN && t == C::OWN - 1) { if (CFG == 3) { waitvm<0>(); STAMP(32); bar(); } bar(); }
          bar();
        }
        SB;
      } else if (n < NT - 2) { waitlgkm<4>(); SB; }
      UNR for (int i = 0; i < 8; i++) {
        const int m = (n & 1) ? 3 - (i >> 1) : i >> 1, ks = (i ^ (i >> 1)) & 1;
        const bf16x8 b = cat(fb[bbuf(par, n)][ks][0], fb[bbuf(par, n)][ks][1]); const f32x4 cin = first && !(i & 1) ? f32x4{0, 0, 0, 0} : acc[m][n];
        const bf16x8 o0 = (CFG == 0 && n >= 4) ? fa[par][m][ks] : b, o1 = (CFG == 0 && n >= 4) ? b : fa[par][m][ks];
        if (first && !(i & 1) && !(CFG == 3 && m >= 2)) asm volatile("v_mfma_f32_16x16x32_bf16 %0, %1, %2, 0" : "=&v"(acc[m][n]) : "v"(o0), "v"(o1));
        else if (!last) asm volatile("v_mfma_f32_16x16x32_bf16 %0, %1, %2, %0" : "+v"(acc[m][n]) : "v"(o0), "v"(o1)); else acc[m][n] = mfma(o0, o1, cin);
        SB;
        if (n < NT - 3) { if (!(i & 1)) rdb(par, n + 2, i >> 1); else if (!last) { if (n == 0 && i < 4) adv(i >> 1); if (n == NT - 4 && 3 - (i >> 1) < NU - 4) upd(3 - (i >> 1)); } }
        else if (n == NT - 3) { if (i < 4) rdb(par, n + 2, i); else if (!last) upd(i - 4 + (NU > 4 ? NU - 4 : 0)); }
        else if (!last) { if (n == NT - 2) { if (i < 4) rdb(par ^ 1, 0, i); else rda(par ^ 1, i - 4); } else { if (i < 4) rda(par ^ 1, i + 4); else rdb(par ^ 1, 1, i - 4); } }
        if (CFG == 0 && last && n >= 3 && i >= 4 && i < 6) { const int hm = i - 4, hn = n - 1;
          ldsw<f32x4>(hn < 4 ? rawk + (hn & 1) * 4096 + hm * 1024 : rawv + hm * 2048, acc[hm][hn % NT]); }
        SB;
      }
    }
    if (CFG == 0 && last) UNR for (int hm = 1; hm >= 0; hm--) ldsw<f32x4>(rawv + hm * 2048 + 1024, acc[hm][5 % NT]);
  };
  SB; lbar();
  STAMP(CFG == 0 ? 12 : CFG == 2 ? 22 : CFG == 1 ? 28 : 36);
  UNR for (int i = 0; i < 4; i++) rdb(0, 0, i);
  UNR for (int i = 0; i < 8; i++) rda(0, i);
  UNR for (int i = 0; i < 4; i++) rdb(0, 1, i);
  SB;
  stage(0, 0, true, false);
  _Pragma("nounroll") for (int t = 1; t < KT - 1; t += 2) { stage(1, t, false, false); stage(0, t + 1, false, false); }
  if (CFG == 0) { rawk = lds0 + L_TR + (wm * 2 + wn) * 8192 + lane * 16; rawv = lds0 + L_RV + (wm * 2 + wn) * 4096 + lane * 16; pin(rawk); pin(rawv); SB; }
  if (CFG != 3) stage(1, KT - 1, false, true);
  else {
    waitlgkm<4>(); SB;
    UNR for (int j = 0; j < 32; j++) { const int p = j >> 1, n = p < 4 ? 0 : 1 + ((p - 4) % 6) / 2, m = p < 4 ? 3 - p : (p < 10 ? 0 : 2) + ((p ^ (p >> 1)) & 1), ks = (j ^ p) & 1;
      acc[m][n % NT] = mfma(cat(fb[n][ks][0], fb[n][ks][1]), fa[1][m][ks], acc[m][n % NT]); SB;
      if (j < 8) rdb(1, 2 + (j >> 2), j & 3); else if (j >= 16) send(j - 16);
      SB; }
  }
}

template <int CFG> struct Loader {
  using C = Cfg<CFG>; using L = Lay<CFG>; static constexpr int N = C::N, KT = C::KT, PER = L::PER, PB = L::PB, NS = C::NS, OWN = C::OWN;
  const char *ab, *bb; unsigned av, bv[2], hv[2], la, lb, lh; int ka, kb, own, r0;
  DI int nxt(int k) const { if (CFG > 1) return k + 1 == KT ? 0 : k + 1; const int n = (k & 8) | ((k + 1) & 7); return n == r0 ? (r0 ^ 8) & (CFG == 0 ? 15 : 12) : n; }
  DI int skip(int k) const { if (OWN && k == own) k = nxt(k + OWN - 1); return k; }
  DI void init(const void* A, int akt, const u16* W, int k0, int col0, int hcol, int rot, int own_, int tm, int lw, int lane, unsigned lds0) {
    const int r = lane >> 3, hr = lane >> 2, t = lw & 1;
    ab = (const char*)A + ((size_t)tm * akt + k0) * 16384 + lw * 4096; av = lane * 16;
    bb = (const char*)W + ((size_t)(k0 * 64 + (lw - (CFG == 3 ? t : 0)) * 16) * N + col0) * 2 + (CFG == 0 ? 2048 : 0);
    bv[0] = r * N * 2 + (((lane & 7) ^ (((r >> 1) & 3) << 1)) * 16);
    hv[0] = hv[1] = hr * N * 2 + (((lane & 3) ^ (((hr >> 2) & 1) << 1)) * 16) + (hcol - col0) * 2 + 64;
    if (CFG == 3) { hv[0] = bv[0] + (4 * t + 4 * (r >> 2)) * N * 2; hv[1] = hv[0] + 16 * N * 2; bv[0] += t * 16 * N * 2; }
    bv[1] = bv[0] + 8 * N * 2;
    la = lds0 + lw * 4096; lb = lds0 + C::B0 + lw * 2048; lh = lds0 + C::B0 + C::NP * 8192 + lw * 1024; own = own_; r0 = rot; ka = kb = skip(rot);
  }
  DI unsigned bslot(int slot) const { slot += C::BS0; if (slot >= NS) slot -= NS; return slot * L::SBB; }
  DI bool nat(int k) const { return CFG == 3 && ((0x1020408u >> k) & 1); }
  template <int I> DI void piece(const char* b, unsigned v0, unsigned v1, unsigned so) const {
    if (I < 2 * C::NP) glds16i<(I >> 1) * 8192 + (I & 1) * 1024, CFG == 0 ? ((I >> 1) - 1) * 2048 : (I >> 1) * 128>(b, (I & 1) ? v1 : v0, lb + so); else glds16i<0, -64>(b, hv[0], lh + so); }
  DI void issueA(int slot) { glds16x4(ab + (size_t)ka * 16384, av, la + slot * 16384); ka = skip(nxt(ka)); }
  DI void issueBk(int slot, int k) const { const char* b = bb + (size_t)k * (128 * N); const bool n = nat(k); const unsigned v0 = n ? hv[0] : bv[0], v1 = n ? hv[1] : bv[1], so = bslot(slot);
    sfor<0, PB>([&](auto I) { piece<I.value>(b, v0, v1, so); }); }
  DI void issueB(int slot) { issueBk(slot, kb); kb = skip(nxt(kb)); }
  const char *pa, *pb; unsigned w0, w1;
  DI void prime() { pa = opq(ab + (size_t)ka * 16384); pb = opq(bb + (size_t)ka * (128 * N)); const bool n = nat(ka); w0 = n ? hv[0] : bv[0]; w1 = n ? hv[1] : bv[1]; }
  DI void issue(int slot) { const unsigned so = bslot(slot); const char* b = pb;
    glds16x4(pa, av, la + slot * 16384);
    piece<0>(b, w0, w1, so); ka = opq(skip(nxt(opq(ka))));
    piece<1>(b, w0, w1, so); pa = opq(ab + (size_t)(unsigned)opq(ka) * 16384);
    if (PB > 2) piece<2 % PB>(b, w0, w1, so);
    pb = opq(bb + (size_t)((unsigned)opq(ka) * (unsigned)(128 * N)));
    sfor<3, PB>([&](auto I) { piece<I.value>(b, w0, w1, so); });
    if (CFG == 3) { const bool n = nat(opq(ka)); w0 = n ? hv[0] : bv[0]; w1 = n ? hv[1] : bv[1]; pin(w0); pin(w1); }
  }
  DI u64 first_need(int n, int k0) const { u64 m = 0; int k = ka;
    for (int i = 0; i < n; i++) { if (CFG == 3) m |= (1ull << ((2 * (k0 + k)) / 7)) | (1ull << ((2 * (k0 + k) + 1) / 7)); else m |= 1ull << k; k = skip(nxt(k)); }
    return m; }
  template <int E1, class HF, class HK, class H1> DI void run3(HF hf, HK hk, H1 h1) {
    int e0 = 0;
    UNR for (int t = 1; t <= 5; t++) { issue((t - 1) % 3); const int e1 = hf(t); waitx<PER>(e0 + e1); bar(); e0 = e1; }
    int slot = 2;
    _Pragma("nounroll") for (int t = 6; t <= KT - 3; t++) { issue(slot); slot++; if (slot == NS) slot = 0; const int e1 = hk(t); waitx<PER, 0, 4>(e0 + e1); bar(); e0 = e1; }
    h1(); waitvm<E1>(); bar();
  }
  template <int E1, bool W1, class H0, class H1, class HK> DI void run4(H0 h0, H1 h1, HK hk) {
    UNR for (int s = 0; s < 4; s++) issueA((OWN + s) & 3);
    prime(); waitvm<12>(); bar(); h0(); waitvm<8>(); bar();
    issue(OWN & 3); waitvm<4 + PER>(); bar();
    int slot = (OWN + 1) & 3, e0 = 0, e1 = 0;
    _Pragma("nounroll") for (int t = 2; t <= KT - OWN - 4; t++) { issue(slot); slot = (slot + 1) & 3;
      const int e2 = hk(t); waitx<2 * PER, 0, 1>(e0 + e1 + e2); e0 = e1; e1 = e2; bar(); }
    if (W1) { h1(); waitvm<PER + E1>(); bar(); waitvm<0>(); bar(); } else { waitvm<PER>(); bar(); h1(); waitvm<E1>(); bar(); }
  }
};
DI u64* flagp(char* ws, int tile, int line, int word) { return (u64*)(ws + WS_FLAG) + (tile * 8 + line) * 16 + word; }
DI u64 wait_flags(u64* f, int lane, int n, u64 tag, u64 need) {
  u64 got = 0;
  _Pragma("nounroll") for (int spin = 0; spin < SPIN_CAP; spin++) { u64 v = tag;
    if (lane < n) asm volatile("global_atomic_or_x2 %0, %1, %2, off sc0 sc1\n\ts_waitcnt vmcnt(0)" : "=&v"(v) : "v"(f + lane), "v"(0ull) : "memory");
    got = __ballot(v == tag);
    if ((got & need) == need) break;
    asm volatile("s_sleep 1" ::: "memory");
  }
  return got;
}
struct Sync { u64* f; int n; u64 tag; bool all;
  template <class H> DI void run(char* ws, int line, const Blk& P, const Blk& C, int lw, int lane, u64 tag_, u64 need, H pre) {
    bar();
    tag = tag_; f = flagp(ws, C.tm * 16 + C.tn, line, 0); n = line == 0 ? 32 : 16; all = true;
    if (lw == 0) { const int i = lane & 15, grp = lane >> 4;
      if (grp < n / 16) __hip_atomic_store(grp == 0 ? flagp(ws, P.tm * 16 + i, line, P.tn) : flagp(ws, i * 16 + C.tn, 1, C.tm), tag, __ATOMIC_RELAXED, __HIP_MEMORY_SCOPE_AGENT); }
    pre();
    if (lw == 0) all = wait_flags(f, lane, n, tag, need) == ~0ull;
    lbar();
  }
  DI void rest(int lane) { if (!all) wait_flags(f, lane, n, tag, ~0ull); }
};
DI int modcol(int lane) { return 4 * (lane & 15) + 2 * (lane >> 5) + ((lane >> 4) & 1); }
DI float modvec(const char* ws, unsigned part, int lane, u32x4 (&p)[4], bool have) {
  if (!have || __any(max(max(umax4(p[0]), umax4(p[1])), max(umax4(p[2]), umax4(p[3]))) == 0xffffffffu)) { const rsrc_t r = mkrsrc(ws, part); poll(p, [&](int i) { return ld_dev(r, lane * 16, i * 1024); }); }
  const f32x4 a = (asf(p[0]) + asf(p[1])) + (asf(p[2]) + asf(p[3]));
  return rbf(sw16_add(sw32_add(a[0], a[2]), sw32_add(a[1], a[3])));
}
DI float modvec(const char* ws, unsigned part, int lane) { u32x4 p[4]; return modvec(ws, part, lane, p, false); }
DI void put_et(unsigned lds0, int vec, int k, float v) { ldsw<u16>(lds0 + L_ET + vec * 128 + fragpos(k), (u16)(__float_as_uint(v) >> 16)); }

template <int CFG, int NTL> struct UV {
  u32x4 o; unsigned oa;
  DI void form(const rsrc_t& r, int tm, int lw, int lane, unsigned lds0, unsigned slotoff) {
    const bf16x8 e0 = ldsr<bf16x8>(lds0 + L_ET + (lane & 1) * 128 + (lane >> 4) * 16), e1 = ldsr<bf16x8>(lds0 + L_ET + (lane & 1) * 128 + 64 + (lane >> 4) * 16);
    f32x4 d[NTL]; const int i2 = min((lane & 15) >> 1, NTL - 1);
    UNR for (int i = 0; i < NTL; i++) { const int tl = min(lw * NTL + i, 2 * Cfg<CFG>::NT - 1); const unsigned a = lds0 + slotoff + btile<CFG>(tl, lane), ts = tl < 4 * Cfg<CFG>::NP ? 2048 : 1024;
      d[i] = mfma(wfrag(a + 2 * ts, ts), e1, mfma(wfrag(a, ts), e0, f32x4{0, 0, 0, 0})); }
    UNR for (int k = 0; k < 4; k++) { float t = d[0][k]; UNR for (int i = 1; i < NTL; i++) t = i2 == i ? d[i][k] : t; o[k] = __float_as_uint(t); }
    oa = r.base + (lw * NTL + i2) * 2048 + tm * 128 + (lane & 1) * 64 + (lane >> 4) * 16; pin(o); pin(oa);
  }
  DI void send(const char* ws) { st_dev(o, mkrsrc(ws, 0), oa, 0); }
  u32x4 v[8]; unsigned ia;
  DI void aim(int lw, int lane) { ia = (lw * NTL + min((lane >> 3) & 3, NTL - 1)) * 2048 + (lane >> 5) * 1024 + (lane & 7) * 16; pin(ia); }
  DI void fetch(const char* base) { sfor<0, 8>([&](auto I) { gld_dev<I.value * 128>(v[I.value], base, ia); }); }
  DI f32x4 sum() const { return ((asf(v[0]) + asf(v[1])) + (asf(v[2]) + asf(v[3]))) + ((asf(v[4]) + asf(v[5])) + (asf(v[6]) + asf(v[7]))); }
  DI void finish(const rsrc_t& r, int lane, unsigned tab, f32x2 bias) {
    pin(v); f32x4 a = sum();
    if (__any(hasnan(a))) { poll(v, [&](int i) { return ld_dev(r, ia, i * 128); }); a = sum(); }
    ldsw<f32x2>(tab + (ia >> 11) * 128 + (lane & 7) * 16 + (lane >> 5) * 8, f32x2{sw32_add(a[0], a[2]), sw32_add(a[1], a[3])} + bias);
  }
};
template <int NPART> struct Stats {
  u32x4 v[NPART / 4];
  DI void fetch(int i, const char* st, int tm, int lw, int lane) { gld_dev<0>(v[i], st + (size_t)(tm * NPART + i) * 1024 + lw * 256, (lane >> 4) * (NPART * 256) + (lane & 15) * 16); }
  DI f32x4 sum() const { f32x4 s = asf(v[0]); UNR for (int i = 1; i < NPART / 4; i++) s += asf(v[i]); return s; }
  template <bool MARKED> DI void finish(const char* ws, unsigned st, int tm, int lw, int lane, unsigned tab) { pin(v); f32x4 s = sum(); float q0 = 0, q1 = 0;
    if (MARKED && __any(hasnan(s))) { poll(v, [&](int i) { return ld_dev(mkrsrc(ws, st + (tm * NPART + i) * 1024 + lw * 256), (lane >> 4) * (NPART * 256) + (lane & 15) * 16, 0); }); s = sum(); }
    float t = sw32_add(s[0], s[2]); t = sw16_add(t, t) * (1.f / NPART);
    const auto mm = __builtin_amdgcn_permlane32_swap(__float_as_uint(t), __float_as_uint(t), false, false);
    UNR for (int i = 0; i < NPART / 4; i++) { const float d0 = __uint_as_float(v[i][0]) - __uint_as_float(mm[0]), d1 = __uint_as_float(v[i][2]) - __uint_as_float(mm[1]); q0 = __builtin_fmaf(d0, d0, q0); q1 = __builtin_fmaf(d1, d1, q1); }
    float q = sw32_add(__builtin_fmaf((float)(D_ / NPART), q0, s[1]), __builtin_fmaf((float)(D_ / NPART), q1, s[3])); q = sw16_add(q, q);
    const float rstd = __builtin_amdgcn_rsqf(q * (1.f / D_) + 1e-5f);
    ldsw<float>(tab + ((lane >> 4) & 1) * 512 + (lw * 32 + (lane & 15) * 2 + (lane >> 5)) * 4, (lane & 16) ? -t * rstd : rstd); }
};

template <bool MAX> DI float fold4(float a, float b, float c, float d) {
  auto op = [](unsigned x, unsigned y) { return MAX ? __builtin_elementwise_maximum(__uint_as_float(x), __uint_as_float(y)) : __uint_as_float(x) + __uint_as_float(y); };
  const auto r0 = __builtin_amdgcn_permlane32_swap(__float_as_uint(a), __float_as_uint(b), false, false), r1 = __builtin_amdgcn_permlane32_swap(__float_as_uint(c), __float_as_uint(d), false, false);
  const auto r2 = __builtin_amdgcn_permlane16_swap(__float_as_uint(op(r0[0], r0[1])), __float_as_uint(op(r1[0], r1[1])), false, false);
  return op(r2[0], r2[1]);
}
DI void spread4(float t, float& a, float& b, float& c, float& d) {
  const auto r2 = __builtin_amdgcn_permlane16_swap(__float_as_uint(t), __float_as_uint(t), false, false);
  const auto r0 = __builtin_amdgcn_permlane32_swap(r2[0], r2[0], false, false), r1 = __builtin_amdgcn_permlane32_swap(r2[1], r2[1], false, false);
  a = __uint_as_float(r0[0]); b = __uint_as_float(r0[1]); c = __uint_as_float(r1[0]); d = __uint_as_float(r1[1]);
}
DI void feat2(const bf16x8 (&wf)[2], bf16x8 x0, bf16x8 x1, bf16x8& fp0, bf16x8& fn0, bf16x8& fp1, bf16x8& fn1) {
  const f32x4 zero = {0, 0, 0, 0}; f32x4 p[2][2] = {{mfma(wf[0], x0, zero), mfma(wf[1], x0, zero)}, {mfma(wf[0], x1, zero), mfma(wf[1], x1, zero)}};
  float mx[2], nx[2], sp[2], sn[2]; f32x4 a[2][2], b[2][2];
  UNR for (int i = 0; i < 2; i++) { const f32x4 u = p[i][0], v = p[i][1];
    mx[i] = fmax3(fmax3(fmax3(u[0], u[1], u[2]), u[3], v[0]), fmax3(v[1], v[2], v[3]), v[3]); nx[i] = fmax3(fmax3(fmax3(-u[0], -u[1], -u[2]), -u[3], -v[0]), fmax3(-v[1], -v[2], -v[3]), -v[3]); }
  spread4(fold4<true>(mx[0], mx[1], nx[0], nx[1]), mx[0], mx[1], nx[0], nx[1]);
  UNR for (int i = 0; i < 2; i++) UNR for (int h = 0; h < 2; h++) UNR for (int j = 0; j < 4; j += 2) { const f32x2 t = {p[i][h][j], p[i][h][j + 1]};
    f32x2 d = t - f32x2{mx[i], mx[i]}, e = -t - f32x2{nx[i], nx[i]}; asm("" : "+v"(d), "+v"(e));
    a[i][h][j] = __builtin_amdgcn_exp2f(d[0]); a[i][h][j + 1] = __builtin_amdgcn_exp2f(d[1]); b[i][h][j] = __builtin_amdgcn_exp2f(e[0]); b[i][h][j + 1] = __builtin_amdgcn_exp2f(e[1]); }
  UNR for (int i = 0; i < 2; i++) { sp[i] = hsum4(a[i][0] + a[i][1]); sn[i] = hsum4(b[i][0] + b[i][1]); }
  spread4(__builtin_amdgcn_rcpf(fold4<false>(sp[0], sp[1], sn[0], sn[1])), sp[0], sp[1], sn[0], sn[1]);
  fp0 = pack8(a[0][0] * sp[0], a[0][1] * sp[0]); fn0 = pack8(b[0][0] * sn[0], b[0][1] * sn[0]); fp1 = pack8(a[1][0] * sp[1], a[1][1] * sp[1]); fn1 = pack8(b[1][0] * sn[1], b[1][1] * sn[1]);
}

constexpr float L2E = 1.4426950408889634f;
DI void kv_prep(const f32x4 (&k)[2][2], const f32x4 (&vv)[2][2], const bf16x8 (&wf)[2], int hf, int w, int lane, unsigned lds0) {
  const int g = lane >> 4, r = lane & 15, row0 = (w >> 1) * 64 + hf * 32; const unsigned uva = lds0 + L_UVT + (w & 1) * 256;
  RowSt st[2]; float u[2], v[2]; f32x4 rs[2], nm[2];
  UNR for (int m = 0; m < 2; m++) st[m].set(lds0 + L_STT + (row0 + m * 16 + r) * 4, L2E);
  const f32x4 u0 = ldsr<f32x4>(uva + 512 + g * 16), v0 = ldsr<f32x4>(uva + 576 + g * 16) * L2E, u1 = ldsr<f32x4>(uva + 640 + g * 16), v1 = ldsr<f32x4>(uva + 704 + g * 16) * L2E;
  UNR for (int c = 0; c < 2; c++) { u[c] = ldsr<float>(uva + 1024 + c * 128 + r * 4); v[c] = ldsr<float>(uva + 1088 + c * 128 + r * 4); asm("" : "+v"(u[c])); asm("" : "+v"(v[c])); }
  UNR for (int m = 0; m < 2; m++) { const unsigned a = lds0 + L_STT + (row0 + m * 16 + 4 * g) * 4; rs[m] = ldsr<f32x4>(a); nm[m] = ldsr<f32x4>(a + 512); }
  bf16x8 a, b, c, d; feat2(wf, pack8(lnf(k[0][0], st[0], u0, v0), lnf(k[0][1], st[0], u1, v1)), pack8(lnf(k[1][0], st[1], u0, v0), lnf(k[1][1], st[1], u1, v1)), a, b, c, d);
  UNR for (int cc = 0; cc < 2; cc++) ldsw<bf16x8>(lds0 + L_VF + w * 4096 + hf * 2048 + cc * 1024 + lane * 16,
      pack8(fma4(vv[0][cc], rs[0], fma4(nm[0], spl(u[cc]), spl(v[cc]))), fma4(vv[1][cc], rs[1], fma4(nm[1], spl(u[cc]), spl(v[cc])))));
  const unsigned trw = lds0 + L_TR + w * 8192 + hf * 2048 + r * 64 + ((g ^ ((r >> 1) & 3)) * 16);
  ldsw<bf16x8>(trw, a); ldsw<bf16x8>(trw + 4096, b); ldsw<bf16x8>(trw + 1024, c); ldsw<bf16x8>(trw + 1024 + 4096, d);
}

DI void touch_code(int lane, unsigned ldsdummy) {
  const char* pc; unsigned len, back;
  asm volatile("s_getpc_b64 %0\n.Lhere%=:\n\ts_mov_b32 %2, .Lhere%=-.Lcode_begin\n\ts_mov_b32 %1, .Lcode_end-.Lcode_begin" : "=s"(pc), "=s"(len), "=s"(back));
  pc -= back;
  const unsigned o = ((blockIdx.x >> 3) * 16 + (lane & 15)) * 128;
  touch(pc, min(o, len), ldsdummy);
}
struct Args { const u16 *x, *wqkv, *wphi, *wo, *w1, *b1, *w2, *b2, *c, *wm; u16* out; char* ws; };

DI int gxcol(int lane) { return 2 * (lane & 31) + (lane >> 5); }
template <int N> DI void gx_load(const char* ws, unsigned off, int lane, u32x4 (&v)[N]) { UNR for (int i = 0; i < N; i++) v[i] = ld_dev(mkrsrc(ws, WS_GX + off), lane * 16, i * 1024); }
template <int N> DI void gx_gather(const char* ws, unsigned off, int lane, unsigned tag, u32x4 (&v)[N], float (&t)[N]) {
  _Pragma("nounroll") for (int spin = 0; spin < SPIN_CAP; spin++) { unsigned bad = 0;
    UNR for (int i = 0; i < N; i++) bad |= (v[i][1] ^ tag) | (v[i][3] ^ tag);
    if (!__any(bad != 0)) break;
    asm volatile("s_sleep 2" ::: "memory");
    gx_load(ws, off, lane, v);
  }
  UNR for (int i = 0; i < N; i++) t[i] = sw32_add(__uint_as_float(v[i][0]), __uint_as_float(v[i][2]));
}
DI float sum8(const float (&p)[8]) { return fadd(fadd(fadd(p[0], p[1]), fadd(p[2], p[3])), fadd(fadd(p[4], p[5]), fadd(p[6], p[7]))); }

DI u64 phase0(const Args& A, int w, unsigned lds0, unsigned& ts) {
  char* ws = A.ws; const Blk B = blk0(), B1 = blk(); const f32x4 zero = {0, 0, 0, 0};
  const int lane = threadIdx.x & 63, r8 = lane >> 3, g = lane >> 4, r = lane & 15;
  u64 ep; asm volatile("s_load_dwordx2 %0, %1, 0x0" : "=s"(ep) : "s"(ws + WS_EPOCH) : "memory");
  unsigned cw; gld4(cw, A.c, B.tm * 128 + (lane & 31) * 4);
  { const char* wb = (const char*)A.wm + (size_t)(B.tm * 8 + w) * 16 * MODN + B.tn * 128; const unsigned wv = r8 * MODN * 2 + (((lane & 7) ^ (((r8 >> 1) & 3) << 1)) * 16);
    glds16(wb + 2 * D_, wv, lds0 + L_UN + w * 1024); glds16(wb, wv, lds0 + L_UN + 8192 + w * 1024); }
  bar();
  const unsigned rv0 = r8 * 2048 + (((lane & 7) ^ (r8 >> 1)) * 16);
  UNR for (int i = 0; i < 2; i++) glds16((const char*)A.x + ((size_t)(B.tm * 128 + w * 16 + i * 8) * D_ + B.tn * 64) * 2, i ? rv0 ^ 64 : rv0, lds0 + L_XT + (w * 2 + i) * 1024);
  { const int e = min(w * 24 + lane, 191), row = e / 12, l = e - row * 12;
    if (lane < 24) touch((const char*)A.wqkv + (size_t)((B1.tm >> 3) * 512 + B.j * 16) * 6144 + B1.xq * 512, row * 6144 + (l >> 2) * 2048 + (l & 3) * 128, lds0 + L_DUM); }
  unsigned pa = lds0 + L_UN + (w >> 2) * 8192 + ptile(w & 3, lane); pin(pa);
  waitvm<5>(); pin(cw);
  asm volatile("s_waitcnt lgkmcnt(0)" : "+s"(ep) :: "memory");
  const u64 tag = ep + 0x9E3779B97F4A7C15ull;
  if (w == 7) ldsw<unsigned>(lds0 + L_SIL2 + fragpos(2 * (lane & 31)), silu2(cw));
  waitvm<3>(); lbar();
  STAMP(1);
  { const f32x4 d = mfma(wfrag(pa + 4096, 2048), ldsr<bf16x8>(lds0 + L_SIL2 + 64 + g * 16), mfma(wfrag(pa, 2048), ldsr<bf16x8>(lds0 + L_SIL2 + g * 16), zero));
    const bool o = r & 1; const u32x4 gr = {__float_as_uint(o ? d[2] : d[0]), (unsigned)tag, __float_as_uint(o ? d[3] : d[1]), (unsigned)tag};
    if (r < 2) st_dev(gr, mkrsrc(ws, WS_GX + (((w >> 2) * 16 + B.tn) * 16 + B.tm) * 512), g * 32 + r * 16, (w & 3) * 128); }
  asm volatile("" ::: "memory");
  if (w < 3) {
    const int l = w * 8 + r8;
    if (l < 20) st_dev(u32x4{~0u, ~0u, ~0u, ~0u}, mkrsrc(ws, 0), (l < 8 ? WS_MODP + (((l >> 1) * 16 + B.tn) * 16 + B.tm) * 256 + (l & 1) * 128 : WS_UV1 + (B1.tn * 12 + l - 8) * 2048 + B1.tm * 128) + (lane & 7) * 16, 0); }
  if (w == 7 && lane < 6) touch(flagp(ws, B1.tm * 16 + B1.tn, 0, 0), lane * 128, lds0 + L_DUM);
  if (w < 3 || w == 7) waitvm<3>(); else waitvm<2>();
  f32x4 xv[4]; f32x2 stat;
  UNR for (int cc = 0; cc < 4; cc++) { const u32x2 t = ldsr<u32x2>(lds0 + L_XT + (w * 16 + r) * 128 + (((cc * 2 + (g >> 1)) ^ (r >> 1)) * 16) + (g & 1) * 8); xv[cc] = unp(t[0], t[1]); }
  { const float mean = xg_sum(hsum4((xv[0] + xv[1]) + (xv[2] + xv[3]))) * (1.f / 64); f32x4 q = zero;
    UNR for (int cc = 0; cc < 4; cc++) { const f32x4 d = xv[cc] - mean; q = fma4(d, d, q); }
    stat = f32x2{mean, xg_sum(hsum4(q))}; pin(stat); pin(xv); }
  waitvm<0>();
  STAMP(2);
  { float t[1]; u32x4 v[1]; gx_load(ws, B.tn * 8192 + w * 1024, lane, v); gx_gather(ws, B.tn * 8192 + w * 1024, lane, (unsigned)tag, v, t); ldsw<float>(lds0 + L_RED + w * 256 + gxcol(lane) * 4, t[0]); }
  STAMP(3);
  lbar();
  float p[8]; UNR for (int i = 0; i < 8; i++) p[i] = ldsr<float>(lds0 + L_RED + i * 256 + lane * 4);
  ldsw<float>(lds0 + L_SROW + w * 256 + lane * 4, rbf(1.f + rbf(sum8(p))));
  asm volatile("" ::: "memory");
  const rsrc_t ry = outrsrc(A.out, (B.tm * 16 + B.tn) * 16384);
  UNR for (int ks = 0; ks < 2; ks++)
    st_dev(pack8u(xv[2 * ks] * ldsr<f32x4>(lds0 + L_SROW + w * 256 + ks * 128 + g * 16), xv[2 * ks + 1] * ldsr<f32x4>(lds0 + L_SROW + w * 256 + ks * 128 + 64 + g * 16)), ry, lane * 16, w * 2048 + ks * 1024);
  if (g == 0) st_dev8(__builtin_bit_cast(u32x2, stat), mkrsrc(ws, WS_ST1 + (size_t)(B.tm * 16 + B.tn) * 1024), r * 8, w * 128);
  asm volatile("" ::: "memory");
  STAMP(4);
  return tag;
}

DI void loader_role(const Args& A, int lw, unsigned lds0, u64 tag, unsigned& ts) {
  char* ws = A.ws; const Blk B = blk(), B0 = blk0(); const f32x4 zero = {0, 0, 0, 0};
  int lane = threadIdx.x & 63; pin(lane);
  const int odd = B.tn & 1; unsigned b1v;
  Loader<2> G2; Sync Y; auto none = [](int) { return 0; };
  unsigned tv; { const int e = min(lane, 55), row = e / 14; tv = row * 7168 + (e - row * 14) * 128; pin(tv); }
  auto w1half = [&]() { touch(opq((const char*)A.w1) + (size_t)((B.tm >> 3) * 512 + B.j * 16 + lw * 4) * 7168 + B.xq * 1792, tv, lds0 + L_DUM); };
  {
    const int g = lane >> 4, r = lane & 15, q = r >> 2, p = lane & 3, r8 = lane >> 3;
    Loader<0> G; G.init(A.out, 16, A.wqkv, 0, B.tn * 64, 0, B.tm, 0, B.tm, lw, lane, lds0);
    G.issueB(0); G.issueB(1);
    const rsrc_t ruv = mkrsrc(ws, WS_UV1 + B.tn * 12 * 2048);
    UV<0, 3> U; Stats<16> S;
    unsigned rwv = r8 * MODN * 2 + (((lane & 7) ^ (((r8 >> 1) & 3) << 1)) * 16); pin(rwv);
    waitvm<12>();
    STAMP(5);
    Y.run(ws, 0, B0, B, lw, lane, tag, G.first_need(3, 0), [&]() { waitvm<6>(); });
    STAMP(11);
    G.issueA(0); G.issueA(1); U.form(ruv, B.tm, lw, lane, lds0, 0); G.prime(); waitvm<4>(); bar();
    G.issue(2); Y.rest(lane); waitvm<10>(); bar();
    auto rest = [&](auto I0, auto I1) {
      const char* wb = opq((const char*)A.wm) + (size_t)B0.tm * 128 * MODN + (lw + 2) * 2 * D_ + B0.tn * 128; const unsigned l0 = lds0 + L_REST + lw * 8192;
      sfor<I0.value, I1.value>([&](auto I) { glds16i<I.value * 1024>(wb + I.value * 16 * MODN, rwv, l0); }); };
    constexpr std::integral_constant<int, 0> c0; constexpr std::integral_constant<int, 2> c4; constexpr std::integral_constant<int, 8> c8;
    U.aim(lw, lane);
    G.run3<9 + c4.value>(
      [&](int t) { if (t <= 4) S.fetch(t - 1, ws + WS_ST1, B.tm, lw, lane); else U.send(ws); return 1; },
      [&](int t) { if (t == 11 && lw == 0) { touch_code(lane, lds0 + L_DUM); return 1; } return 0; },
      [&]() { U.fetch(opq(ws) + ruv.base); glds16((const char*)A.wphi + B.tn * 4096 + lw * 1024, lane * 16, lds0 + L_WPHI + lw * 1024); rest(c0, c4); });
    rest(c4, c8);
    prio(1);
    S.finish<false>(ws, 0, 0, lw, lane, lds0 + L_STT);
    waitvm<8>(); U.finish(ruv, lane, lds0 + L_UVT, f32x2{0, 0});
    G2.init(ws + WS_O, 16, A.wo, 0, B.tn * 64, 0, B.rot, 0, B.tm, lw, lane, lds0);
    prio(0);
    lbar();
    { bf16x8 wf[2]; f32x4 k[2][2], vv[2][2];
      UNR for (int m = 0; m < 2; m++) UNR for (int c = 0; c < 2; c++) { k[m][c] = ldsr<f32x4>(lds0 + L_TR + lw * 8192 + c * 4096 + m * 1024 + lane * 16); vv[m][c] = ldsr<f32x4>(lds0 + L_RV + lw * 4096 + (m * 2 + c) * 1024 + lane * 16); }
      UNR for (int c = 0; c < 2; c++) wf[c] = wfrag(lds0 + L_WPHI + (lw & 1) * 2048 + (4 * g + q) * 64 + c * 32 + p * 8, 1024);
      UNR for (int s = 0; s < 4; s++) G2.issueB(s);
      { const int tl = min(lw * 4 + ((lane >> 3) & 3), 13);
        gld4(b1v, A.b1 + B.tn * 224, (tl < 12 ? odd * 64 + tl * 32 : (odd ? 0 : 384) + (tl - 12) * 32) + (lane & 3) * 8 + (lane >> 5) * 4); }
      kv_prep(k, vv, wf, 0, lw, lane, lds0); }
    if (lw == 0 && lane < 16) __hip_atomic_store(flagp(ws, lane * 16 + B.tn, 5, B.tm), tag, __ATOMIC_RELAXED, __HIP_MEMORY_SCOPE_AGENT);
    lbar();
    prio(1);
    const int hd = lw >> 1, sg = lw & 1;
    const u32x4 one4 = {0x3f803f80u, 0x3f803f80u, 0x3f803f80u, 0x3f803f80u}; const bf16x8 ones = __builtin_bit_cast(bf16x8, one4);
    const unsigned tra = lds0 + L_TR + hd * 8192 + sg * 4096 + (4 * g + q) * 64 + ((p ^ (((4 * g + q) >> 1) & 3)) * 16), vfa = lds0 + L_VF + hd * 4096 + lane * 16;
    f32x4 kv[2][2] = {{zero, zero}, {zero, zero}}, z[2] = {zero, zero};
    UNR for (int k = 0; k < 4; k++) { const bf16x8 v0 = ldsr<bf16x8>(vfa + (k >> 1) * 8192 + (k & 1) * 2048), v1 = ldsr<bf16x8>(vfa + (k >> 1) * 8192 + (k & 1) * 2048 + 1024);
      UNR for (int ft = 0; ft < 2; ft++) { const bf16x8 af = wfrag(tra + (k >> 1) * 16384 + (k & 1) * 2048 + ft * 8, 1024);
        kv[ft][0] = mfma(af, v0, kv[ft][0]); kv[ft][1] = mfma(af, v1, kv[ft][1]); z[ft] = mfma(af, ones, z[ft]); } }
    const rsrc_t rk = mkrsrc(ws, WS_KV1 + (size_t)(B.tn * 4 + lw) * 32768), rz = mkrsrc(ws, WS_ZP + (size_t)(B.tn * 4 + lw) * 2048), r2 = mkrsrc(ws, WS_KV2 + (size_t)B.tn * 131072);
    UNR for (int c = 0; c < 2; c++) st_dev(pack8u(kv[0][c], kv[1][c]), rk, lane * 16, B.tm * 2048 + c * 1024);
    { const bool o = lane & 1; const f32x4 zs = {o ? z[1][0] : z[0][0], o ? z[1][1] : z[0][1], o ? z[1][2] : z[0][2], o ? z[1][3] : z[0][3]};
      if ((lane & 14) == 0) st_dev(__builtin_bit_cast(u32x4, zs), rz, (lane & 1) * 64 + g * 16, B.tm * 128); }
    STAMP(16);
    u64 an = 0; asm volatile("" : "+v"(an));
    asm volatile("s_mov_b64 exec, 0xffff\n\tglobal_atomic_or_x2 %0, %1, %0, off sc0 sc1\n\ts_mov_b64 exec, -1" : "+v"(an) : "v"(flagp(ws, B.tm * 16 + B.tn, 5, lane & 15)) : "memory");
    waitvm<4>();
    f32x4 md = gemv64(lds0 + L_REST + lw * 8192, ldsr<bf16x8>(lds0 + L_SIL2 + g * 16), ldsr<bf16x8>(lds0 + L_SIL2 + 64 + g * 16), lane); pin(md);
    auto modout = [&]() { if (r < 4) st_dev(__builtin_bit_cast(u32x4, md), mkrsrc(ws, WS_MODP + (size_t)((lw * 16 + B0.tn) * 16 + B0.tm) * 256), r * 64 + g * 16, 0); };
    waitvm<0>(); pin(an); pin(b1v);
    if ((__ballot(an == tag) & 0xffff) != 0xffff) wait_flags(flagp(ws, B.tm * 16 + B.tn, 5, 0), lane, 16, tag, ~0ull);
    u32x4 h[2], zz[2]; poll(h, [&](int i) { return ld_dev(rk, (lane >> 3) * 2048 + (lane & 7) * 16, B.tm * 128 + i * 16384); });
    UNR for (int i = 0; i < 2; i++) gld_dev<0>(zz[i], ws + rz.base + i * 64, r * 128 + g * 16);
    pin(h);
    { float e[4], o[4];
      UNR for (int k = 0; k < 4; k++) { e[k] = fadd(blo(h[0][k]), blo(h[1][k])); o[k] = fadd(bhi(h[0][k]), bhi(h[1][k])); }
      float te = fold4<false>(e[0], e[1], e[2], e[3]), to = fold4<false>(o[0], o[1], o[2], o[3]); te += dpp<0x128>(te); to += dpp<0x128>(to);
      spread4(__uint_as_float(cvtpk(te, to)), e[0], e[1], e[2], e[3]);
      const u32x4 sum = {__float_as_uint(e[0]), __float_as_uint(e[1]), __float_as_uint(e[2]), __float_as_uint(e[3])};
      UNR for (int i = 0; i < 2; i++) st_dev(sum, r2, (lane >> 3) * 8192 + (lane & 7) * 16, i * 65536 + lw * 2048 + B.tm * 128); }
    STAMP(17);
    modout();
    waitvm<3>(); pin(zz);
    if (__any(max(umax4(zz[0]), umax4(zz[1])) == 0xffffffffu)) poll(zz, [&](int i) { return ld_dev(rz, r * 128 + g * 16, i * 64); });
    ldsw<bf16x8>(lds0 + L_KX + lw * 1024 + lane * 16, pack8(row16_sum4(asf(zz[0])), row16_sum4(asf(zz[1]))));
    waitvm<0>();
    lbar();
    prio(0);
  }
  Loader<1> G3;
  {
    Y.run(ws, 2, B, B, lw, lane, tag, G2.first_need(4, 0), [&]() {});
    STAMP(21);
    G3.init(A.out, 16, A.w1, 0, B.tn * 224 + odd * 32, B.tn * 224 + (odd ? 0 : 192), B.tm, B.tn, B.tm, lw, lane, lds0);
    const int tk = 2 + (B.j >> 2);
    G2.run4<7, false>([&]() { Y.rest(lane); }, [&]() { G3.issueBk(0, B.tn); }, [&](int t) { if (t == tk) { w1half(); return 1; } return 0; });
    if (blockIdx.x == 0 && lw == 0 && lane == 0) __hip_atomic_store((u64*)(ws + WS_EPOCH), tag, __ATOMIC_RELAXED, __HIP_MEMORY_SCOPE_AGENT);
  }
  Loader<3> G4; const int own4 = (7 * (B.tn & 7) + 1) >> 1;
  {
    const rsrc_t ruv = mkrsrc(ws, WS_UV2 + B.tn * 16 * 2048);
    UV<1, 4> U; Stats<16> S; const int t4 = 7 + ((B.j + 16 * (B.tm >> 3)) & 31) * 7 / 32;
    bar();
    G3.issueB(1);
    waitvm<7>();
    Y.run(ws, 3, B, B, lw, lane, tag, G3.first_need(2, 0), [&]() {
      if (lw == 1 || lw == 2) { const f32x4 s = ldsr<f32x4>(lds0 + L_UVT + (lw - 1) * 1024 + lane * 16); const float d = s[0] - s[2];
        st_dev8(__builtin_bit_cast(u32x2, f32x2{0.5f * fadd(s[0], s[2]), __builtin_fmaf(16.f * d, d, fadd(s[1], s[3]))}), mkrsrc(ws, WS_ST2 + (B.tm * 16 + B.tn) * 1024), lane * 8, (lw - 1) * 512); }
      G3.issueB(2); waitvm<7>(); });
    STAMP(27);
    G3.issueA(1); G3.issueA(2); U.form(ruv, B.tm, lw, lane, lds0, B.tm == B.tn ? 2 * Lay<1>::SBB : 0); G3.prime();
    f32x2 bias = (lane & 4) ? f32x2{blo(b1v), bhi(b1v)} : f32x2{0, 0}; pin(bias); U.aim(lw, lane);
    Y.rest(lane); waitvm<4>(); bar();
    G3.run3<8>(
      [&](int t) { if (t == 1) U.send(ws); else S.fetch(t - 2, ws + WS_ST2, B.tm, lw, lane); return 1; },
      [&](int t) { int e = 0;
        if (t == t4) {
          const char* p = opq((const char*)A.w2) + (size_t)((B.xq >> 1) * 1792 + B.j * 56 + lw * 14) * 2048 + (B.xq & 1) * 1024; const unsigned v = min(lane >> 3, 6) * 2048 + (lane & 7) * 128;
          touch(p, v, lds0 + L_DUM); touch(p + 7 * 2048, v, lds0 + L_DUM); e += 2; }
        if (t == 11 && lw == 0) { touch_code(lane, lds0 + L_DUM); e += 1; }
        return e; },
      [&]() { U.fetch(opq(ws) + ruv.base); });
    { const int r0 = (B.rot * 25) >> 4; G4.init(ws + WS_H1, 56, A.w2, (B.tn >> 3) * 28, (B.tn & 7) * 128, 0, r0 < own4 ? r0 : r0 + 3, own4, B.tm, lw, lane, lds0); G4.la ^= (B.tn & 8) ? 0 : 4096; }
    G4.issueBk(0, own4);
    prio(1);
    S.finish<true>(ws, WS_ST2, B.tm, lw, lane, lds0 + L_STT);
    waitvm<4>(); U.finish(ruv, lane, lds0 + L_UVT, bias);
    prio(0);
    lbar();
    UNR for (int s = 1; s < 3; s++) G4.issueBk(s, own4 + s);
  }
  {
    waitvm<0>(); bar();
    G4.issueB(3); bar(); G4.issueB(0); bar(); G4.issueB(1);
    Y.run(ws, 4, B, B, lw, lane, tag, G4.first_need(4, (B.tn >> 3) * 28), [&]() { G4.issueB(2); });
    STAMP(35);
    G4.run4<4, true>([&]() { Y.rest(lane); }, [&]() {
      glds16x4(opq(ws) + WS_X1 + (size_t)(B.tm * 16 + (B.tn & 7) * 2 + (lw & 1)) * 16384 + ((lw >> 1) * 4 + (B.tn >> 3) * 2) * 2048, lane * 16, lds0 + L_OT + lw * 4096); }, none);
    asm volatile(".Lcode_end:");
  }
}

DI void compute_role(const Args& A, int w, unsigned lds0, u64 tag, unsigned& ts) {
  char* ws = A.ws; const Blk B = blk(); const f32x4 zero = {0, 0, 0, 0}; const u32x4 mark = {~0u, ~0u, ~0u, ~0u};
  const int wm = w >> 1, wn = w & 1; int lane = threadIdx.x & 63; pin(lane);
  unsigned b2v;
  {
    const int g = lane >> 4, r = lane & 15, q = r >> 2, p = lane & 3;
    auto marks = [&](int lw) { const int r8 = lane >> 3;
      UNR for (int c = 0; c < 4; c++) st_dev(mark, mkrsrc(ws, c < 2 ? WS_KV1 + (B.tn * 4 + lw) * 32768 + B.tm * 2048 : WS_KV2 + (B.tn * 16 + B.tm) * 8192 + lw * 2048), lane * 16, (c & 1) * 1024);
      st_dev(mark, mkrsrc(ws, 0), (r8 == 0 ? WS_ZP + (B.tn * 4 + lw) * 2048 + B.tm * 128 : r8 < 5 ? WS_UV2 + (B.tn * 16 + lw * 4 + r8 - 1) * 2048 + B.tm * 128 : WS_ST2 + blockIdx.x * 1024 + (lw * 2 + (r8 & 1)) * 128) + (lane & 7) * 16, 0); };
    waitvm<0>();
    STAMP(5);
    bar();
    if (w < 2) { float t[8]; u32x4 gv[8]; gx_load(ws, (w * 16 + B.tm) * 8192, lane, gv); gx_gather(ws, (w * 16 + B.tm) * 8192, lane, (unsigned)tag, gv, t);
      const float v = rbf(sum8(t)); put_et(lds0, w, gxcol(lane), w ? v : rbf(1.f + v)); }
    else { __builtin_amdgcn_s_sleep(2); marks(w * 2 - 4); marks(w * 2 - 3); }
    lbar();
    f32x4 acc[4][6];
    compute_loop<0>(acc, lds0, wm, wn, lane, ts, [](int) {});
    STAMP(13);
    waitvm<0>();
    lbar();
    STAMP(14);
    bf16x8 wf[2];
    UNR for (int c = 0; c < 2; c++) wf[c] = wfrag(lds0 + L_WPHI + wn * 2048 + (4 * g + q) * 64 + c * 32 + p * 8, 1024);
    { const f32x4 k[2][2] = {{acc[2][2], acc[2][3]}, {acc[3][2], acc[3][3]}}, vv[2][2] = {{acc[2][4], acc[2][5]}, {acc[3][4], acc[3][5]}}; kv_prep(k, vv, wf, 1, w, lane, lds0); }
    lbar();
    STAMP(15);
    { const int r8 = lane >> 3; const unsigned rv0 = r8 * 2048 + (((lane & 7) ^ (r8 >> 1)) * 16);
      UNR for (int i = 0; i < 4; i++) glds16((const char*)A.x + ((size_t)(B.tm * 128 + (w * 4 + i) * 8) * D_ + B.tn * 64) * 2, (i & 1) ? rv0 ^ 64 : rv0, lds0 + L_RES + (w * 4 + i) * 1024);
      gld2(b2v, A.b2 + ((B.tn & 7) * 2 + wn) * 64, lane * 2); }
    asm volatile("" : "+v"(acc[0][0]), "+v"(acc[0][1]), "+v"(acc[1][0]), "+v"(acc[1][1]), "+v"(acc[2][0]), "+v"(acc[2][1]), "+v"(acc[3][0]), "+v"(acc[3][1]));
    bf16x8 qp[4], qn[4]; RowSt st[4]; const unsigned uva = lds0 + L_UVT + wn * 256;
    UNR for (int m = 0; m < 4; m++) st[m].set(lds0 + L_STT + (wm * 64 + m * 16 + r) * 4, L2E);
    { const f32x4 u0 = ldsr<f32x4>(uva + g * 16), v0 = ldsr<f32x4>(uva + 64 + g * 16) * L2E, u1 = ldsr<f32x4>(uva + 128 + g * 16), v1 = ldsr<f32x4>(uva + 192 + g * 16) * L2E;
      UNR for (int m = 0; m < 4; m += 2) feat2(wf, pack8(lnf(acc[m][0], st[m], u0, v0), lnf(acc[m][1], st[m], u1, v1)),
                                               pack8(lnf(acc[m + 1][0], st[m + 1], u0, v0), lnf(acc[m + 1][1], st[m + 1], u1, v1)), qp[m], qn[m], qp[m + 1], qn[m + 1]); }
    asm volatile("" : "+v"(qp[0]), "+v"(qp[1]), "+v"(qp[2]), "+v"(qp[3]), "+v"(qn[0]), "+v"(qn[1]), "+v"(qn[2]), "+v"(qn[3]) :: "memory");
    STAMP(16);
    UNR for (int i = 0; i < 4; i++) st_dev(mark, mkrsrc(ws, xchoff(B.tm, B.tn, w)), lane * 16, i * 1024);
    bar();
    STAMP(17);
    const rsrc_t r2 = mkrsrc(ws, WS_KV2 + (size_t)(B.tn * 16 + B.tm) * 8192 + wn * 4096);
    u32x4 kk[4]; UNR for (int i = 0; i < 4; i++) kk[i] = ld_dev(r2, lane * 16, i * 1024);
    const bf16x8 z0 = ldsr<bf16x8>(lds0 + L_KX + wn * 2048 + lane * 16), z1 = ldsr<bf16x8>(lds0 + L_KX + wn * 2048 + 1024 + lane * 16);
    float rd[4];
    UNR for (int m = 0; m < 4; m++) rd[m] = __builtin_amdgcn_rcpf(mfma(z1, qn[m], mfma(z0, qp[m], zero))[0] + 1e-6f);
    asm volatile("" : "+v"(rd[0]), "+v"(rd[1]), "+v"(rd[2]), "+v"(rd[3]));
    if (__any(max(max(umax4(kk[0]), umax4(kk[1])), max(umax4(kk[2]), umax4(kk[3]))) == 0xffffffffu)) poll(kk, [&](int i) { return ld_dev(r2, lane * 16, i * 1024); });
    const rsrc_t ro = mkrsrc(ws, WS_O + (size_t)(B.tm * 16 + B.tn) * 16384);
    UNR for (int m = 0; m < 4; m++) {
      const f32x4 n0 = mfma(__builtin_bit_cast(bf16x8, kk[2]), qn[m], mfma(__builtin_bit_cast(bf16x8, kk[0]), qp[m], zero)), n1 = mfma(__builtin_bit_cast(bf16x8, kk[3]), qn[m], mfma(__builtin_bit_cast(bf16x8, kk[1]), qp[m], zero));
      st_dev(pack8u(n0 * rd[m], n1 * rd[m]), ro, lane * 16, (wm * 8 + wn) * 1024 + m * 2048);
    }
    STAMP(18);
    waitvm<0>(); pin(b2v); bar();
    STAMP(19);
  }
  pin(lane);
  const int fp0 = B.tn * 7 + (B.tn & 1) + wn, fph = (B.tn & 1) ? B.tn * 7 : B.tn * 7 + 6;
  {
    const int g = lane >> 4, r = lane & 15;
    u32x4 gp[4]; UNR for (int i = 0; i < 4; i++) gp[i] = ld_dev(mkrsrc(ws, WS_MODP + (48 + (B.tn & 7) * 2 + wn) * 4096), lane * 16, i * 1024);
    { const float v = modvec(ws, WS_MODP + (size_t)((w == 0 ? 0 : w == 3 ? 1 : 2) * 16 + (w < 2 ? B.tn : B.tm)) * 4096, lane), s = (w == 1 || w == 2) ? rbf(1.f + v) : v;
      if (w < 2) ldsw<float>(lds0 + L_GT + w * 256 + modcol(lane) * 4, s); else put_et(lds0, w - 2, modcol(lane), s); }
    f32x4 acc[4][2], xv[4][2], xs[4][2], gate[2], gs[2];
    UNR for (int m = 0; m < 4; m++) UNR for (int n = 0; n < 2; n++) { const u32x2 t = ldsr<u32x2>(lds0 + L_RES + (wm * 64 + m * 16 + r) * 128 + (((wn * 4 + n * 2 + (g >> 1)) ^ (r >> 1)) * 16) + (g & 1) * 8); xv[m][n] = unp(t[0], t[1]); }
    UNR for (int m = 0; m < 4; m++) pin(xv[m]);
    lbar();
    UNR for (int n = 0; n < 2; n++) { gate[n] = ldsr<f32x4>(lds0 + L_GT + wn * 128 + n * 64 + g * 16); const f32x4 sc = ldsr<f32x4>(lds0 + L_GT + 256 + wn * 128 + n * 64 + g * 16); gs[n] = gate[n] * sc;
      UNR for (int m = 0; m < 4; m++) xs[m][n] = xv[m][n] * sc; }
    UNR for (int m = 0; m < 4; m++) pin(xs[m]);
    pin(gate); pin(gs);
    if (w < 2) ldsw<float>(lds0 + L_GB + w * 256 + modcol(lane) * 4, modvec(ws, WS_MODP + (size_t)(48 + (B.tn & 7) * 2 + w) * 4096, lane, gp, true));
    else ldsw<float>(lds0 + L_GB + w * 256 + lane * 4, blo(b2v));
    compute_loop<2>(acc, lds0, wm, wn, lane, ts, [](int) {});
    STAMP(23);
    const unsigned tb = (B.tm * 16 + B.tn) * 16384 + (wm * 8 + wn) * 1024; float mean[4], m2[4]; f32x4 v[4][2];
    UNR for (int m = 0; m < 4; m++) { const u32x4 y = pack8u(fma4(gs[0], acc[m][0], xs[m][0]), fma4(gs[1], acc[m][1], xs[m][1]));
      st_dev(y, outrsrc(A.out, tb), lane * 16, m * 2048); ldsw<u32x4>(lds0 + (wm * 8 + wn) * 1024 + m * 2048 + lane * 16, y); }
    UNR for (int m = 0; m < 4; m++) pin(acc[m]);
    asm volatile("" ::: "memory");
    UNR for (int m = 0; m < 4; m++) { v[m][0] = fma4(gate[0], acc[m][0], xv[m][0]); v[m][1] = fma4(gate[1], acc[m][1], xv[m][1]); mean[m] = hsum4(v[m][0] + v[m][1]); }
    const float mf = fold4<false>(mean[0], mean[1], mean[2], mean[3]) * (1.f / 32); spread4(mf, mean[0], mean[1], mean[2], mean[3]);
    UNR for (int m = 0; m < 4; m++) { const f32x4 d0 = v[m][0] - mean[m], d1 = v[m][1] - mean[m]; m2[m] = hsum4(fma4(d0, d0, d1 * d1)); }
    ldsw<f32x2>(lds0 + L_UVT + (wm * 64 + ((g >> 1) + 2 * (g & 1)) * 16 + r) * 16 + wn * 8, f32x2{mf, fold4<false>(m2[0], m2[1], m2[2], m2[3])});
    STAMP(24);
    asm volatile("" ::: "memory");
    UNR for (int m = 0; m < 4; m++) st_dev(pack8u(v[m][0], v[m][1]), mkrsrc(ws, WS_X1 + tb), lane * 16, m * 2048);
    waitvm<4>();
    STAMP(25);
  }
  pin(lane);
  {
    const int g = lane >> 4, r = lane & 15;
    f32x4 acc[4][7];
    compute_loop<1>(acc, lds0, wm, wn, lane, ts, [](int) {});
    STAMP(29);
    bar();
    STAMP(30);
    f32x4 u[7], v[7];
    UNR for (int n = 0; n < 7; n++) { const unsigned a = lds0 + L_UVT + (n < 6 ? (n >> 1) * 4 + wn * 2 + (n & 1) : 12 + wn) * 128 + g * 16; u[n] = ldsr<f32x4>(a); v[n] = ldsr<f32x4>(a + 64); }
    const rsrc_t rh = mkrsrc(ws, WS_H1 + (size_t)B.tm * 56 * 16384);
    RowSt st[4]; u32x2 h6[4];
    UNR for (int m = 0; m < 4; m++) st[m].set(lds0 + L_STT + (wm * 64 + m * 16 + r) * 4, 1.f);
    auto act = [&](int m, int n) { const f32x4 t = __builtin_elementwise_max(lnf(acc[m][n], st[m], u[n], v[n]), zero); return t * t; };
    UNR for (int n = 0; n < 6; n += 2) { const int fp = fp0 + n;
      const unsigned la = lds0 + (n >> 1) * 16384 + wn * 1024 + wm * 8192 + lane * 16, sw = (B.tn & 8) ? 0 : 4096;
      UNR for (int m = 0; m < 4; m++) { const u32x4 hh = pack8u(act(m, n), act(m, n + 1)); st_dev(hh, rh, lane * 16, (fp >> 1) * 16384 + (fp & 1) * 1024 + wm * 8192 + m * 2048); ldsw<u32x4>(la + (m < 2 ? sw : 4096 - sw) + (m & 1) * 2048, hh); }
      asm volatile("" ::: "memory"); }
    UNR for (int m = 0; m < 4; m++) { const f32x4 t = act(m, 6); h6[m] = u32x2{cvtpk(t[0], t[1]), cvtpk(t[2], t[3])}; }
    UNR for (int m = 0; m < 4; m += 2) { const auto a = __builtin_amdgcn_permlane16_swap(h6[m][0], h6[m + 1][0], false, false), b = __builtin_amdgcn_permlane16_swap(h6[m][1], h6[m + 1][1], false, false);
      st_dev(u32x4{a[0], b[0], a[1], b[1]}, rh, (g & 1) * 2048 + (g >> 1) * 256 + r * 16, (fph >> 1) * 16384 + (fph & 1) * 1024 + wm * 8192 + m * 2048 + wn * 512); }
    STAMP(31);
  }
  pin(lane);
  {
    const int g = lane >> 4, r = lane & 15, kh = B.tn >> 3, cg = (B.tn & 7) * 2 + wn;
    u32x4 xr[4], pp[4]; f32x4 acc[4][4], gate[4], keep[2][4];
    UNR for (int n = 0; n < 4; n++) acc[2][n] = acc[3][n] = ldsr<f32x4>(lds0 + L_GB + 512 + wn * 256 + n * 64 + g * 16);
    pin(acc[2]); pin(acc[3]);
    const rsrc_t rp = mkrsrc(ws, xchoff(B.tm, B.tn ^ 8, w)); unsigned so = xchoff(B.tm, B.tn, w) + lane * 16; pin(so);
    compute_loop<3>(acc, lds0, wm, wn, lane, ts, [&](int j) {
      if (j == 0 || j == 2 || j == 7 || j == 9) { const int m = j == 2 || j == 9, np = j > 2; st_dev(pack8u(acc[m][2 * np], acc[m][2 * np + 1]), mkrsrc(ws, 0), so, (m * 2 + np) * 1024); }
      if (j >= 12) { xr[j - 12] = ldsr<u32x4>(lds0 + L_OT + w * 4096 + (j - 12) * 1024 + lane * 16); gate[j - 12] = ldsr<f32x4>(lds0 + L_GB + wn * 256 + (j - 12) * 64 + g * 16); } });
    STAMP(37);
    UNR for (int mm = 0; mm < 2; mm++) UNR for (int n = 0; n < 4; n++) keep[mm][n] = fma4(gate[n], acc[2 + mm][n], unp(xr[mm * 2 + (n >> 1)][(n & 1) * 2], xr[mm * 2 + (n >> 1)][(n & 1) * 2 + 1]));
    pin(keep[0]); pin(keep[1]);
    STAMP(40);
    waitvm<0>();
    STAMP(41);
    poll(pp, [&](int i) { return ld_dev(rp, lane * 16, i * 1024); });
    STAMP(38);
    const unsigned ot = lds0 + L_OT + w * 4096;
    UNR for (int mm = 0; mm < 2; mm++) UNR for (int n = 0; n < 4; n++) { const u32x4 t = pp[mm * 2 + (n >> 1)]; const f32x4 y = fma4(gate[n], unp(t[(n & 1) * 2], t[(n & 1) * 2 + 1]), keep[mm][n]);
      ldsw<u32x2>(ot + (mm * 16 + r) * 128 + (((n * 2 + (g >> 1)) ^ (r >> 1)) * 16) + (g & 1) * 8, u32x2{cvtpk(y[0], y[1]), cvtpk(y[2], y[3])}); }
    lwait();
    const int r8 = lane >> 3; const unsigned rv0 = r8 * 2048 + (((lane & 7) ^ (r8 >> 1)) * 16);
    const rsrc_t ro = outrsrc(A.out, ((B.tm * 128 + wm * 64 + kh * 32) * D_ + cg * 64) * 2);
    UNR for (int i = 0; i < 4; i++) st_dev(ldsr<u32x4>(ot + i * 1024 + lane * 16), ro, (i & 1) ? rv0 ^ 64 : rv0, i * 16384);
    STAMP(39);
  }
}

__global__ __launch_bounds__(512, 2) void k_block(Args A) {
  __shared__ __attribute__((aligned(1024))) char smem[L_TOT];
  const int w = __builtin_amdgcn_readfirstlane(threadIdx.x >> 6);
  const unsigned lds0 = (unsigned)(size_t)(LAS char*)smem;
  asm volatile(".Lcode_begin:");
  if (w == 3) touch_code(threadIdx.x & 63, lds0 + L_DUM);
  unsigned ts = 0; STAMP(0);
  const u64 tag = phase0(A, w, lds0, ts);
  if (w >= 4) loader_role(A, w - 4, lds0, tag, ts); else compute_role(A, w, lds0, tag, ts);
}

extern "C" void kernel_launch(void* const* d_in, const int* in_sizes, int n_in, void* d_out, int out_size, void* d_ws, size_t ws_size, hipStream_t stream) {
  Args A; A.x = (const u16*)d_in[0]; A.wqkv = (const u16*)d_in[1]; A.wphi = (const u16*)d_in[2]; A.wo = (const u16*)d_in[3]; A.w1 = (const u16*)d_in[5]; A.b1 = (const u16*)d_in[6];
  A.w2 = (const u16*)d_in[7]; A.b2 = (const u16*)d_in[8]; A.c = (const u16*)d_in[9]; A.wm = (const u16*)d_in[10]; A.out = (u16*)d_out; A.ws = (char*)d_ws;
  k_block<<<256, 512, 0, stream>>>(A);
}
```
